```python
import torch

def _softmax(x, axis=-1):
    return torch.softmax(x, dim=axis)
def _gelu(x):
    return torch.nn.functional.gelu(x, approximate="tanh")
def _silu(x):
    return torch.nn.functional.silu(x)
def _sigmoid(x):
    return torch.sigmoid(x)
def _layernorm(x, axis=-1, eps=1e-5):
    m = x.mean(dim=axis, keepdim=True)
    v = x.var(dim=axis, keepdim=True, unbiased=False)
    return (x - m) / torch.sqrt(v + eps)
def _scatter_add(tgt, idx, src, axis=0):
    return tgt.clone().index_add_(axis, idx, src)
def _segment_sum(src, seg, n):
    out = torch.zeros((n,) + src.shape[1:], dtype=src.dtype)
    return out.index_add_(0, seg, src)
def _pad(x, pw):
    flat = tuple(v for pair in reversed(pw) for v in pair)
    return torch.nn.functional.pad(x, flat)

kernel_name = 'recurrent_scan__sliding_block_cached__moe_arch_mod__prompt'

def setup_inputs():

    _w = (lambda a: (lambda t: t.to(torch.bfloat16) if t.is_floating_point() else t)(torch.as_tensor(a)))
    def _A_setup():
        B, Sq, D = 1, 2048, 448
        Hq, Hkv, Sk, Dh = 16, 8, 2048, 256
        assert Hq % Hkv == 0, (Hq, Hkv)
        return {
            "x": _w((lambda *s: torch.randn(*s, dtype=torch.bfloat16) * 0.05)(B, Sq, D)),
            "k_cache": _w((lambda *s: torch.randn(*s, dtype=torch.bfloat16) * 0.8)(B, Hkv, Sk, Dh)),
            "v_cache": _w((lambda *s: torch.randn(*s, dtype=torch.bfloat16) * 0.2)(B, Hkv, Sk, Dh)),
            "wq": _w((lambda *s: torch.randn(*s, dtype=torch.bfloat16) * 0.8)(D, Hq * Dh) * (D ** -0.5)),
            "wo": _w((lambda *s: torch.randn(*s, dtype=torch.bfloat16) * 0.2)(Hq * Dh, D) * ((Hq * Dh) ** -0.5)),
        }
    _A_in = _A_setup()

    def _B_setup():
        B, S, D, F, C = 1, 2048, 448, 1792, 2
        return {
            "x": _w((lambda *s: torch.randn(*s, dtype=torch.bfloat16) * 0.05)(B, S, D)),
            "w_r": _w((lambda *s: torch.randn(*s, dtype=torch.bfloat16) * 0.2)(D)),
            "w_gate": _w((lambda *s: torch.randn(*s, dtype=torch.bfloat16) * 0.1)(D, F)),
            "w_up": _w((lambda *s: torch.randn(*s, dtype=torch.bfloat16) * 0.1)(D, F)),
            "w_down": _w((lambda *s: torch.randn(*s, dtype=torch.bfloat16) * 0.1)(F, D)),
        }
    _B_in = _B_setup()

    return {
        "A_x": _A_in["x"],
        "A_k_cache": _A_in["k_cache"],
        "A_v_cache": _A_in["v_cache"],
        "A_wq": _A_in["wq"],
        "A_wo": _A_in["wo"],
        "B_x": _B_in["x"],
        "B_w_r": _B_in["w_r"],
        "B_w_gate": _B_in["w_gate"],
        "B_w_up": _B_in["w_up"],
        "B_w_down": _B_in["w_down"],
    }

def reference(A_x, A_k_cache, A_v_cache, A_wq, A_wo, B_x, B_w_r, B_w_gate, B_w_up, B_w_down):
    def _A_ref(x, k_cache, v_cache, wq, wo):
        B, Sq, D = x.shape
        Hq, Dh = 16, 256
        Hkv, Sk = k_cache.shape[1], k_cache.shape[2]
        W = min(4096, max(256, Sk // 4))
        rep = Hq // Hkv
        q = (lambda x, *d: x.permute(*d))((x @ wq).reshape(B, Sq, Hq, Dh), 0, 2, 1, 3)
        k = (lambda x, n, axis: torch.repeat_interleave(x, n, dim=axis))(k_cache[:, :, Sk - W:, :], rep, axis=1)
        v = (lambda x, n, axis: torch.repeat_interleave(x, n, dim=axis))(v_cache[:, :, Sk - W:, :], rep, axis=1)
        scores = torch.einsum("bhqd,bhkd->bhqk", q, k) * (Dh ** -0.5)
        p = _softmax(scores, axis=-1)
        o = torch.einsum("bhqk,bhkd->bhqd", p, v)
        return (lambda x, *d: x.permute(*d))(o, 0, 2, 1, 3).reshape(B, Sq, Hq * Dh) @ wo

    def _B_ref(x, w_r, w_gate, w_up, w_down):
        B, S, D = x.shape
        C = 2
        r = x @ w_r
        idx = torch.argsort(-r, axis=-1)[:, :C]
        idx_e = (lambda x, n, axis: torch.repeat_interleave(x, n, dim=axis))(idx.reshape(B, C, 1), D, axis=2)
        x_sel = (lambda x, i, axis: torch.take_along_dim(x, i, dim=axis))(x, idx_e, axis=1)
        g = _silu(x_sel @ w_gate)
        u = x_sel @ w_up
        y = (g * u) @ w_down
        r_sel = _sigmoid((lambda x, i, axis: torch.take_along_dim(x, i, dim=axis))(r, idx, axis=1))
        y = y * r_sel.reshape(B, C, 1)
        b_off = (torch.arange(B) * S).reshape(B, 1)
        flat_idx = (idx + b_off).reshape(B * C)
        zero = (x * 0.0).reshape(B * S, D)
        upd = _scatter_add(zero, flat_idx, y.reshape(B * C, D), axis=0)
        return x + upd.reshape(B, S, D)

    x = A_x
    xn = _layernorm(x, axis=-1)
    s = _A_ref(xn, A_k_cache, A_v_cache, A_wq, A_wo)
    g = _silu(_B_ref(xn, B_w_r, B_w_gate, B_w_up, B_w_down))
    return x + s * g
```

```cpp
#include <hip/hip_runtime.h>
#include <hip/hip_bf16.h>
#include <stdint.h>

typedef unsigned short u16;
__device__ __forceinline__ float bf2f(u16 v) { return __uint_as_float(((unsigned)v) << 16); }
__device__ __forceinline__ u16 f2bf(float f) { __hip_bfloat16 b = __float2bfloat16(f); return *(u16*)&b; }
__device__ __forceinline__ float rbf(float f) { return bf2f(f2bf(f)); }

constexpr int S = 2048, D = 448, HQ = 16, HKV = 8, SK = 2048, DH = 256, W = 512, F = 1792;

__global__ void k_ln(const u16* x, const u16* wr, u16* xn, float* r) {
  int row = blockIdx.x; int lane = threadIdx.x;
  float v[7]; float s = 0;
  for (int i = 0; i < 7; i++) { v[i] = bf2f(x[row * D + lane + 64 * i]); s += v[i]; }
  for (int o = 32; o; o >>= 1) s += __shfl_xor(s, o);
  float m = s / D; float q = 0;
  for (int i = 0; i < 7; i++) { float d = v[i] - m; q += d * d; }
  for (int o = 32; o; o >>= 1) q += __shfl_xor(q, o);
  float var = rbf(q / D); float mb = rbf(m);
  float den = rbf(sqrtf(rbf(var + 1e-5f)));
  float acc = 0;
  for (int i = 0; i < 7; i++) { float d = rbf(v[i] - mb); float y = rbf(d / den); xn[row * D + lane + 64 * i] = f2bf(y); acc += y * bf2f(wr[lane + 64 * i]); }
  for (int o = 32; o; o >>= 1) acc += __shfl_xor(acc, o);
  if (lane == 0) r[row] = rbf(acc);
}
__global__ void k_gemm(const u16* A, const u16* B, u16* C, int M, int N, int K) {
  __shared__ float As[16][17], Bs[16][17];
  int tx = threadIdx.x & 15, ty = threadIdx.x >> 4;
  int row = blockIdx.y * 16 + ty, col = blockIdx.x * 16 + tx; float acc = 0;
  for (int k0 = 0; k0 < K; k0 += 16) {
    As[ty][tx] = bf2f(A[(size_t)row * K + k0 + tx]); Bs[ty][tx] = bf2f(B[(size_t)(k0 + ty) * N + col]);
    __syncthreads();
    for (int k = 0; k < 16; k++) acc += As[ty][k] * Bs[k][tx];
    __syncthreads();
  }
  C[(size_t)row * N + col] = f2bf(acc);
}
__global__ void k_attn(const u16* q, const u16* kc, const u16* vc, u16* o) {
  __shared__ float qs[DH], p[W], red[8];
  int row = blockIdx.x, h = blockIdx.y, t = threadIdx.x, g = h / 2;
  qs[t] = bf2f(q[(size_t)row * 4096 + h * DH + t]); __syncthreads();
  const u16* kb = kc + ((size_t)g * SK + (SK - W)) * DH; const u16* vb = vc + ((size_t)g * SK + (SK - W)) * DH;
  float sc[2]; float mx = -1e30f;
  for (int j = 0; j < 2; j++) { int key = t + 256 * j; float a = 0; for (int d = 0; d < DH; d++) a += qs[d] * bf2f(kb[key * DH + d]); sc[j] = rbf(rbf(a) * 0.0625f); mx = fmaxf(mx, sc[j]); }
  for (int o2 = 32; o2; o2 >>= 1) mx = fmaxf(mx, __shfl_xor(mx, o2));
  if ((t & 63) == 0) red[t >> 6] = mx; __syncthreads();
  mx = fmaxf(fmaxf(red[0], red[1]), fmaxf(red[2], red[3]));
  float e[2], sm = 0; for (int j = 0; j < 2; j++) { e[j] = __expf(sc[j] - mx); sm += e[j]; }
  for (int o2 = 32; o2; o2 >>= 1) sm += __shfl_xor(sm, o2);
  if ((t & 63) == 0) red[4 + (t >> 6)] = sm; __syncthreads();
  sm = red[4] + red[5] + red[6] + red[7];
  for (int j = 0; j < 2; j++) p[t + 256 * j] = rbf(e[j] / sm);
  __syncthreads();
  float a = 0; for (int key = 0; key < W; key++) a += p[key] * bf2f(vb[key * DH + t]);
  o[(size_t)row * 4096 + h * DH + t] = f2bf(a);
}
__global__ void k_top2(const float* r, int* idx) {
  if (threadIdx.x == 0) { int i0 = -1, i1 = -1; float b0 = -INFINITY, b1 = -INFINITY;
    for (int i = 0; i < S; i++) { float v = r[i]; if (i0 < 0 || v > b0) { b1 = b0; i1 = i0; b0 = v; i0 = i; } else if (i1 < 0 || v > b1) { b1 = v; i1 = i; } }
    idx[0] = i0; idx[1] = i1; }
}
__global__ void k_gu(const u16* xn, const int* idx, const u16* wg, const u16* wu, float* h) {
  int f = blockIdx.x * 64 + threadIdx.x, c = blockIdx.y; int row = idx[c]; float g = 0, u = 0;
  for (int d = 0; d < D; d++) { float xv = bf2f(xn[row * D + d]); g += xv * bf2f(wg[d * F + f]); u += xv * bf2f(wu[d * F + f]); }
  g = rbf(g); u = rbf(u); g = rbf(g / (1.f + __expf(-g))); h[c * F + f] = rbf(g * u);
}
__global__ void k_down(const float* h, const int* idx, const float* r, const u16* wd, float* y) {
  int n = blockIdx.x * 64 + threadIdx.x, c = blockIdx.y; float a = 0;
  for (int f = 0; f < F; f++) a += h[c * F + f] * bf2f(wd[f * D + n]);
  float rs = rbf(1.f / (1.f + __expf(-r[idx[c]]))); y[c * D + n] = rbf(rbf(a) * rs);
}
__global__ void k_fin(const u16* x, const u16* xn, const u16* s, const int* idx, const float* y, u16* out) {
  int i = blockIdx.x * 256 + threadIdx.x; int row = i / D, n = i % D;
  float b = bf2f(xn[i]); if (row == idx[0]) b = rbf(b + y[n]); if (row == idx[1]) b = rbf(b + y[D + n]);
  float g = rbf(b / (1.f + __expf(-b)));
  out[i] = f2bf(bf2f(x[i]) + rbf(bf2f(s[i]) * g));
}
extern "C" void kernel_launch(void* const* d_in, const int* in_sizes, int n_in, void* d_out, int out_size, void* d_ws, size_t ws_size, hipStream_t stream) {
  const u16* x = (const u16*)d_in[0]; const u16* kc = (const u16*)d_in[1]; const u16* vc = (const u16*)d_in[2];
  const u16* wq = (const u16*)d_in[3]; const u16* wo = (const u16*)d_in[4]; const u16* wr = (const u16*)d_in[6];
  const u16* wg = (const u16*)d_in[7]; const u16* wu = (const u16*)d_in[8]; const u16* wd = (const u16*)d_in[9];
  char* ws = (char*)d_ws;
  u16* xn = (u16*)ws; u16* q = (u16*)(ws + (4 << 20)); u16* o = (u16*)(ws + (24 << 20)); u16* s = (u16*)(ws + (44 << 20));
  float* r = (float*)(ws + (48 << 20)); int* idx = (int*)(ws + (49 << 20)); float* h = (float*)(ws + (50 << 20)); float* y = (float*)(ws + (51 << 20));
  k_ln<<<S, 64, 0, stream>>>(x, wr, xn, r);
  k_gemm<<<dim3(4096 / 16, S / 16), 256, 0, stream>>>(xn, wq, q, S, 4096, D);
  k_attn<<<dim3(S, HQ), 256, 0, stream>>>(q, kc, vc, o);
  k_gemm<<<dim3(D / 16, S / 16), 256, 0, stream>>>(o, wo, s, S, D, 4096);
  k_top2<<<1, 64, 0, stream>>>(r, idx);
  k_gu<<<dim3(F / 64, 2), 64, 0, stream>>>(xn, idx, wg, wu, h);
  k_down<<<dim3(D / 64, 2), 64, 0, stream>>>(h, idx, r, wd, y);
  k_fin<<<S * D / 256, 256, 0, stream>>>(x, xn, s, idx, y, (u16*)d_out);
}
```

```cpp
#include <hip/hip_runtime.h>
#include <stdint.h>

constexpr size_t WS_PART = 0, WS_SYNC = (size_t)32 << 20, WS_DBG = (size_t)40 << 20;

__global__ __launch_bounds__(256, 1) __attribute__((aligned(4096))) void fused_block(const char* x, const char* kc, const char* vc, const char* wq, const char* wo, const char* wr,
                                                      const char* wg, const char* wu, const char* wd, char* ws, char* out) {
  const unsigned b = blockIdx.x, w = __builtin_amdgcn_readfirstlane(threadIdx.x >> 6);
  unsigned long long ka = (unsigned long long)__builtin_amdgcn_kernarg_segment_ptr();
  asm volatile(
"K1START_%=:\n"
"s_load_dwordx16 s[8:23], s[0:1], 0x0\n"
"s_load_dwordx4 s[24:27], s[0:1], 0x40\n"
"s_load_dwordx2 s[28:29], s[0:1], 0x50\n"
"v_mbcnt_lo_u32_b32 v0, -1, 0\n"
"v_mbcnt_hi_u32_b32 v0, -1, v0\n"
"s_getpc_b64 s[56:57]\n"
"K1PC_%=:\n"
"s_and_b32 s57, s57, 0xffff\n"
"s_mov_b32 s58, K1SLOW_%=-K1PC_%=\n"
"s_mov_b32 s59, 0x20000\n"
"s_lshr_b32 s97, s30, 3\n"
"s_lshl_b32 s97, s97, 2\n"
"s_add_u32 s97, s97, s31\n"
"s_lshl_b32 s97, s97, 10\n"
"s_and_b32 s97, s97, 65535\n"
"v_lshrrev_b32 v15, 3, v0\n"
"v_min_u32 v15, 1, v15\n"
"v_lshlrev_b32 v15, 30, v15\n"
"v_lshl_add_u32 v15, v0, 7, v15\n"
"buffer_load_dword a126, v15, s[56:59], s97 offen offset:0\n"
"s_and_b32 s97, s30, 7\n"
"s_lshl_b32 s97, s97, 1\n"
"s_bfe_u32 s98, s30, 0x10003\n"
"s_or_b32 s95, s97, s98\n"
"s_and_b32 s97, s30, 7\n"
"s_lshr_b32 s97, s97, 3\n"
"s_lshl_b32 s97, s97, 4\n"
"s_lshr_b32 s98, s30, 4\n"
"s_add_u32 s96, s97, s98\n"
"s_lshl_b32 s3, s96, 2\n"
"s_add_u32 s3, s3, s31\n"
"s_lshl_b32 s64, s31, 7\n"
"s_lshl_b32 s65, s31, 12\n"
"s_lshl_b32 s80, s96, 4\n"
"s_add_u32 s80, s80, s95\n"
"s_lshl_b32 s80, s80, 3\n"
"s_mul_i32 s68, s80, 896\n"
"s_add_u32 s68, s68, s64\n"
"s_cmp_lt_u32 s31, 3\n"
"s_cselect_b32 s81, 0, 1073741824\n"
"s_add_u32 s70, s68, s81\n"
"s_add_u32 s71, s64, s81\n"
"v_and_b32 v160, 7, v0\n"
"v_lshrrev_b32 v7, 3, v0\n"
"v_lshlrev_b32 v19, 4, v7\n"
"v_mul_u32_u24 v18, 896, v160\n"
"v_add_u32 v18, v18, v19\n"
"v_bfe_u32 v1, v0, 2, 3\n"
"v_lshrrev_b32 v2, 5, v0\n"
"v_and_b32 v3, 3, v0\n"
"v_bfe_u32 v4, v0, 4, 1\n"
"v_xor_b32 v7, v3, v4\n"
"v_lshlrev_b32 v7, 4, v7\n"
"v_lshl_add_u32 v5, v2, 6, v7\n"
"v_lshlrev_b32 v7, 1, v4\n"
"v_xor_b32 v7, v3, v7\n"
"v_lshlrev_b32 v7, 4, v7\n"
"v_lshl_add_u32 v6, v2, 6, v7\n"
"v_bfe_u32 v7, v0, 2, 2\n"
"v_lshl_add_u32 v8, v4, 4, v7\n"
"v_lshl_add_u32 v234, v8, 13, v6\n"
"v_mul_u32_u24 v7, 3584, v8\n"
"v_add_u32 v176, v7, v6\n"
"v_mul_u32_u24 v8, 896, v8\n"
"v_add_u32 v196, v8, v6\n"
"v_add_u32 v197, 6144, v196\n"
"v_add_u32 v198, 1536, v196\n"
"v_add_u32 v199, 7680, v196\n"
"v_lshlrev_b32 v224, 4, v0\n"
"v_bfe_u32 v7, v0, 2, 1\n"
"v_bfe_u32 v8, v0, 3, 2\n"
"v_lshlrev_b32 v8, 12, v8\n"
"v_lshl_add_u32 v7, v7, 9, v8\n"
"v_add_u32 v238, v7, v5\n"
"v_lshrrev_b32 v7, 4, v0\n"
"v_min_u32 v7, 1, v7\n"
"v_lshlrev_b32 v7, 30, v7\n"
"v_lshl_add_u32 v195, v0, 7, v7\n"
"v_add_u32 v7, 4, v0\n"
"v_lshrrev_b32 v7, 5, v7\n"
"v_min_u32 v7, 1, v7\n"
"v_lshlrev_b32 v7, 30, v7\n"
"v_lshl_add_u32 v212, v0, 7, v7\n"
"v_lshrrev_b32 v8, 2, v0\n"
"v_lshl_add_u32 v7, v8, 13, v7\n"
"v_lshl_add_u32 v213, v3, 7, v7\n"
"s_lshr_b32 s97, s31, 1\n"
"s_mul_i32 s66, s97, 28672\n"
"s_and_b32 s97, s96, 3\n"
"s_lshl_b32 s97, s97, 8\n"
"s_add_u32 s66, s66, s97\n"
"s_and_b32 s97, s64, 128\n"
"s_add_u32 s66, s66, s97\n"
"s_and_b32 s97, s3, 13\n"
"s_cmp_eq_u32 s97, 13\n"
"s_cselect_b32 s97, 1073741824, 0\n"
"s_add_u32 s66, s66, s97\n"
"v_lshlrev_b32 v162, 5, v160\n"
"s_lshl_b32 s97, s31, 3\n"
"v_add_u32 v161, s97, v162\n"
"v_bfe_u32 v7, v0, 3, 1\n"
"v_lshl_add_u32 v7, v7, 1, v4\n"
"v_lshlrev_b32 v7, 8, v7\n"
"v_lshl_add_u32 v7, v2, 11, v7\n"
"v_lshl_add_u32 v163, v160, 4, v7\n"
"s_lshl_b32 s97, s96, 2\n"
"s_lshr_b32 s98, s95, 2\n"
"s_add_u32 s69, s97, s98\n"
"s_mul_i32 s93, s69, 28672\n"
"s_bfe_u32 s97, s95, 0x10001\n"
"s_lshl_b32 s97, s97, 10\n"
"s_add_u32 s93, s93, s97\n"
"s_and_b32 s97, s95, 1\n"
"s_lshl_b32 s97, s97, 7\n"
"s_add_u32 s93, s93, s97\n"
"s_add_u32 s93, s93, s65\n"
"s_add_u32 s93, s93, 2097152\n"
"v_min_u32 v7, 1, v0\n"
"v_lshlrev_b32 v241, 30, v7\n"
"s_mul_i32 s94, s69, 256\n"
"s_and_b32 s97, s95, 3\n"
"s_lshl_b32 s97, s97, 2\n"
"s_add_u32 s97, s97, s31\n"
"s_lshl_b32 s97, s97, 3\n"
"s_add_u32 s94, s94, s97\n"
"s_add_u32 s94, s94, 4194304\n"
"s_add_u32 s92, s93, 16384\n"
"s_add_u32 s92, s92, s81\n"
"s_mov_b32 s99, 0x3f803f80\n"
"s_lshr_b32 s97, s95, 1\n"
"s_lshl_b32 s97, s97, 20\n"
"s_add_u32 s97, s97, 786432\n"
"s_mul_i32 s98, s95, 229376\n"
"s_waitcnt lgkmcnt(0)\n"
"s_mov_b32 s32, s8\n"
"s_and_b32 s33, s9, 0xffff\n"
"s_mov_b32 s34, 1835008\n"
"s_mov_b32 s35, 0x20000\n"
"buffer_load_dwordx4 v[152:155], v18, s[32:35], s68 offen offset:0\n"
"buffer_load_dwordx4 v[156:159], v18, s[32:35], s70 offen offset:512\n"
"s_add_u32 s84, s26, 33554432\n"
"s_addc_u32 s85, s27, 0\n"
"s_load_dwordx2 s[86:87], s[84:85], 0x0\n"
"s_add_u32 s48, s26, 33554432\n"
"s_addc_u32 s49, s27, 0\n"
"s_and_b32 s49, s49, 0xffff\n"
"s_mov_b32 s50, 8388608\n"
"s_mov_b32 s51, 0x20000\n"
"s_add_u32 s44, s12, s97\n"
"s_addc_u32 s45, s13, 0\n"
"s_and_b32 s45, s45, 0xffff\n"
"s_mov_b32 s46, 262144\n"
"s_mov_b32 s47, 0x20000\n"
"s_add_u32 s60, s16, s98\n"
"s_addc_u32 s61, s17, 0\n"
"s_and_b32 s61, s61, 0xffff\n"
"s_mov_b32 s62, 229376\n"
"s_mov_b32 s63, 0x20000\n"
"s_add_u32 s40, s10, s97\n"
"s_addc_u32 s41, s11, 0\n"
"s_and_b32 s41, s41, 0xffff\n"
"s_mov_b32 s42, 262144\n"
"s_mov_b32 s43, 0x20000\n"
"s_and_b32 s12, s96, 12\n"
"s_add_u32 s12, s12, s31\n"
"s_lshl_b32 s12, s12, 14\n"
"s_lshl_b32 s97, s95, 9\n"
"s_sub_u32 s98, 3670016, s97\n"
"s_add_u32 s36, s14, s97\n"
"s_addc_u32 s37, s15, 0\n"
"s_and_b32 s37, s37, 0xffff\n"
"s_mov_b32 s38, s98\n"
"s_mov_b32 s39, 0x20000\n"
"s_lshr_b32 s10, s31, 1\n"
"s_lshl_b32 s10, s10, 18\n"
"s_and_b32 s97, s64, 128\n"
"s_add_u32 s10, s10, s97\n"
"s_add_u32 m0, s65, 16384\n"
"v_add_u32 v235, 64512, v234\n"
"v_add_u32 v236, 30720, v234\n"
"v_add_u32 v237, 95232, v234\n"
"s_add_u32 s67, s12, 0\n"
"v_lshrrev_b32 v16, 4, v0\n"
"v_and_b32 v7, 1, v16\n"
"v_lshl_add_u32 v17, v7, 1, v2\n"
"v_bfe_u32 v7, v0, 2, 1\n"
"buffer_load_dwordx4 v238, s[44:47], s67 offen offset:0 sc1 lds\n"
"v_xor_b32 v7, v17, v7\n"
"v_lshlrev_b32 v7, 4, v7\n"
"v_and_b32 v8, 7, v0\n"
"v_lshl_add_u32 v7, v8, 6, v7\n"
"buffer_load_dwordx4 v238, s[44:47], s67 offen offset:1024 sc1 lds\n"
"v_bfe_u32 v8, v0, 3, 1\n"
"v_lshl_add_u32 v225, v8, 10, v7\n"
"v_lshlrev_b32 v7, 3, v3\n"
"v_lshl_add_u32 v7, v1, 6, v7\n"
"buffer_load_dwordx4 v238, s[44:47], s67 offen offset:2048 sc1 lds\n"
"v_lshl_add_u32 v7, v2, 10, v7\n"
"v_lshl_add_u32 v228, v4, 5, v7\n"
"v_xor_b32 v231, 32, v228\n"
"v_add_u32 v226, 65536, v225\n"
"buffer_load_dwordx4 v238, s[44:47], s67 offen offset:3072 sc1 lds\n"
"v_add_u32 v229, 65536, v228\n"
"v_add_u32 v232, 65536, v231\n"
"v_add_u32 v227, 131072, v225\n"
"s_add_u32 m0, s65, 32768\n"
"v_add_u32 v230, 131072, v228\n"
"v_add_u32 v233, 131072, v231\n"
"v_add_u32 v192, s65, v225\n"
"v_add_u32 v193, s65, v226\n"
"s_add_u32 s67, s66, 0\n"
"v_add_u32 v194, s65, v227\n"
"v_add_u32 v177, 27648, v176\n"
"v_add_u32 v178, 12288, v176\n"
"v_add_u32 v179, 39936, v176\n"
"buffer_load_dwordx4 v196, s[60:63], s67 offen offset:0 sc1 lds\n"
"v_mov_b32 v252, 0x3f803f80\n"
"v_accvgpr_write_b32 a120, v252\n"
"v_accvgpr_write_b32 a121, v252\n"
"v_accvgpr_write_b32 a122, v252\n"
"buffer_load_dwordx4 v197, s[60:63], s67 offen offset:1024 sc1 lds\n"
"v_accvgpr_write_b32 a123, v252\n"
"v_add_u32 v239, 65536, v224\n"
"v_add_u32 v240, 131072, v224\n"
"v_and_b32 v252, 16, v224\n"
"buffer_load_dwordx4 v198, s[60:63], s67 offen offset:2048 sc1 lds\n"
"v_mov_b32 v253, 0x3f80\n"
"v_lshlrev_b32 v253, v252, v253\n"
"v_bfe_u32 v252, v224, 5, 3\n"
"v_cmp_eq_u32_e64 s[82:83], 0, v252\n"
"buffer_load_dwordx4 v199, s[60:63], s67 offen offset:3072 sc1 lds\n"
"s_nop 0\n"
"v_cndmask_b32_e64 v254, 0, v253, s[82:83]\n"
"v_accvgpr_write_b32 a48, v254\n"
"v_cmp_eq_u32_e64 s[82:83], 1, v252\n"
"s_mov_b32 s56, s18\n"
"s_and_b32 s57, s19, 0xffff\n"
"v_cndmask_b32_e64 v254, 0, v253, s[82:83]\n"
"v_accvgpr_write_b32 a49, v254\n"
"v_cmp_eq_u32_e64 s[82:83], 2, v252\n"
"s_nop 1\n"
"v_cndmask_b32_e64 v254, 0, v253, s[82:83]\n"
"s_mov_b32 s58, 896\n"
"v_accvgpr_write_b32 a50, v254\n"
"v_cmp_eq_u32_e64 s[82:83], 3, v252\n"
"s_nop 1\n"
"v_cndmask_b32_e64 v254, 0, v253, s[82:83]\n"
"v_accvgpr_write_b32 a51, v254\n"
"s_mov_b32 s59, 0x20000\n"
"v_cmp_eq_u32_e64 s[82:83], 4, v252\n"
"s_nop 1\n"
"v_cndmask_b32_e64 v254, 0, v253, s[82:83]\n"
"v_accvgpr_write_b32 a52, v254\n"
"v_cmp_eq_u32_e64 s[82:83], 5, v252\n"
"buffer_load_dwordx4 v[144:147], v19, s[56:59], s64 offen offset:0\n"
"s_nop 0\n"
"v_cndmask_b32_e64 v254, 0, v253, s[82:83]\n"
"v_accvgpr_write_b32 a53, v254\n"
"v_cmp_eq_u32_e64 s[82:83], 6, v252\n"
"s_nop 1\n"
"v_cndmask_b32_e64 v254, 0, v253, s[82:83]\n"
"buffer_load_dwordx4 v[148:151], v19, s[56:59], s71 offen offset:512\n"
"v_accvgpr_write_b32 a54, v254\n"
"v_cmp_eq_u32_e64 s[82:83], 7, v252\n"
"s_nop 1\n"
"v_cndmask_b32_e64 v254, 0, v253, s[82:83]\n"
"v_accvgpr_write_b32 a55, v254\n"
"s_lshl_b32 s97, s3, 1\n"
"s_and_b32 s88, s95, 1\n"
"s_add_u32 s97, s97, s88\n"
"s_lshl_b32 s97, s97, 11\n"
"buffer_load_dword a126, v195, s[44:47], s97 offen offset:0\n"
"s_mul_i32 s97, s3, 3584\n"
"buffer_load_dword a126, v212, s[60:63], s97 offen offset:0\n"
"s_waitcnt vmcnt(13)\n"
"v_dot2_f32_bf16 v7, v152, s99, 0\n"
"v_dot2_f32_bf16 v8, v152, v152, 0\n"
"v_dot2_f32_bf16 v164, v153, s99, 0\n"
"v_dot2_f32_bf16 v165, v153, v153, 0\n"
"v_dot2_f32_bf16 v7, v154, s99, v7\n"
"v_dot2_f32_bf16 v8, v154, v154, v8\n"
"v_dot2_f32_bf16 v164, v155, s99, v164\n"
"v_dot2_f32_bf16 v165, v155, v155, v165\n"
"s_waitcnt vmcnt(12)\n"
"v_dot2_f32_bf16 v7, v156, s99, v7\n"
"v_dot2_f32_bf16 v8, v156, v156, v8\n"
"v_dot2_f32_bf16 v164, v157, s99, v164\n"
"v_dot2_f32_bf16 v165, v157, v157, v165\n"
"v_dot2_f32_bf16 v7, v158, s99, v7\n"
"v_dot2_f32_bf16 v8, v158, v158, v8\n"
"v_dot2_f32_bf16 v164, v159, s99, v164\n"
"v_dot2_f32_bf16 v165, v159, v159, v165\n"
"s_nop 1\n"
"v_add_f32 v164, v164, v7\n"
"v_add_f32 v165, v165, v8\n"
"s_nop 0\n"
"v_add_f32_dpp v164, v164, v164 row_ror:8 row_mask:0xf bank_mask:0xf\n"
"v_add_f32_dpp v165, v165, v165 row_ror:8 row_mask:0xf bank_mask:0xf\n"
"v_mov_b32 v7, v164\n"
"v_mov_b32 v8, v165\n"
"s_nop 0\n"
"v_permlane16_swap_b32 v164, v7\n"
"v_permlane16_swap_b32 v165, v8\n"
"v_add_f32 v164, v164, v7\n"
"v_add_f32 v165, v165, v8\n"
"v_mov_b32 v7, v164\n"
"v_mov_b32 v8, v165\n"
"s_nop 0\n"
"v_permlane32_swap_b32 v164, v7\n"
"v_permlane32_swap_b32 v165, v8\n"
"v_add_f32 v164, v164, v7\n"
"v_add_f32 v165, v165, v8\n"
"ds_write_b64 v161, v[164:165] offset:0\n"
"v_lshlrev_b32 v128, 16, v152\n"
"v_and_b32 v129, 0xffff0000, v152\n"
"v_lshlrev_b32 v130, 16, v153\n"
"v_and_b32 v131, 0xffff0000, v153\n"
"v_lshlrev_b32 v132, 16, v154\n"
"v_and_b32 v133, 0xffff0000, v154\n"
"v_lshlrev_b32 v134, 16, v155\n"
"v_and_b32 v135, 0xffff0000, v155\n"
"s_waitcnt lgkmcnt(0)\n"
"s_waitcnt lgkmcnt(0)\n"
"s_barrier\n"
"ds_read_b128 v[168:171], v162 offset:0\n"
"ds_read_b128 v[172:175], v162 offset:16\n"
"v_lshlrev_b32 v136, 16, v156\n"
"v_and_b32 v137, 0xffff0000, v156\n"
"v_lshlrev_b32 v138, 16, v157\n"
"v_and_b32 v139, 0xffff0000, v157\n"
"v_lshlrev_b32 v140, 16, v158\n"
"v_and_b32 v141, 0xffff0000, v158\n"
"v_lshlrev_b32 v142, 16, v159\n"
"v_and_b32 v143, 0xffff0000, v159\n"
"s_add_u32 s86, s86, 0x7f4a7c15\n"
"s_addc_u32 s87, s87, 0x9e3779b9\n"
"s_mov_b64 s[6:7], s[86:87]\n"
"s_and_b32 s86, s86, 0xbfffbfff\n"
"s_and_b32 s87, s87, 0xbfffbfff\n"
"s_waitcnt lgkmcnt(0)\n"
"v_add_f32 v168, v168, v172\n"
"v_add_f32 v169, v169, v173\n"
"v_add_f32 v170, v170, v174\n"
"v_add_f32 v171, v171, v175\n"
"v_add_f32 v168, v168, v170\n"
"v_add_f32 v169, v169, v171\n"
"v_mul_f32 v168, 0x3b124925, v168\n"
"v_mul_f32 v169, 0x3b124925, v169\n"
"v_cvt_pk_bf16_f32 v166, v168, v168\n"
"v_fma_f32 v169, -v168, v168, v169\n"
"v_lshlrev_b32 v166, 16, v166\n"
"v_max_f32 v169, 0, v169\n"
"v_cvt_pk_bf16_f32 v169, v169, v169\n"
"v_lshlrev_b32 v169, 16, v169\n"
"v_add_f32 v169, 0x3727c5ac, v169\n"
"v_cvt_pk_bf16_f32 v169, v169, v169\n"
"v_lshlrev_b32 v169, 16, v169\n"
"v_sqrt_f32 v169, v169\n"
"s_nop 0\n"
"v_cvt_pk_bf16_f32 v169, v169, v169\n"
"v_lshlrev_b32 v169, 16, v169\n"
"v_rcp_f32 v167, v169\n"
"s_nop 0\n"
"v_mul_f32 v170, 0x3db8aa3b, v167\n"
"v_mul_f32_e64 v171, -v166, v170\n"
"v_fma_f32 v128, v128, v170, v171\n"
"v_fma_f32 v129, v129, v170, v171\n"
"v_fma_f32 v130, v130, v170, v171\n"
"v_fma_f32 v131, v131, v170, v171\n"
"v_fma_f32 v132, v132, v170, v171\n"
"v_fma_f32 v133, v133, v170, v171\n"
"v_fma_f32 v134, v134, v170, v171\n"
"v_fma_f32 v135, v135, v170, v171\n"
"v_cvt_pk_bf16_f32 v152, v128, v129\n"
"v_cvt_pk_bf16_f32 v153, v130, v131\n"
"v_cvt_pk_bf16_f32 v154, v132, v133\n"
"v_cvt_pk_bf16_f32 v155, v134, v135\n"
"buffer_store_dwordx4 v[152:155], v163, s[48:51], s93 offen offset:0 sc1\n"
"v_fma_f32 v136, v136, v170, v171\n"
"v_fma_f32 v137, v137, v170, v171\n"
"v_fma_f32 v138, v138, v170, v171\n"
"v_fma_f32 v139, v139, v170, v171\n"
"v_fma_f32 v140, v140, v170, v171\n"
"v_fma_f32 v141, v141, v170, v171\n"
"v_fma_f32 v142, v142, v170, v171\n"
"v_fma_f32 v143, v143, v170, v171\n"
"v_cvt_pk_bf16_f32 v156, v136, v137\n"
"v_cvt_pk_bf16_f32 v157, v138, v139\n"
"v_cvt_pk_bf16_f32 v158, v140, v141\n"
"v_cvt_pk_bf16_f32 v159, v142, v143\n"
"buffer_store_dwordx4 v[156:159], v163, s[48:51], s92 offen offset:0 sc1\n"
"v_lshlrev_b32 v172, 3, v160\n"
"s_lshl_b32 s68, s80, 3\n"
"s_add_u32 s68, s68, 32768\n"
"s_xor_b32 s97, s81, 1073741824\n"
"s_add_u32 s68, s68, s97\n"
"buffer_store_dwordx2 v[166:167], v172, s[48:51], s68 offen offset:0 sc1\n"
"s_add_u32 m0, s65, 49152\n"
"s_add_u32 s67, s12, 128\n"
"buffer_load_dwordx4 v238, s[44:47], s67 offen offset:0 sc1 lds\n"
"buffer_load_dwordx4 v238, s[44:47], s67 offen offset:1024 sc1 lds\n"
"buffer_load_dwordx4 v238, s[44:47], s67 offen offset:2048 sc1 lds\n"
"buffer_load_dwordx4 v238, s[44:47], s67 offen offset:3072 sc1 lds\n"
"s_add_u32 m0, s65, 65536\n"
"s_add_u32 s67, s66, 57344\n"
"buffer_load_dwordx4 v196, s[60:63], s67 offen offset:0 sc1 lds\n"
"buffer_load_dwordx4 v197, s[60:63], s67 offen offset:1024 sc1 lds\n"
"buffer_load_dwordx4 v198, s[60:63], s67 offen offset:2048 sc1 lds\n"
"buffer_load_dwordx4 v199, s[60:63], s67 offen offset:3072 sc1 lds\n"
"s_mov_b32 s68, s94\n"
"v_mov_b32 v250, s86\n"
"v_mov_b32 v251, s87\n"
"s_waitcnt vmcnt(15)\n"
"s_barrier\n"
"ds_read_b128 a[80:83], v192 offset:16384\n"
"ds_read_b128 a[84:87], v192 offset:18432\n"
"ds_read_b128 a[88:91], v192 offset:16896\n"
"ds_read_b128 a[92:95], v192 offset:18944\n"
"ds_read_b64_tr_b16 a[0:1], v228 offset:32768\n"
"ds_read_b64_tr_b16 a[2:3], v228 offset:34816\n"
"ds_read_b64_tr_b16 a[4:5], v228 offset:40960\n"
"ds_read_b64_tr_b16 a[6:7], v228 offset:43008\n"
"ds_read_b64_tr_b16 a[8:9], v231 offset:32768\n"
"ds_read_b64_tr_b16 a[10:11], v231 offset:34816\n"
"ds_read_b64_tr_b16 a[12:13], v231 offset:40960\n"
"ds_read_b64_tr_b16 a[14:15], v231 offset:43008\n"
"s_waitcnt lgkmcnt(6)\n"
"v_mfma_f32_16x16x32_bf16 a[192:195], a[80:83], a[0:3], 0\n"
"s_add_u32 m0, s65, 81920\n"
"s_add_u32 s67, s12, 256\n"
"ds_read_b64_tr_b16 a[16:17], v228 offset:33280\n"
"ds_read_b64_tr_b16 a[18:19], v228 offset:35328\n"
"ds_read_b64_tr_b16 a[20:21], v228 offset:41472\n"
"ds_read_b64_tr_b16 a[22:23], v228 offset:43520\n"
"s_waitcnt lgkmcnt(8)\n"
"v_mfma_f32_16x16x32_bf16 a[192:195], a[88:91], a[4:7], a[192:195]\n"
"buffer_load_dwordx4 v238, s[44:47], s67 offen offset:0 sc1 lds\n"
"ds_read_b64_tr_b16 a[24:25], v231 offset:33280\n"
"v_mfma_f32_16x16x32_bf16 a[196:199], a[92:95], a[4:7], 0\n"
"ds_read_b64_tr_b16 a[26:27], v231 offset:35328\n"
"ds_read_b64_tr_b16 a[28:29], v231 offset:41472\n"
"v_mfma_f32_16x16x32_bf16 a[196:199], a[84:87], a[0:3], a[196:199]\n"
"s_waitcnt lgkmcnt(7)\n"
"ds_read_b64_tr_b16 a[30:31], v231 offset:43520\n"
"ds_read_b64_tr_b16 a[32:33], v228 offset:36864\n"
"v_mfma_f32_16x16x32_bf16 a[200:203], a[80:83], a[8:11], 0\n"
"ds_read_b64_tr_b16 a[34:35], v228 offset:38912\n"
"ds_read_b64_tr_b16 a[36:37], v228 offset:45056\n"
"v_mfma_f32_16x16x32_bf16 a[200:203], a[88:91], a[12:15], a[200:203]\n"
"buffer_load_dwordx4 v238, s[44:47], s67 offen offset:1024 sc1 lds\n"
"v_mfma_f32_16x16x32_bf16 a[204:207], a[92:95], a[12:15], 0\n"
"ds_read_b64_tr_b16 a[38:39], v228 offset:47104\n"
"v_bfe_u32 v249, v224, 4, 4\n"
"v_mfma_f32_16x16x32_bf16 a[204:207], a[84:87], a[8:11], a[204:207]\n"
"s_waitcnt lgkmcnt(8)\n"
"ds_read_b64_tr_b16 a[40:41], v231 offset:36864\n"
"v_lshlrev_b32 v249, 3, v249\n"
"v_mfma_f32_16x16x32_bf16 a[208:211], a[80:83], a[16:19], 0\n"
"s_mul_i32 s70, s3, 256\n"
"s_add_u32 s70, s70, 4194304\n"
"s_mul_i32 s92, s3, 28672\n"
"v_mfma_f32_16x16x32_bf16 a[208:211], a[88:91], a[20:23], a[208:211]\n"
"s_add_u32 s92, s92, 2097152\n"
"buffer_load_dwordx4 v238, s[44:47], s67 offen offset:2048 sc1 lds\n"
"v_mfma_f32_16x16x32_bf16 a[212:215], a[92:95], a[20:23], 0\n"
"s_waitcnt vmcnt(11)\n"
"ds_read_b64_tr_b16 a[42:43], v231 offset:38912\n"
"ds_read_b64_tr_b16 a[44:45], v231 offset:45056\n"
"v_mfma_f32_16x16x32_bf16 a[212:215], a[84:87], a[16:19], a[212:215]\n"
"s_waitcnt lgkmcnt(7)\n"
"ds_read_b64_tr_b16 a[46:47], v231 offset:47104\n"
"ds_read_b64_tr_b16 a[0:1], v228 offset:37376\n"
"v_mfma_f32_16x16x32_bf16 a[216:219], a[80:83], a[24:27], 0\n"
"buffer_load_dwordx4 v238, s[44:47], s67 offen offset:3072 sc1 lds\n"
"s_add_u32 m0, s65, 98304\n"
"s_add_u32 s67, s66, 114688\n"
"buffer_store_dwordx2 v[250:251], v241, s[48:51], s68 offen offset:0 sc1\n"
"v_mfma_f32_16x16x32_bf16 a[216:219], a[88:91], a[28:31], a[216:219]\n"
"ds_read_b64_tr_b16 a[2:3], v228 offset:39424\n"
"ds_read_b64_tr_b16 a[4:5], v228 offset:45568\n"
"v_mfma_f32_16x16x32_bf16 a[220:223], a[92:95], a[28:31], 0\n"
"ds_read_b64_tr_b16 a[6:7], v228 offset:47616\n"
"ds_read_b64_tr_b16 a[8:9], v231 offset:37376\n"
"v_mfma_f32_16x16x32_bf16 a[220:223], a[84:87], a[24:27], a[220:223]\n"
"s_waitcnt lgkmcnt(9)\n"
"buffer_load_dwordx4 v196, s[60:63], s67 offen offset:0 sc1 lds\n"
"v_mfma_f32_16x16x32_bf16 a[224:227], a[80:83], a[32:35], 0\n"
"s_mov_b64 exec, 0\n"
"s_cmp_eq_u32 s31, 0\n"
"s_cbranch_scc1 IWB0_%=\n"
"s_cmp_eq_u32 s31, 1\n"
"s_cbranch_scc1 IWB1_%=\n"
"s_cmp_eq_u32 s31, 2\n"
"s_cbranch_scc1 IWB2_%=\n"
"s_cmp_eq_u32 s31, 3\n"
"s_cbranch_scc1 IWB3_%=\n"
"IRB_%=:\n"
"s_mov_b64 exec, -1\n"
"s_nop 3\n"
"s_waitcnt vmcnt(6)\n"
"s_barrier\n"
"v_mfma_f32_16x16x32_bf16 a[224:227], a[88:91], a[36:39], a[224:227]\n"
"ds_read_b64_tr_b16 a[10:11], v231 offset:39424\n"
"ds_read_b64_tr_b16 a[12:13], v231 offset:45568\n"
"v_mfma_f32_16x16x32_bf16 a[228:231], a[92:95], a[36:39], 0\n"
"buffer_load_dwordx4 v197, s[60:63], s67 offen offset:1024 sc1 lds\n"
"v_mfma_f32_16x16x32_bf16 a[228:231], a[84:87], a[32:35], a[228:231]\n"
"s_waitcnt lgkmcnt(7)\n"
"ds_read_b64_tr_b16 a[14:15], v231 offset:47616\n"
"ds_read_b64_tr_b16 a[16:17], v229 offset:0\n"
"v_mfma_f32_16x16x32_bf16 a[232:235], a[80:83], a[40:43], 0\n"
"ds_read_b128 a[96:99], v192 offset:49152\n"
"ds_read_b64_tr_b16 a[18:19], v229 offset:2048\n"
"v_mfma_f32_16x16x32_bf16 a[232:235], a[88:91], a[44:47], a[232:235]\n"
"buffer_load_dwordx4 v198, s[60:63], s67 offen offset:2048 sc1 lds\n"
"v_mfma_f32_16x16x32_bf16 a[236:239], a[92:95], a[44:47], 0\n"
"ds_read_b128 a[100:103], v192 offset:51200\n"
"ds_read_b64_tr_b16 a[20:21], v229 offset:8192\n"
"v_mfma_f32_16x16x32_bf16 a[236:239], a[84:87], a[40:43], a[236:239]\n"
"s_waitcnt lgkmcnt(9)\n"
"ds_read_b128 a[104:107], v192 offset:49664\n"
"ds_read_b64_tr_b16 a[22:23], v229 offset:10240\n"
"v_mfma_f32_16x16x32_bf16 a[240:243], a[80:83], a[0:3], 0\n"
"buffer_load_dwordx4 v199, s[60:63], s67 offen offset:3072 sc1 lds\n"
"s_add_u32 m0, s65, 114688\n"
"s_add_u32 s67, s12, 384\n"
"ds_read_b128 a[108:111], v192 offset:51712\n"
"v_mfma_f32_16x16x32_bf16 a[240:243], a[88:91], a[4:7], a[240:243]\n"
"ds_read_b64_tr_b16 a[24:25], v232 offset:0\n"
"ds_read_b64_tr_b16 a[26:27], v232 offset:2048\n"
"v_mfma_f32_16x16x32_bf16 a[244:247], a[92:95], a[4:7], 0\n"
"ds_read_b64_tr_b16 a[28:29], v232 offset:8192\n"
"ds_read_b64_tr_b16 a[30:31], v232 offset:10240\n"
"v_mfma_f32_16x16x32_bf16 a[244:247], a[84:87], a[0:3], a[244:247]\n"
"s_waitcnt lgkmcnt(12)\n"
"buffer_load_dwordx4 v238, s[44:47], s67 offen offset:0 sc1 lds\n"
"v_mfma_f32_16x16x32_bf16 a[248:251], a[80:83], a[8:11], 0\n"
"ds_read_b64_tr_b16 a[32:33], v229 offset:512\n"
"ds_read_b64_tr_b16 a[34:35], v229 offset:2560\n"
"v_mfma_f32_16x16x32_bf16 a[248:251], a[88:91], a[12:15], a[248:251]\n"
"ds_read_b64_tr_b16 a[36:37], v229 offset:8704\n"
"ds_read_b64_tr_b16 a[38:39], v229 offset:10752\n"
"v_mfma_f32_16x16x32_bf16 a[252:255], a[92:95], a[12:15], 0\n"
"buffer_load_dwordx4 v238, s[44:47], s67 offen offset:1024 sc1 lds\n"
"v_mfma_f32_16x16x32_bf16 a[252:255], a[84:87], a[8:11], a[252:255]\n"
"s_waitcnt lgkmcnt(9)\n"
"s_waitcnt lgkmcnt(8)\n"
"v_mfma_f32_16x16x32_bf16 a[192:195], a[96:99], a[16:19], a[192:195]\n"
"ds_read_b64_tr_b16 a[40:41], v232 offset:512\n"
"ds_read_b64_tr_b16 a[42:43], v232 offset:2560\n"
"v_mfma_f32_16x16x32_bf16 a[192:195], a[104:107], a[20:23], a[192:195]\n"
"buffer_load_dwordx4 v238, s[44:47], s67 offen offset:2048 sc1 lds\n"
"v_mfma_f32_16x16x32_bf16 a[196:199], a[108:111], a[20:23], a[196:199]\n"
"ds_read_b64_tr_b16 a[44:45], v232 offset:8704\n"
"ds_read_b64_tr_b16 a[46:47], v232 offset:10752\n"
"v_mfma_f32_16x16x32_bf16 a[196:199], a[100:103], a[16:19], a[196:199]\n"
"s_waitcnt lgkmcnt(8)\n"
"ds_read_b64_tr_b16 a[0:1], v229 offset:4096\n"
"ds_read_b64_tr_b16 a[2:3], v229 offset:6144\n"
"v_mfma_f32_16x16x32_bf16 a[200:203], a[96:99], a[24:27], a[200:203]\n"
"buffer_load_dwordx4 v238, s[44:47], s67 offen offset:3072 sc1 lds\n"
"s_add_u32 m0, s65, 131072\n"
"s_add_u32 s67, s66, 172032\n"
"v_mfma_f32_16x16x32_bf16 a[200:203], a[104:107], a[28:31], a[200:203]\n"
"ds_read_b64_tr_b16 a[4:5], v229 offset:12288\n"
"ds_read_b64_tr_b16 a[6:7], v229 offset:14336\n"
"v_mfma_f32_16x16x32_bf16 a[204:207], a[108:111], a[28:31], a[204:207]\n"
"ds_read_b64_tr_b16 a[8:9], v232 offset:4096\n"
"ds_read_b64_tr_b16 a[10:11], v232 offset:6144\n"
"v_mfma_f32_16x16x32_bf16 a[204:207], a[100:103], a[24:27], a[204:207]\n"
"s_waitcnt lgkmcnt(10)\n"
"buffer_load_dwordx4 v196, s[60:63], s67 offen offset:0 sc1 lds\n"
"v_mfma_f32_16x16x32_bf16 a[208:211], a[96:99], a[32:35], a[208:211]\n"
"ds_read_b64_tr_b16 a[12:13], v232 offset:12288\n"
"ds_read_b64_tr_b16 a[14:15], v232 offset:14336\n"
"v_mfma_f32_16x16x32_bf16 a[208:211], a[104:107], a[36:39], a[208:211]\n"
"ds_read_b64_tr_b16 a[16:17], v229 offset:4608\n"
"ds_read_b64_tr_b16 a[18:19], v229 offset:6656\n"
"v_mfma_f32_16x16x32_bf16 a[212:215], a[108:111], a[36:39], a[212:215]\n"
"buffer_load_dwordx4 v197, s[60:63], s67 offen offset:1024 sc1 lds\n"
"v_mfma_f32_16x16x32_bf16 a[212:215], a[100:103], a[32:35], a[212:215]\n"
"s_waitcnt lgkmcnt(10)\n"
"ds_read_b64_tr_b16 a[20:21], v229 offset:12800\n"
"ds_read_b64_tr_b16 a[22:23], v229 offset:14848\n"
"v_mfma_f32_16x16x32_bf16 a[216:219], a[96:99], a[40:43], a[216:219]\n"
"ds_read_b64_tr_b16 a[24:25], v232 offset:4608\n"
"ds_read_b64_tr_b16 a[26:27], v232 offset:6656\n"
"v_mfma_f32_16x16x32_bf16 a[216:219], a[104:107], a[44:47], a[216:219]\n"
"buffer_load_dwordx4 v198, s[60:63], s67 offen offset:2048 sc1 lds\n"
"v_mfma_f32_16x16x32_bf16 a[220:223], a[108:111], a[44:47], a[220:223]\n"
"ds_read_b64_tr_b16 a[28:29], v232 offset:12800\n"
"ds_read_b64_tr_b16 a[30:31], v232 offset:14848\n"
"v_mfma_f32_16x16x32_bf16 a[220:223], a[100:103], a[40:43], a[220:223]\n"
"s_waitcnt lgkmcnt(12)\n"
"v_lshlrev_b32 v173, 16, v144\n"
"v_mul_f32 v164, v128, v173\n"
"v_mfma_f32_16x16x32_bf16 a[224:227], a[96:99], a[0:3], a[224:227]\n"
"s_waitcnt vmcnt(7)\n"
"s_barrier\n"
"buffer_load_dwordx4 v199, s[60:63], s67 offen offset:3072 sc1 lds\n"
"s_add_u32 m0, s65, 147456\n"
"s_add_u32 s67, s10, 0\n"
"v_mfma_f32_16x16x32_bf16 a[224:227], a[104:107], a[4:7], a[224:227]\n"
"ds_read_b64_tr_b16 a[32:33], v229 offset:32768\n"
"ds_read_b128 a[80:83], v193 offset:16384\n"
"v_mfma_f32_16x16x32_bf16 a[228:231], a[108:111], a[4:7], a[228:231]\n"
"s_mul_i32 s18, s3, 57344\n"
"buffer_load_dword a126, v213, s[36:39], s18 offen offset:0\n"
"v_mfma_f32_16x16x32_bf16 a[228:231], a[100:103], a[0:3], a[228:231]\n"
"s_waitcnt lgkmcnt(10)\n"
"buffer_load_dwordx4 v234, s[36:39], s67 offen offset:0 sc1 lds\n"
"v_mfma_f32_16x16x32_bf16 a[232:235], a[96:99], a[8:11], a[232:235]\n"
"ds_read_b64_tr_b16 a[34:35], v229 offset:34816\n"
"ds_read_b128 a[84:87], v193 offset:18432\n"
"v_mfma_f32_16x16x32_bf16 a[232:235], a[104:107], a[12:15], a[232:235]\n"
"ds_read_b64_tr_b16 a[36:37], v229 offset:40960\n"
"ds_read_b128 a[88:91], v193 offset:16896\n"
"v_mfma_f32_16x16x32_bf16 a[236:239], a[108:111], a[12:15], a[236:239]\n"
"buffer_load_dwordx4 v235, s[36:39], s67 offen offset:1024 sc1 lds\n"
"v_mfma_f32_16x16x32_bf16 a[236:239], a[100:103], a[8:11], a[236:239]\n"
"s_waitcnt lgkmcnt(10)\n"
"ds_read_b64_tr_b16 a[38:39], v229 offset:43008\n"
"ds_read_b128 a[92:95], v193 offset:18944\n"
"v_mfma_f32_16x16x32_bf16 a[240:243], a[96:99], a[16:19], a[240:243]\n"
"buffer_load_dwordx2 v[244:245], v249, s[48:51], s70 offen offset:0 sc1\n"
"v_mfma_f32_16x16x32_bf16 a[240:243], a[104:107], a[20:23], a[240:243]\n"
"IWB0_%=:\n"
"s_cbranch_execz IWB4_%=\n"
"buffer_load_dwordx4 v236, s[36:39], s67 offen offset:2048 sc1 lds\n"
"ds_read_b64_tr_b16 a[40:41], v232 offset:32768\n"
"v_mfma_f32_16x16x32_bf16 a[244:247], a[108:111], a[20:23], a[244:247]\n"
"ds_read_b64_tr_b16 a[42:43], v232 offset:34816\n"
"ds_read_b64_tr_b16 a[44:45], v232 offset:40960\n"
"v_mfma_f32_16x16x32_bf16 a[244:247], a[100:103], a[16:19], a[244:247]\n"
"s_waitcnt lgkmcnt(11)\n"
"ds_read_b64_tr_b16 a[46:47], v232 offset:43008\n"
"ds_read_b64_tr_b16 a[0:1], v229 offset:33280\n"
"v_mfma_f32_16x16x32_bf16 a[248:251], a[96:99], a[24:27], a[248:251]\n"
"buffer_load_dwordx4 v237, s[36:39], s67 offen offset:3072 sc1 lds\n"
"v_mfma_f32_16x16x32_bf16 a[248:251], a[104:107], a[28:31], a[248:251]\n"
"s_add_u32 m0, s65, 16384\n"
"s_add_u32 s67, s10, 256\n"
"ds_read_b64_tr_b16 a[2:3], v229 offset:35328\n"
"v_mfma_f32_16x16x32_bf16 a[252:255], a[108:111], a[28:31], a[252:255]\n"
"ds_read_b64_tr_b16 a[4:5], v229 offset:41472\n"
"ds_read_b64_tr_b16 a[6:7], v229 offset:43520\n"
"v_mfma_f32_16x16x32_bf16 a[252:255], a[100:103], a[24:27], a[252:255]\n"
"s_waitcnt lgkmcnt(9)\n"
"s_waitcnt lgkmcnt(8)\n"
"v_mfma_f32_16x16x32_bf16 a[192:195], a[80:83], a[32:35], a[192:195]\n"
"ds_read_b64_tr_b16 a[8:9], v232 offset:33280\n"
"ds_read_b64_tr_b16 a[10:11], v232 offset:35328\n"
"v_mfma_f32_16x16x32_bf16 a[192:195], a[88:91], a[36:39], a[192:195]\n"
"buffer_load_dwordx4 v234, s[36:39], s67 offen offset:0 sc1 lds\n"
"v_mfma_f32_16x16x32_bf16 a[196:199], a[92:95], a[36:39], a[196:199]\n"
"ds_read_b64_tr_b16 a[12:13], v232 offset:41472\n"
"ds_read_b64_tr_b16 a[14:15], v232 offset:43520\n"
"v_mfma_f32_16x16x32_bf16 a[196:199], a[84:87], a[32:35], a[196:199]\n"
"s_waitcnt lgkmcnt(8)\n"
"ds_read_b64_tr_b16 a[16:17], v229 offset:36864\n"
"ds_read_b64_tr_b16 a[18:19], v229 offset:38912\n"
"v_mfma_f32_16x16x32_bf16 a[200:203], a[80:83], a[40:43], a[200:203]\n"
"ds_read_b64_tr_b16 a[20:21], v229 offset:45056\n"
"ds_read_b64_tr_b16 a[22:23], v229 offset:47104\n"
"v_mfma_f32_16x16x32_bf16 a[200:203], a[88:91], a[44:47], a[200:203]\n"
"buffer_load_dwordx4 v235, s[36:39], s67 offen offset:1024 sc1 lds\n"
"v_mfma_f32_16x16x32_bf16 a[204:207], a[92:95], a[44:47], a[204:207]\n"
"ds_read_b64_tr_b16 a[24:25], v232 offset:36864\n"
"ds_read_b64_tr_b16 a[26:27], v232 offset:38912\n"
"v_mfma_f32_16x16x32_bf16 a[204:207], a[84:87], a[40:43], a[204:207]\n"
"s_waitcnt lgkmcnt(10)\n"
"ds_read_b64_tr_b16 a[28:29], v232 offset:45056\n"
"ds_read_b64_tr_b16 a[30:31], v232 offset:47104\n"
"v_mfma_f32_16x16x32_bf16 a[208:211], a[80:83], a[0:3], a[208:211]\n"
"ds_read_b64_tr_b16 a[32:33], v229 offset:37376\n"
"ds_read_b64_tr_b16 a[34:35], v229 offset:39424\n"
"v_mfma_f32_16x16x32_bf16 a[208:211], a[88:91], a[4:7], a[208:211]\n"
"buffer_load_dwordx4 v236, s[36:39], s67 offen offset:2048 sc1 lds\n"
"v_mfma_f32_16x16x32_bf16 a[212:215], a[92:95], a[4:7], a[212:215]\n"
"ds_read_b64_tr_b16 a[36:37], v229 offset:45568\n"
"ds_read_b64_tr_b16 a[38:39], v229 offset:47616\n"
"v_mfma_f32_16x16x32_bf16 a[212:215], a[84:87], a[0:3], a[212:215]\n"
"s_waitcnt lgkmcnt(12)\n"
"ds_read_b64_tr_b16 a[40:41], v232 offset:37376\n"
"ds_read_b64_tr_b16 a[42:43], v232 offset:39424\n"
"v_mfma_f32_16x16x32_bf16 a[216:219], a[80:83], a[8:11], a[216:219]\n"
"ds_read_b64_tr_b16 a[44:45], v232 offset:45568\n"
"ds_read_b64_tr_b16 a[46:47], v232 offset:47616\n"
"v_mfma_f32_16x16x32_bf16 a[216:219], a[88:91], a[12:15], a[216:219]\n"
"buffer_load_dwordx4 v237, s[36:39], s67 offen offset:3072 sc1 lds\n"
"v_mfma_f32_16x16x32_bf16 a[220:223], a[92:95], a[12:15], a[220:223]\n"
"s_add_u32 m0, s65, 32768\n"
"s_add_u32 s67, s10, 524288\n"
"v_and_b32 v173, 0xffff0000, v144\n"
"v_mfma_f32_16x16x32_bf16 a[220:223], a[84:87], a[8:11], a[220:223]\n"
"s_waitcnt lgkmcnt(12)\n"
"v_fmac_f32 v164, v129, v173\n"
"v_lshlrev_b32 v173, 16, v145\n"
"v_mfma_f32_16x16x32_bf16 a[224:227], a[80:83], a[16:19], a[224:227]\n"
"s_waitcnt vmcnt(10)\n"
"s_barrier\n"
"s_waitcnt vmcnt(6)\n"
"v_mfma_f32_16x16x32_bf16 a[224:227], a[88:91], a[20:23], a[224:227]\n"
"ds_read_b64_tr_b16 a[0:1], v230 offset:0\n"
"v_cmp_eq_u32_e64 s[14:15], s86, v244\n"
"v_mfma_f32_16x16x32_bf16 a[228:231], a[92:95], a[20:23], a[228:231]\n"
"ds_read_b128 a[96:99], v193 offset:49152\n"
"v_cmp_eq_u32_e64 s[16:17], s87, v245\n"
"v_mfma_f32_16x16x32_bf16 a[228:231], a[84:87], a[16:19], a[228:231]\n"
"s_waitcnt lgkmcnt(10)\n"
"buffer_load_dwordx4 v234, s[36:39], s67 offen offset:0 sc1 lds\n"
"v_mfma_f32_16x16x32_bf16 a[232:235], a[80:83], a[24:27], a[232:235]\n"
"s_and_b64 s[14:15], s[14:15], s[16:17]\n"
"ds_read_b64_tr_b16 a[2:3], v230 offset:2048\n"
"ds_read_b128 a[100:103], v193 offset:51200\n"
"v_mfma_f32_16x16x32_bf16 a[232:235], a[88:91], a[28:31], a[232:235]\n"
"ds_read_b64_tr_b16 a[4:5], v230 offset:8192\n"
"ds_read_b128 a[104:107], v193 offset:49664\n"
"v_mfma_f32_16x16x32_bf16 a[236:239], a[92:95], a[28:31], a[236:239]\n"
"ds_read_b64_tr_b16 a[6:7], v230 offset:10240\n"
"ds_read_b128 a[108:111], v193 offset:51712\n"
"v_mfma_f32_16x16x32_bf16 a[236:239], a[84:87], a[24:27], a[236:239]\n"
"s_waitcnt lgkmcnt(12)\n"
"ds_read_b64_tr_b16 a[8:9], v233 offset:0\n"
"ds_read_b64_tr_b16 a[10:11], v233 offset:2048\n"
"v_mfma_f32_16x16x32_bf16 a[240:243], a[80:83], a[32:35], a[240:243]\n"
"buffer_load_dwordx4 v235, s[36:39], s67 offen offset:1024 sc1 lds\n"
"v_mfma_f32_16x16x32_bf16 a[240:243], a[88:91], a[36:39], a[240:243]\n"
"s_cmp_eq_u64 s[14:15], -1\n"
"s_cbranch_scc0 SPX_%=\n"
"SBX_%=:\n"
"s_add_u32 s13, s92, 0\n"
"v_mfma_f32_16x16x32_bf16 a[244:247], a[92:95], a[36:39], a[244:247]\n"
"ds_read_b64_tr_b16 a[12:13], v233 offset:8192\n"
"ds_read_b64_tr_b16 a[14:15], v233 offset:10240\n"
"v_mfma_f32_16x16x32_bf16 a[244:247], a[84:87], a[32:35], a[244:247]\n"
"s_waitcnt lgkmcnt(12)\n"
"buffer_load_dwordx4 a[128:131], v224, s[48:51], s13 offen offset:0 sc1\n"
"v_mfma_f32_16x16x32_bf16 a[248:251], a[80:83], a[40:43], a[248:251]\n"
"ds_read_b64_tr_b16 a[16:17], v230 offset:512\n"
"ds_read_b64_tr_b16 a[18:19], v230 offset:2560\n"
"v_mfma_f32_16x16x32_bf16 a[248:251], a[88:91], a[44:47], a[248:251]\n"
"buffer_load_dwordx4 v236, s[36:39], s67 offen offset:2048 sc1 lds\n"
"v_mfma_f32_16x16x32_bf16 a[252:255], a[92:95], a[44:47], a[252:255]\n"
"buffer_load_dwordx4 a[132:135], v224, s[48:51], s13 offen offset:1024 sc1\n"
"v_mfma_f32_16x16x32_bf16 a[252:255], a[84:87], a[40:43], a[252:255]\n"
"s_waitcnt lgkmcnt(7)\n"
"s_waitcnt lgkmcnt(6)\n"
"ds_read_b64_tr_b16 a[20:21], v230 offset:8704\n"
"v_mfma_f32_16x16x32_bf16 a[192:195], a[96:99], a[0:3], a[192:195]\n"
"s_add_u32 s13, s92, 2048\n"
"ds_read_b64_tr_b16 a[22:23], v230 offset:10752\n"
"ds_read_b64_tr_b16 a[24:25], v233 offset:512\n"
"v_mfma_f32_16x16x32_bf16 a[192:195], a[104:107], a[4:7], a[192:195]\n"
"buffer_load_dwordx4 a[136:139], v224, s[48:51], s13 offen offset:0 sc1\n"
"v_mfma_f32_16x16x32_bf16 a[196:199], a[108:111], a[4:7], a[196:199]\n"
"buffer_load_dwordx4 v237, s[36:39], s67 offen offset:3072 sc1 lds\n"
"ds_read_b64_tr_b16 a[26:27], v233 offset:2560\n"
"v_mfma_f32_16x16x32_bf16 a[196:199], a[100:103], a[0:3], a[196:199]\n"
"s_waitcnt lgkmcnt(6)\n"
"ds_read_b64_tr_b16 a[28:29], v233 offset:8704\n"
"ds_read_b64_tr_b16 a[30:31], v233 offset:10752\n"
"v_mfma_f32_16x16x32_bf16 a[200:203], a[96:99], a[8:11], a[200:203]\n"
"s_add_u32 m0, s65, 49152\n"
"buffer_load_dwordx4 a[140:143], v224, s[48:51], s13 offen offset:1024 sc1\n"
"v_mfma_f32_16x16x32_bf16 a[200:203], a[104:107], a[12:15], a[200:203]\n"
"s_add_u32 s67, s10, 524544\n"
"ds_read_b64_tr_b16 a[32:33], v230 offset:4096\n"
"ds_read_b64_tr_b16 a[34:35], v230 offset:6144\n"
"v_mfma_f32_16x16x32_bf16 a[204:207], a[108:111], a[12:15], a[204:207]\n"
"s_add_u32 s13, s92, 4096\n"
"ds_read_b64_tr_b16 a[36:37], v230 offset:12288\n"
"ds_read_b64_tr_b16 a[38:39], v230 offset:14336\n"
"v_mfma_f32_16x16x32_bf16 a[204:207], a[100:103], a[8:11], a[204:207]\n"
"s_waitcnt lgkmcnt(8)\n"
"buffer_load_dwordx4 a[144:147], v224, s[48:51], s13 offen offset:0 sc1\n"
"v_mfma_f32_16x16x32_bf16 a[208:211], a[96:99], a[16:19], a[208:211]\n"
"ds_read_b64_tr_b16 a[40:41], v233 offset:4096\n"
"ds_read_b64_tr_b16 a[42:43], v233 offset:6144\n"
"v_mfma_f32_16x16x32_bf16 a[208:211], a[104:107], a[20:23], a[208:211]\n"
"buffer_load_dwordx4 v234, s[36:39], s67 offen offset:0 sc1 lds\n"
"v_mfma_f32_16x16x32_bf16 a[212:215], a[108:111], a[20:23], a[212:215]\n"
"buffer_load_dwordx4 a[148:151], v224, s[48:51], s13 offen offset:1024 sc1\n"
"v_mfma_f32_16x16x32_bf16 a[212:215], a[100:103], a[16:19], a[212:215]\n"
"s_waitcnt lgkmcnt(6)\n"
"ds_read_b64_tr_b16 a[44:45], v233 offset:12288\n"
"ds_read_b64_tr_b16 a[46:47], v233 offset:14336\n"
"v_mfma_f32_16x16x32_bf16 a[216:219], a[96:99], a[24:27], a[216:219]\n"
"s_add_u32 s13, s92, 6144\n"
"ds_read_b64_tr_b16 a[0:1], v230 offset:4608\n"
"ds_read_b64_tr_b16 a[2:3], v230 offset:6656\n"
"v_mfma_f32_16x16x32_bf16 a[216:219], a[104:107], a[28:31], a[216:219]\n"
"buffer_load_dwordx4 v235, s[36:39], s67 offen offset:1024 sc1 lds\n"
"v_mfma_f32_16x16x32_bf16 a[220:223], a[108:111], a[28:31], a[220:223]\n"
"buffer_load_dwordx4 a[152:155], v224, s[48:51], s13 offen offset:0 sc1\n"
"v_mfma_f32_16x16x32_bf16 a[220:223], a[100:103], a[24:27], a[220:223]\n"
"s_waitcnt lgkmcnt(6)\n"
"ds_read_b64_tr_b16 a[4:5], v230 offset:12800\n"
"ds_read_b64_tr_b16 a[6:7], v230 offset:14848\n"
"v_mfma_f32_16x16x32_bf16 a[224:227], a[96:99], a[32:35], a[224:227]\n"
"s_waitcnt vmcnt(13)\n"
"s_barrier\n"
"ds_read_b64_tr_b16 a[8:9], v233 offset:4608\n"
"v_mfma_f32_16x16x32_bf16 a[224:227], a[104:107], a[36:39], a[224:227]\n"
"buffer_load_dwordx4 v236, s[36:39], s67 offen offset:2048 sc1 lds\n"
"v_mfma_f32_16x16x32_bf16 a[228:231], a[108:111], a[36:39], a[228:231]\n"
"buffer_load_dwordx4 a[156:159], v224, s[48:51], s13 offen offset:1024 sc1\n"
"ds_read_b64_tr_b16 a[10:11], v233 offset:6656\n"
"v_mfma_f32_16x16x32_bf16 a[228:231], a[100:103], a[32:35], a[228:231]\n"
"s_waitcnt lgkmcnt(6)\n"
"ds_read_b64_tr_b16 a[12:13], v233 offset:12800\n"
"ds_read_b64_tr_b16 a[14:15], v233 offset:14848\n"
"v_mfma_f32_16x16x32_bf16 a[232:235], a[96:99], a[40:43], a[232:235]\n"
"ds_read_b64_tr_b16 a[16:17], v230 offset:16384\n"
"ds_read_b64_tr_b16 a[18:19], v230 offset:18432\n"
"v_mfma_f32_16x16x32_bf16 a[232:235], a[104:107], a[44:47], a[232:235]\n"
"buffer_load_dwordx4 v237, s[36:39], s67 offen offset:3072 sc1 lds\n"
"v_mfma_f32_16x16x32_bf16 a[236:239], a[108:111], a[44:47], a[236:239]\n"
"s_add_u32 m0, s65, 65536\n"
"ds_read_b64_tr_b16 a[20:21], v230 offset:24576\n"
"ds_read_b64_tr_b16 a[22:23], v230 offset:26624\n"
"v_mfma_f32_16x16x32_bf16 a[236:239], a[100:103], a[40:43], a[236:239]\n"
"s_waitcnt lgkmcnt(8)\n"
"ds_read_b64_tr_b16 a[24:25], v233 offset:16384\n"
"ds_read_b64_tr_b16 a[26:27], v233 offset:18432\n"
"v_mfma_f32_16x16x32_bf16 a[240:243], a[96:99], a[0:3], a[240:243]\n"
"s_add_u32 s67, s10, 1048576\n"
"ds_read_b64_tr_b16 a[28:29], v233 offset:24576\n"
"ds_read_b64_tr_b16 a[30:31], v233 offset:26624\n"
"v_mfma_f32_16x16x32_bf16 a[240:243], a[104:107], a[4:7], a[240:243]\n"
"ds_read_b64_tr_b16 a[32:33], v230 offset:16896\n"
"ds_read_b64_tr_b16 a[34:35], v230 offset:18944\n"
"v_mfma_f32_16x16x32_bf16 a[244:247], a[108:111], a[4:7], a[244:247]\n"
"buffer_load_dwordx4 v234, s[36:39], s67 offen offset:0 sc1 lds\n"
"v_mfma_f32_16x16x32_bf16 a[244:247], a[100:103], a[0:3], a[244:247]\n"
"s_waitcnt lgkmcnt(10)\n"
"ds_read_b64_tr_b16 a[36:37], v230 offset:25088\n"
"ds_read_b64_tr_b16 a[38:39], v230 offset:27136\n"
"v_mfma_f32_16x16x32_bf16 a[248:251], a[96:99], a[8:11], a[248:251]\n"
"ds_read_b64_tr_b16 a[40:41], v233 offset:16896\n"
"ds_read_b64_tr_b16 a[42:43], v233 offset:18944\n"
"v_mfma_f32_16x16x32_bf16 a[248:251], a[104:107], a[12:15], a[248:251]\n"
"ds_read_b64_tr_b16 a[44:45], v233 offset:25088\n"
"ds_read_b64_tr_b16 a[46:47], v233 offset:27136\n"
"v_mfma_f32_16x16x32_bf16 a[252:255], a[108:111], a[12:15], a[252:255]\n"
"buffer_load_dwordx4 v235, s[36:39], s67 offen offset:1024 sc1 lds\n"
"v_mfma_f32_16x16x32_bf16 a[252:255], a[100:103], a[8:11], a[252:255]\n"
"s_waitcnt vmcnt(10)\n"
"s_waitcnt lgkmcnt(12)\n"
"ds_read_b64_tr_b16 a[0:1], v230 offset:20480\n"
"v_mfma_f32_16x16x32_bf16 v[0:3], a[16:19], a[128:131], 0\n"
"s_add_u32 s13, s92, 8192\n"
"ds_read_b64_tr_b16 a[2:3], v230 offset:22528\n"
"ds_read_b64_tr_b16 a[4:5], v230 offset:28672\n"
"v_mfma_f32_16x16x32_bf16 v[0:3], a[20:23], a[136:139], v[0:3]\n"
"ds_read_b64_tr_b16 a[6:7], v230 offset:30720\n"
"ds_read_b64_tr_b16 a[8:9], v233 offset:20480\n"
"v_mfma_f32_16x16x32_bf16 v[4:7], a[20:23], a[140:143], 0\n"
"buffer_load_dwordx4 v236, s[36:39], s67 offen offset:2048 sc1 lds\n"
"v_mfma_f32_16x16x32_bf16 v[4:7], a[16:19], a[132:135], v[4:7]\n"
"s_waitcnt lgkmcnt(13)\n"
"ds_read_b64_tr_b16 a[10:11], v233 offset:22528\n"
"ds_read_b64_tr_b16 a[12:13], v233 offset:28672\n"
"v_mfma_f32_16x16x32_bf16 v[8:11], a[24:27], a[128:131], 0\n"
"buffer_load_dwordx4 a[160:163], v224, s[48:51], s13 offen offset:0 sc1\n"
"v_mfma_f32_16x16x32_bf16 v[8:11], a[28:31], a[136:139], v[8:11]\n"
"ds_read_b64_tr_b16 a[14:15], v233 offset:30720\n"
"ds_read_b64_tr_b16 a[16:17], v230 offset:20992\n"
"v_mfma_f32_16x16x32_bf16 v[12:15], a[28:31], a[140:143], 0\n"
"buffer_load_dwordx4 v237, s[36:39], s67 offen offset:3072 sc1 lds\n"
"v_mfma_f32_16x16x32_bf16 v[12:15], a[24:27], a[132:135], v[12:15]\n"
"s_waitcnt lgkmcnt(13)\n"
"s_add_u32 m0, s65, 81920\n"
"ds_read_b64_tr_b16 a[18:19], v230 offset:23040\n"
"v_mfma_f32_16x16x32_bf16 v[16:19], a[32:35], a[128:131], 0\n"
"s_add_u32 s67, s10, 1048832\n"
"buffer_load_dwordx4 a[164:167], v224, s[48:51], s13 offen offset:1024 sc1\n"
"v_mfma_f32_16x16x32_bf16 v[16:19], a[36:39], a[136:139], v[16:19]\n"
"ds_read_b64_tr_b16 a[20:21], v230 offset:29184\n"
"ds_read_b64_tr_b16 a[22:23], v230 offset:31232\n"
"v_mfma_f32_16x16x32_bf16 v[20:23], a[36:39], a[140:143], 0\n"
"ds_read_b64_tr_b16 a[24:25], v233 offset:20992\n"
"ds_read_b64_tr_b16 a[26:27], v233 offset:23040\n"
"v_mfma_f32_16x16x32_bf16 v[20:23], a[32:35], a[132:135], v[20:23]\n"
"s_waitcnt lgkmcnt(14)\n"
"buffer_load_dwordx4 v234, s[36:39], s67 offen offset:0 sc1 lds\n"
"v_mfma_f32_16x16x32_bf16 v[24:27], a[40:43], a[128:131], 0\n"
"s_add_u32 s13, s92, 10240\n"
"ds_read_b64_tr_b16 a[28:29], v233 offset:29184\n"
"v_fmac_f32 v164, v130, v173\n"
"v_mfma_f32_16x16x32_bf16 v[24:27], a[44:47], a[136:139], v[24:27]\n"
"ds_read_b64_tr_b16 a[30:31], v233 offset:31232\n"
"v_and_b32 v173, 0xffff0000, v145\n"
"v_mfma_f32_16x16x32_bf16 v[28:31], a[44:47], a[140:143], 0\n"
"buffer_load_dwordx4 a[168:171], v224, s[48:51], s13 offen offset:0 sc1\n"
"v_mfma_f32_16x16x32_bf16 v[28:31], a[40:43], a[132:135], v[28:31]\n"
"s_waitcnt lgkmcnt(12)\n"
"ds_read_b64_tr_b16 a[32:33], v228 offset:16384\n"
"v_fmac_f32 v164, v131, v173\n"
"v_mfma_f32_16x16x32_bf16 v[32:35], a[0:3], a[128:131], 0\n"
"v_lshlrev_b32 v173, 16, v146\n"
"v_fmac_f32 v164, v132, v173\n"
"v_mfma_f32_16x16x32_bf16 v[32:35], a[4:7], a[136:139], v[32:35]\n"
"buffer_load_dwordx4 v235, s[36:39], s67 offen offset:1024 sc1 lds\n"
"v_mfma_f32_16x16x32_bf16 v[36:39], a[4:7], a[140:143], 0\n"
"ds_read_b64_tr_b16 a[34:35], v228 offset:18432\n"
"v_and_b32 v173, 0xffff0000, v146\n"
"v_mfma_f32_16x16x32_bf16 v[36:39], a[0:3], a[132:135], v[36:39]\n"
"s_waitcnt lgkmcnt(10)\n"
"buffer_load_dwordx4 a[172:175], v224, s[48:51], s13 offen offset:1024 sc1\n"
"v_mfma_f32_16x16x32_bf16 v[40:43], a[8:11], a[128:131], 0\n"
"ds_read_b64_tr_b16 a[36:37], v228 offset:24576\n"
"v_fmac_f32 v164, v133, v173\n"
"v_mfma_f32_16x16x32_bf16 v[40:43], a[12:15], a[136:139], v[40:43]\n"
"ds_read_b64_tr_b16 a[38:39], v228 offset:26624\n"
"v_lshlrev_b32 v173, 16, v147\n"
"v_mfma_f32_16x16x32_bf16 v[44:47], a[12:15], a[140:143], 0\n"
"ds_read_b64_tr_b16 a[40:41], v231 offset:16384\n"
"v_fmac_f32 v164, v134, v173\n"
"v_mfma_f32_16x16x32_bf16 v[44:47], a[8:11], a[132:135], v[44:47]\n"
"s_waitcnt lgkmcnt(9)\n"
"buffer_load_dwordx4 v236, s[36:39], s67 offen offset:2048 sc1 lds\n"
"v_mfma_f32_16x16x32_bf16 v[48:51], a[16:19], a[128:131], 0\n"
"ds_read_b64_tr_b16 a[42:43], v231 offset:18432\n"
"v_and_b32 v173, 0xffff0000, v147\n"
"v_mfma_f32_16x16x32_bf16 v[48:51], a[20:23], a[136:139], v[48:51]\n"
"ds_read_b64_tr_b16 a[44:45], v231 offset:24576\n"
"v_fmac_f32 v164, v135, v173\n"
"v_mfma_f32_16x16x32_bf16 v[52:55], a[20:23], a[140:143], 0\n"
"v_lshlrev_b32 v173, 16, v148\n"
"v_fmac_f32 v164, v136, v173\n"
"v_mfma_f32_16x16x32_bf16 v[52:55], a[16:19], a[132:135], v[52:55]\n"
"s_waitcnt lgkmcnt(7)\n"
"ds_read_b64_tr_b16 a[46:47], v231 offset:26624\n"
"v_and_b32 v173, 0xffff0000, v148\n"
"v_mfma_f32_16x16x32_bf16 v[56:59], a[24:27], a[128:131], 0\n"
"ds_read_b64_tr_b16 a[0:1], v228 offset:16896\n"
"v_fmac_f32 v164, v137, v173\n"
"v_mfma_f32_16x16x32_bf16 v[56:59], a[28:31], a[136:139], v[56:59]\n"
"buffer_load_dwordx4 v237, s[36:39], s67 offen offset:3072 sc1 lds\n"
"v_mfma_f32_16x16x32_bf16 v[60:63], a[28:31], a[140:143], 0\n"
"s_add_u32 m0, s65, 98304\n"
"ds_read_b64_tr_b16 a[2:3], v228 offset:18944\n"
"v_lshlrev_b32 v173, 16, v149\n"
"v_mfma_f32_16x16x32_bf16 v[60:63], a[24:27], a[132:135], v[60:63]\n"
"s_waitcnt lgkmcnt(6)\n"
"ds_read_b64_tr_b16 a[4:5], v228 offset:25088\n"
"v_fmac_f32 v164, v138, v173\n"
"v_mfma_f32_16x16x32_bf16 v[64:67], a[32:35], a[128:131], 0\n"
"s_add_u32 s67, s10, 1572864\n"
"ds_read_b64_tr_b16 a[6:7], v228 offset:27136\n"
"v_and_b32 v173, 0xffff0000, v149\n"
"v_mfma_f32_16x16x32_bf16 v[64:67], a[36:39], a[136:139], v[64:67]\n"
"ds_read_b64_tr_b16 a[8:9], v231 offset:16896\n"
"v_fmac_f32 v164, v139, v173\n"
"v_mfma_f32_16x16x32_bf16 v[68:71], a[36:39], a[140:143], 0\n"
"ds_read_b64_tr_b16 a[10:11], v231 offset:18944\n"
"v_lshlrev_b32 v173, 16, v150\n"
"v_fmac_f32 v164, v140, v173\n"
"v_mfma_f32_16x16x32_bf16 v[68:71], a[32:35], a[132:135], v[68:71]\n"
"s_waitcnt lgkmcnt(6)\n"
"ds_read_b64_tr_b16 a[12:13], v231 offset:25088\n"
"v_and_b32 v173, 0xffff0000, v150\n"
"v_mfma_f32_16x16x32_bf16 v[72:75], a[40:43], a[128:131], 0\n"
"s_mov_b32 s52, s26\n"
"s_and_b32 s53, s27, 0xffff\n"
"ds_read_b64_tr_b16 a[14:15], v231 offset:27136\n"
"v_fmac_f32 v164, v141, v173\n"
"v_mfma_f32_16x16x32_bf16 v[72:75], a[44:47], a[136:139], v[72:75]\n"
"s_mov_b32 s54, 67108864\n"
"s_mov_b32 s55, 0x20000\n"
"s_mul_i32 s93, s3, 458752\n"
"v_lshlrev_b32 v173, 16, v151\n"
"v_mfma_f32_16x16x32_bf16 v[76:79], a[44:47], a[140:143], 0\n"
"s_lshl_b32 s97, s95, 10\n"
"s_add_u32 s93, s93, s97\n"
"ds_read_b64_tr_b16 a[16:17], v228 offset:20480\n"
"v_fmac_f32 v164, v142, v173\n"
"v_mfma_f32_16x16x32_bf16 v[76:79], a[40:43], a[132:135], v[76:79]\n"
"s_waitcnt lgkmcnt(5)\n"
"s_mul_i32 s0, s3, 256\n"
"buffer_load_dwordx4 v234, s[36:39], s67 offen offset:0 sc1 lds\n"
"v_mfma_f32_16x16x32_bf16 v[80:83], a[0:3], a[128:131], 0\n"
"s_lshl_b32 s97, s95, 3\n"
"ds_read_b64_tr_b16 a[18:19], v228 offset:22528\n"
"ds_read_b64_tr_b16 a[20:21], v228 offset:28672\n"
"ds_read_b64_tr_b16 a[22:23], v228 offset:30720\n"
"v_mfma_f32_16x16x32_bf16 v[80:83], a[4:7], a[136:139], v[80:83]\n"
"s_add_u32 s0, s0, s97\n"
"s_add_u32 s0, s0, 6291456\n"
"v_and_b32 v173, 0xffff0000, v151\n"
"v_fmac_f32 v164, v143, v173\n"
"v_mfma_f32_16x16x32_bf16 v[84:87], a[4:7], a[140:143], 0\n"
"s_mul_i32 s1, s69, 256\n"
"s_add_u32 s1, s1, 6291456\n"
"s_mul_i32 s94, s69, 28\n"
"v_add_f32_dpp v164, v164, v164 row_ror:8 row_mask:0xf bank_mask:0xf\n"
"v_mfma_f32_16x16x32_bf16 v[84:87], a[0:3], a[132:135], v[84:87]\n"
"s_waitcnt lgkmcnt(4)\n"
"s_and_b32 s98, s95, 3\n"
"s_add_u32 s94, s94, s98\n"
"v_mov_b32 v173, v164\n"
"v_mfma_f32_16x16x32_bf16 v[88:91], a[8:11], a[128:131], 0\n"
"ds_read_b64_tr_b16 a[24:25], v231 offset:20480\n"
"buffer_load_dwordx4 v235, s[36:39], s67 offen offset:1024 sc1 lds\n"
"ds_read_b64_tr_b16 a[26:27], v231 offset:22528\n"
"ds_read_b64_tr_b16 a[28:29], v231 offset:28672\n"
"ds_read_b64_tr_b16 a[30:31], v231 offset:30720\n"
"v_mfma_f32_16x16x32_bf16 v[88:91], a[12:15], a[136:139], v[88:91]\n"
"s_lshl_b32 s94, s94, 14\n"
"s_lshl_b32 s98, s31, 8\n"
"v_permlane16_swap_b32 v164, v173\n"
"v_mfma_f32_16x16x32_bf16 v[92:95], a[12:15], a[140:143], 0\n"
"v_add_f32 v164, v164, v173\n"
"v_mov_b32 v173, v164\n"
"s_nop 1\n"
"v_permlane32_swap_b32 v164, v173\n"
"v_add_f32 v164, v164, v173\n"
"ds_write_b32 v161, v164 offset:512\n"
"v_mfma_f32_16x16x32_bf16 v[92:95], a[8:11], a[132:135], v[92:95]\n"
"s_waitcnt lgkmcnt(5)\n"
"s_add_u32 s94, s94, s98\n"
"s_andn2_b32 s2, s80, 15\n"
"s_and_b32 s97, s30, 7\n"
"s_mul_i32 s5, s97, 28\n"
"s_lshr_b32 s98, s30, 3\n"
"s_add_u32 s5, s5, s98\n"
"s_cmp_lt_u32 s30, 224\n"
"s_cselect_b32 s5, s5, s30\n"
"s_cselect_b32 s71, 1605632, 0\n"
"s_mul_i32 s11, s97, 200704\n"
"s_lshl_b32 s98, s98, 7\n"
"s_add_u32 s11, s11, s98\n"
"s_and_b32 s97, s31, 1\n"
"s_mul_i32 s97, s97, 114688\n"
"s_add_u32 s11, s11, s97\n"
"s_cmp_lt_u32 s31, 2\n"
"s_cselect_b64 s[84:85], s[20:21], s[22:23]\n"
"s_mov_b32 s56, s84\n"
"s_and_b32 s57, s85, 0xffff\n"
"s_mov_b32 s58, s71\n"
"s_mov_b32 s59, 0x20000\n"
"s_cmp_eq_u32 s31, 0\n"
"s_cselect_b32 s4, 0, 1073741824\n"
"v_mfma_f32_16x16x32_bf16 v[96:99], a[16:19], a[128:131], 0\n"
"s_waitcnt lgkmcnt(0)\n"
"s_waitcnt vmcnt(14)\n"
"s_barrier\n"
"ds_read_b64_tr_b16 a[32:33], v228 offset:20992\n"
"ds_read_b64_tr_b16 a[34:35], v228 offset:23040\n"
"ds_read_b64_tr_b16 a[36:37], v228 offset:29184\n"
"ds_read_b64_tr_b16 a[38:39], v228 offset:31232\n"
"v_mfma_f32_16x16x32_bf16 v[96:99], a[20:23], a[136:139], v[96:99]\n"
"buffer_load_dwordx4 v236, s[36:39], s67 offen offset:2048 sc1 lds\n"
"ds_read_b64_tr_b16 a[40:41], v231 offset:20992\n"
"v_mfma_f32_16x16x32_bf16 v[100:103], a[20:23], a[140:143], 0\n"
"s_lshl_b32 s97, s95, 19\n"
"ds_read_b64_tr_b16 a[42:43], v231 offset:23040\n"
"ds_read_b64_tr_b16 a[44:45], v231 offset:29184\n"
"v_mfma_f32_16x16x32_bf16 v[100:103], a[16:19], a[132:135], v[100:103]\n"
"ds_read_b128 v[168:171], v162 offset:512\n"
"ds_read_b64_tr_b16 a[46:47], v231 offset:31232\n"
"v_mfma_f32_16x16x32_bf16 v[104:107], a[24:27], a[128:131], 0\n"
"s_add_u32 s97, s97, 50331648\n"
"ds_read_b128 v[172:175], v162 offset:528\n"
"ds_read_b64_tr_b16 a[0:1], v228 offset:32768\n"
"v_mfma_f32_16x16x32_bf16 v[104:107], a[28:31], a[136:139], v[104:107]\n"
"buffer_load_dwordx4 v237, s[36:39], s67 offen offset:3072 sc1 lds\n"
"v_mfma_f32_16x16x32_bf16 v[108:111], a[28:31], a[140:143], 0\n"
"s_add_u32 m0, s65, 114688\n"
"s_add_u32 s67, s10, 1573120\n"
"ds_read_b64_tr_b16 a[2:3], v228 offset:34816\n"
"v_mfma_f32_16x16x32_bf16 v[108:111], a[24:27], a[132:135], v[108:111]\n"
"s_waitcnt lgkmcnt(8)\n"
"ds_read_b64_tr_b16 a[4:5], v228 offset:40960\n"
"ds_read_b64_tr_b16 a[6:7], v228 offset:43008\n"
"v_mfma_f32_16x16x32_bf16 v[112:115], a[32:35], a[128:131], 0\n"
"ds_read_b64_tr_b16 a[8:9], v231 offset:32768\n"
"ds_read_b64_tr_b16 a[10:11], v231 offset:34816\n"
"v_mfma_f32_16x16x32_bf16 v[112:115], a[36:39], a[136:139], v[112:115]\n"
"buffer_load_dwordx4 v234, s[36:39], s67 offen offset:0 sc1 lds\n"
"v_mfma_f32_16x16x32_bf16 v[116:119], a[36:39], a[140:143], 0\n"
"s_waitcnt lgkmcnt(6)\n"
"ds_read_b64_tr_b16 a[12:13], v231 offset:40960\n"
"ds_read_b64_tr_b16 a[14:15], v231 offset:43008\n"
"v_mfma_f32_16x16x32_bf16 v[116:119], a[32:35], a[132:135], v[116:119]\n"
"ds_read_b64_tr_b16 a[16:17], v228 offset:33280\n"
"ds_read_b64_tr_b16 a[18:19], v228 offset:35328\n"
"v_mfma_f32_16x16x32_bf16 v[120:123], a[40:43], a[128:131], 0\n"
"ds_read_b64_tr_b16 a[20:21], v228 offset:41472\n"
"ds_read_b64_tr_b16 a[22:23], v228 offset:43520\n"
"v_mfma_f32_16x16x32_bf16 v[120:123], a[44:47], a[136:139], v[120:123]\n"
"buffer_load_dwordx4 v235, s[36:39], s67 offen offset:1024 sc1 lds\n"
"v_mfma_f32_16x16x32_bf16 v[124:127], a[44:47], a[140:143], 0\n"
"ds_read_b64_tr_b16 a[24:25], v231 offset:33280\n"
"v_add_f32 v168, v168, v170\n"
"v_mfma_f32_16x16x32_bf16 v[124:127], a[40:43], a[132:135], v[124:127]\n"
"s_waitcnt lgkmcnt(9)\n"
"ds_read_b64_tr_b16 a[26:27], v231 offset:35328\n"
"v_mfma_f32_16x16x32_bf16 v[0:3], a[0:3], a[144:147], v[0:3]\n"
"s_add_u32 s13, s92, 12288\n"
"ds_read_b64_tr_b16 a[28:29], v231 offset:41472\n"
"v_add_f32 v172, v172, v174\n"
"v_mfma_f32_16x16x32_bf16 v[0:3], a[4:7], a[152:155], v[0:3]\n"
"buffer_load_dwordx4 v236, s[36:39], s67 offen offset:2048 sc1 lds\n"
"v_mfma_f32_16x16x32_bf16 v[4:7], a[4:7], a[156:159], v[4:7]\n"
"ds_read_b64_tr_b16 a[30:31], v231 offset:43520\n"
"v_add_f32 v168, v168, v172\n"
"v_mfma_f32_16x16x32_bf16 v[4:7], a[0:3], a[148:151], v[4:7]\n"
"s_waitcnt lgkmcnt(8)\n"
"ds_read_b64_tr_b16 a[32:33], v228 offset:36864\n"
"v_mul_f32 v168, 0x41317218, v168\n"
"v_mfma_f32_16x16x32_bf16 v[8:11], a[8:11], a[144:147], v[8:11]\n"
"ds_read_b64_tr_b16 a[34:35], v228 offset:38912\n"
"ds_read_b64_tr_b16 a[36:37], v228 offset:45056\n"
"v_mfma_f32_16x16x32_bf16 v[8:11], a[12:15], a[152:155], v[8:11]\n"
"buffer_load_dwordx4 v237, s[36:39], s67 offen offset:3072 sc1 lds\n"
"v_mfma_f32_16x16x32_bf16 v[12:15], a[12:15], a[156:159], v[12:15]\n"
"s_add_u32 m0, s65, 131072\n"
"buffer_load_dwordx4 a[176:179], v224, s[48:51], s13 offen offset:0 sc1\n"
"v_mfma_f32_16x16x32_bf16 v[12:15], a[8:11], a[148:151], v[12:15]\n"
"s_waitcnt lgkmcnt(7)\n"
"ds_read_b64_tr_b16 a[38:39], v228 offset:47104\n"
"v_ashrrev_i32 v169, 31, v168\n"
"v_mfma_f32_16x16x32_bf16 v[16:19], a[16:19], a[144:147], v[16:19]\n"
"s_add_u32 s67, s10, 2097152\n"
"ds_read_b64_tr_b16 a[40:41], v231 offset:36864\n"
"v_or_b32 v169, 0x80000000, v169\n"
"v_mfma_f32_16x16x32_bf16 v[16:19], a[20:23], a[152:155], v[16:19]\n"
"buffer_load_dwordx4 a[180:183], v224, s[48:51], s13 offen offset:1024 sc1\n"
"v_mfma_f32_16x16x32_bf16 v[20:23], a[20:23], a[156:159], v[20:23]\n"
"ds_read_b64_tr_b16 a[42:43], v231 offset:38912\n"
"v_xor_b32 v169, v169, v168\n"
"v_mfma_f32_16x16x32_bf16 v[20:23], a[16:19], a[148:151], v[20:23]\n"
"s_waitcnt lgkmcnt(6)\n"
"ds_read_b64_tr_b16 a[44:45], v231 offset:45056\n"
"ds_read_b64_tr_b16 a[46:47], v231 offset:47104\n"
"v_mfma_f32_16x16x32_bf16 v[24:27], a[24:27], a[144:147], v[24:27]\n"
"s_sub_u32 s68, 2047, s80\n"
"ds_read_b64_tr_b16 a[0:1], v228 offset:37376\n"
"ds_read_b64_tr_b16 a[2:3], v228 offset:39424\n"
"v_mfma_f32_16x16x32_bf16 v[24:27], a[28:31], a[152:155], v[24:27]\n"
"s_add_u32 s13, s92, 14336\n"
"buffer_load_dwordx4 v234, s[36:39], s67 offen offset:0 sc1 lds\n"
"v_mfma_f32_16x16x32_bf16 v[28:31], a[28:31], a[156:159], v[28:31]\n"
"ds_read_b64_tr_b16 a[4:5], v228 offset:45568\n"
"v_sub_u32 v170, s68, v160\n"
"v_mfma_f32_16x16x32_bf16 v[28:31], a[24:27], a[148:151], v[28:31]\n"
"s_waitcnt lgkmcnt(7)\n"
"s_mov_b32 s70, 0xfffff800\n"
"ds_read_b64_tr_b16 a[6:7], v228 offset:47616\n"
"v_mfma_f32_16x16x32_bf16 v[32:35], a[32:35], a[144:147], v[32:35]\n"
"ds_read_b64_tr_b16 a[8:9], v231 offset:37376\n"
"v_and_or_b32 v252, v169, s70, v170\n"
"v_mfma_f32_16x16x32_bf16 v[32:35], a[36:39], a[152:155], v[32:35]\n"
"buffer_load_dwordx4 v235, s[36:39], s67 offen offset:1024 sc1 lds\n"
"v_mfma_f32_16x16x32_bf16 v[36:39], a[36:39], a[156:159], v[36:39]\n"
"ds_read_b64_tr_b16 a[10:11], v231 offset:39424\n"
"ds_read_b64_tr_b16 a[12:13], v231 offset:45568\n"
"v_mfma_f32_16x16x32_bf16 v[36:39], a[32:35], a[148:151], v[36:39]\n"
"s_waitcnt lgkmcnt(7)\n"
"buffer_load_dwordx4 a[184:187], v224, s[48:51], s13 offen offset:0 sc1\n"
"v_mov_b32 v254, 0\n"
"v_mfma_f32_16x16x32_bf16 v[40:43], a[40:43], a[144:147], v[40:43]\n"
"ds_read_b64_tr_b16 a[14:15], v231 offset:47616\n"
"ds_read_b64_tr_b16 a[16:17], v228 offset:49152\n"
"v_mfma_f32_16x16x32_bf16 v[40:43], a[44:47], a[152:155], v[40:43]\n"
"ds_read_b64_tr_b16 a[18:19], v228 offset:51200\n"
"v_min_u32_dpp v168, v252, v252 quad_perm:[1,0,3,2] row_mask:0xf bank_mask:0xf\n"
"v_mfma_f32_16x16x32_bf16 v[44:47], a[44:47], a[156:159], v[44:47]\n"
"buffer_load_dwordx4 a[188:191], v224, s[48:51], s13 offen offset:1024 sc1\n"
"v_max_u32_dpp v169, v254, v254 quad_perm:[1,0,3,2] row_mask:0xf bank_mask:0xf\n"
"v_mfma_f32_16x16x32_bf16 v[44:47], a[40:43], a[148:151], v[44:47]\n"
"s_waitcnt lgkmcnt(6)\n"
"buffer_load_dwordx4 v236, s[36:39], s67 offen offset:2048 sc1 lds\n"
"ds_read_b64_tr_b16 a[20:21], v228 offset:57344\n"
"v_mfma_f32_16x16x32_bf16 v[48:51], a[0:3], a[144:147], v[48:51]\n"
"ds_read_b64_tr_b16 a[22:23], v228 offset:59392\n"
"v_max_u32_dpp v252, v252, v252 quad_perm:[1,0,3,2] row_mask:0xf bank_mask:0xf\n"
"v_mfma_f32_16x16x32_bf16 v[48:51], a[4:7], a[152:155], v[48:51]\n"
"ds_read_b64_tr_b16 a[24:25], v231 offset:49152\n"
"ds_read_b64_tr_b16 a[26:27], v231 offset:51200\n"
"v_mfma_f32_16x16x32_bf16 v[52:55], a[4:7], a[156:159], v[52:55]\n"
"ds_read_b64_tr_b16 a[28:29], v231 offset:57344\n"
"v_max_u32 v254, v168, v169\n"
"v_mfma_f32_16x16x32_bf16 v[52:55], a[0:3], a[148:151], v[52:55]\n"
"s_waitcnt lgkmcnt(7)\n"
"buffer_load_dwordx4 v237, s[36:39], s67 offen offset:3072 sc1 lds\n"
"v_min_u32_dpp v168, v252, v252 quad_perm:[2,3,0,1] row_mask:0xf bank_mask:0xf\n"
"v_mfma_f32_16x16x32_bf16 v[56:59], a[8:11], a[144:147], v[56:59]\n"
"s_add_u32 m0, s65, 147456\n"
"ds_read_b64_tr_b16 a[30:31], v231 offset:59392\n"
"ds_read_b64_tr_b16 a[32:33], v228 offset:49664\n"
"v_mfma_f32_16x16x32_bf16 v[56:59], a[12:15], a[152:155], v[56:59]\n"
"s_add_u32 s67, s10, 2097408\n"
"ds_read_b64_tr_b16 a[34:35], v228 offset:51712\n"
"v_max_u32_dpp v169, v254, v254 quad_perm:[2,3,0,1] row_mask:0xf bank_mask:0xf\n"
"v_mfma_f32_16x16x32_bf16 v[60:63], a[12:15], a[156:159], v[60:63]\n"
"ds_read_b64_tr_b16 a[36:37], v228 offset:57856\n"
"v_max_u32_dpp v252, v252, v252 quad_perm:[2,3,0,1] row_mask:0xf bank_mask:0xf\n"
"v_mfma_f32_16x16x32_bf16 v[60:63], a[8:11], a[148:151], v[60:63]\n"
"IWB1_%=:\n"
"s_cbranch_execz IWB5_%=\n"
"s_waitcnt lgkmcnt(7)\n"
"ds_read_b64_tr_b16 a[38:39], v228 offset:59904\n"
"ds_read_b64_tr_b16 a[40:41], v231 offset:49664\n"
"v_mfma_f32_16x16x32_bf16 v[64:67], a[16:19], a[144:147], v[64:67]\n"
"ds_read_b64_tr_b16 a[42:43], v231 offset:51712\n"
"v_max_u32 v254, v168, v169\n"
"v_mfma_f32_16x16x32_bf16 v[64:67], a[20:23], a[152:155], v[64:67]\n"
"ds_read_b64_tr_b16 a[44:45], v231 offset:57856\n"
"v_min_u32_dpp v168, v252, v252 row_half_mirror row_mask:0xf bank_mask:0xf\n"
"v_mfma_f32_16x16x32_bf16 v[68:71], a[20:23], a[156:159], v[68:71]\n"
"buffer_load_dwordx4 v234, s[36:39], s67 offen offset:0 sc1 lds\n"
"v_mfma_f32_16x16x32_bf16 v[68:71], a[16:19], a[148:151], v[68:71]\n"
"s_waitcnt lgkmcnt(7)\n"
"ds_read_b64_tr_b16 a[46:47], v231 offset:59904\n"
"v_max_u32_dpp v169, v254, v254 row_half_mirror row_mask:0xf bank_mask:0xf\n"
"v_mfma_f32_16x16x32_bf16 v[72:75], a[24:27], a[144:147], v[72:75]\n"
"ds_read_b64_tr_b16 a[0:1], v228 offset:53248\n"
"ds_read_b64_tr_b16 a[2:3], v228 offset:55296\n"
"v_mfma_f32_16x16x32_bf16 v[72:75], a[28:31], a[152:155], v[72:75]\n"
"ds_read_b64_tr_b16 a[4:5], v228 offset:61440\n"
"v_max_u32_dpp v252, v252, v252 row_half_mirror row_mask:0xf bank_mask:0xf\n"
"v_mfma_f32_16x16x32_bf16 v[76:79], a[28:31], a[156:159], v[76:79]\n"
"buffer_load_dwordx4 v235, s[36:39], s67 offen offset:1024 sc1 lds\n"
"v_mfma_f32_16x16x32_bf16 v[76:79], a[24:27], a[148:151], v[76:79]\n"
"s_waitcnt lgkmcnt(7)\n"
"ds_read_b64_tr_b16 a[6:7], v228 offset:63488\n"
"v_max_u32 v254, v168, v169\n"
"v_mfma_f32_16x16x32_bf16 v[80:83], a[32:35], a[144:147], v[80:83]\n"
"ds_read_b64_tr_b16 a[8:9], v231 offset:53248\n"
"v_mov_b32 v253, s86\n"
"v_mfma_f32_16x16x32_bf16 v[80:83], a[36:39], a[152:155], v[80:83]\n"
"ds_read_b64_tr_b16 a[10:11], v231 offset:55296\n"
"ds_read_b64_tr_b16 a[12:13], v231 offset:61440\n"
"v_mfma_f32_16x16x32_bf16 v[84:87], a[36:39], a[156:159], v[84:87]\n"
"buffer_load_dwordx4 v236, s[36:39], s67 offen offset:2048 sc1 lds\n"
"v_mfma_f32_16x16x32_bf16 v[84:87], a[32:35], a[148:151], v[84:87]\n"
"s_waitcnt lgkmcnt(7)\n"
"ds_read_b64_tr_b16 a[14:15], v231 offset:63488\n"
"v_mov_b32 v255, s87\n"
"v_mfma_f32_16x16x32_bf16 v[88:91], a[40:43], a[144:147], v[88:91]\n"
"s_lshl_b32 s68, s30, 4\n"
"ds_read_b64_tr_b16 a[16:17], v228 offset:53760\n"
"ds_read_b64_tr_b16 a[18:19], v228 offset:55808\n"
"v_mfma_f32_16x16x32_bf16 v[88:91], a[44:47], a[152:155], v[88:91]\n"
"s_add_u32 s68, s68, 16384\n"
"ds_read_b64_tr_b16 a[20:21], v228 offset:61952\n"
"ds_read_b64_tr_b16 a[22:23], v228 offset:64000\n"
"v_mfma_f32_16x16x32_bf16 v[92:95], a[44:47], a[156:159], v[92:95]\n"
"s_add_u32 s68, s68, s4\n"
"buffer_load_dwordx4 v237, s[36:39], s67 offen offset:3072 sc1 lds\n"
"v_mfma_f32_16x16x32_bf16 v[92:95], a[40:43], a[148:151], v[92:95]\n"
"s_waitcnt lgkmcnt(8)\n"
"ds_read_b64_tr_b16 a[24:25], v231 offset:53760\n"
"ds_read_b64_tr_b16 a[26:27], v231 offset:55808\n"
"v_mfma_f32_16x16x32_bf16 v[96:99], a[0:3], a[144:147], v[96:99]\n"
"s_waitcnt vmcnt(20)\n"
"s_barrier\n"
"ds_read_b64_tr_b16 a[28:29], v231 offset:61952\n"
"v_mfma_f32_16x16x32_bf16 v[96:99], a[4:7], a[152:155], v[96:99]\n"
"s_add_u32 m0, s65, 0\n"
"buffer_store_dwordx4 v[252:255], v241, s[48:51], s68 offen offset:0 sc1\n"
"v_mfma_f32_16x16x32_bf16 v[100:103], a[4:7], a[156:159], v[100:103]\n"
"s_add_u32 s67, s10, 2621440\n"
"ds_read_b64_tr_b16 a[30:31], v231 offset:64000\n"
"ds_read_b64_tr_b16 a[32:33], v229 offset:0\n"
"v_mfma_f32_16x16x32_bf16 v[100:103], a[0:3], a[148:151], v[100:103]\n"
"s_waitcnt lgkmcnt(9)\n"
"ds_read_b64_tr_b16 a[34:35], v229 offset:2048\n"
"ds_read_b64_tr_b16 a[36:37], v229 offset:8192\n"
"v_mfma_f32_16x16x32_bf16 v[104:107], a[8:11], a[144:147], v[104:107]\n"
"buffer_load_dwordx4 v234, s[36:39], s67 offen offset:0 sc1 lds\n"
"v_mfma_f32_16x16x32_bf16 v[104:107], a[12:15], a[152:155], v[104:107]\n"
"ds_read_b64_tr_b16 a[38:39], v229 offset:10240\n"
"ds_read_b64_tr_b16 a[40:41], v232 offset:0\n"
"v_mfma_f32_16x16x32_bf16 v[108:111], a[12:15], a[156:159], v[108:111]\n"
"ds_read_b64_tr_b16 a[42:43], v232 offset:2048\n"
"ds_read_b64_tr_b16 a[44:45], v232 offset:8192\n"
"v_mfma_f32_16x16x32_bf16 v[108:111], a[8:11], a[148:151], v[108:111]\n"
"s_waitcnt lgkmcnt(11)\n"
"ds_read_b64_tr_b16 a[46:47], v232 offset:10240\n"
"ds_read_b64_tr_b16 a[0:1], v229 offset:512\n"
"v_mfma_f32_16x16x32_bf16 v[112:115], a[16:19], a[144:147], v[112:115]\n"
"buffer_load_dwordx4 v235, s[36:39], s67 offen offset:1024 sc1 lds\n"
"v_mfma_f32_16x16x32_bf16 v[112:115], a[20:23], a[152:155], v[112:115]\n"
"ds_read_b64_tr_b16 a[2:3], v229 offset:2560\n"
"ds_read_b64_tr_b16 a[4:5], v229 offset:8704\n"
"v_mfma_f32_16x16x32_bf16 v[116:119], a[20:23], a[156:159], v[116:119]\n"
"ds_read_b64_tr_b16 a[6:7], v229 offset:10752\n"
"ds_read_b64_tr_b16 a[8:9], v232 offset:512\n"
"v_mfma_f32_16x16x32_bf16 v[116:119], a[16:19], a[148:151], v[116:119]\n"
"s_waitcnt lgkmcnt(13)\n"
"ds_read_b64_tr_b16 a[10:11], v232 offset:2560\n"
"ds_read_b64_tr_b16 a[12:13], v232 offset:8704\n"
"v_mfma_f32_16x16x32_bf16 v[120:123], a[24:27], a[144:147], v[120:123]\n"
"buffer_load_dwordx4 v236, s[36:39], s67 offen offset:2048 sc1 lds\n"
"v_mfma_f32_16x16x32_bf16 v[120:123], a[28:31], a[152:155], v[120:123]\n"
"ds_read_b64_tr_b16 a[14:15], v232 offset:10752\n"
"ds_read_b64_tr_b16 a[16:17], v229 offset:4096\n"
"v_mfma_f32_16x16x32_bf16 v[124:127], a[28:31], a[156:159], v[124:127]\n"
"ds_read_b64_tr_b16 a[18:19], v229 offset:6144\n"
"v_bfe_u32 v184, v224, 8, 1\n"
"v_mfma_f32_16x16x32_bf16 v[124:127], a[24:27], a[148:151], v[124:127]\n"
"s_waitcnt lgkmcnt(14)\n"
"ds_read_b64_tr_b16 a[20:21], v229 offset:12288\n"
"v_mfma_f32_16x16x32_bf16 v[0:3], a[32:35], a[160:163], v[0:3]\n"
"s_add_u32 s13, s92, 16384\n"
"buffer_load_dwordx4 v237, s[36:39], s67 offen offset:3072 sc1 lds\n"
"v_mfma_f32_16x16x32_bf16 v[0:3], a[36:39], a[168:171], v[0:3]\n"
"s_add_u32 m0, s65, 16384\n"
"buffer_load_dwordx4 a[128:131], v224, s[48:51], s13 offen offset:0 sc1\n"
"v_mfma_f32_16x16x32_bf16 v[4:7], a[36:39], a[172:175], v[4:7]\n"
"s_add_u32 s67, s10, 2621696\n"
"ds_read_b64_tr_b16 a[22:23], v229 offset:14336\n"
"v_accvgpr_read_b32 v200, a192\n"
"v_mfma_f32_16x16x32_bf16 v[4:7], a[32:35], a[164:167], v[4:7]\n"
"s_waitcnt lgkmcnt(12)\n"
"ds_read_b64_tr_b16 a[24:25], v232 offset:4096\n"
"v_accvgpr_read_b32 v201, a193\n"
"v_mfma_f32_16x16x32_bf16 v[8:11], a[40:43], a[160:163], v[8:11]\n"
"s_lshl_b32 s18, s3, 1\n"
"buffer_load_dwordx4 a[132:135], v224, s[48:51], s13 offen offset:1024 sc1\n"
"v_mfma_f32_16x16x32_bf16 v[8:11], a[44:47], a[168:171], v[8:11]\n"
"s_and_b32 s88, s95, 1\n"
"ds_read_b64_tr_b16 a[26:27], v232 offset:6144\n"
"v_accvgpr_read_b32 v202, a194\n"
"v_mfma_f32_16x16x32_bf16 v[12:15], a[44:47], a[172:175], v[12:15]\n"
"s_add_u32 s18, s18, s88\n"
"buffer_load_dwordx4 v234, s[36:39], s67 offen offset:0 sc1 lds\n"
"v_mfma_f32_16x16x32_bf16 v[12:15], a[40:43], a[164:167], v[12:15]\n"
"s_waitcnt lgkmcnt(10)\n"
"s_lshl_b32 s18, s18, 11\n"
"ds_read_b64_tr_b16 a[28:29], v232 offset:12288\n"
"v_mfma_f32_16x16x32_bf16 v[16:19], a[0:3], a[160:163], v[16:19]\n"
"buffer_load_dword a126, v195, s[40:43], s18 offen offset:0\n"
"v_mfma_f32_16x16x32_bf16 v[16:19], a[4:7], a[168:171], v[16:19]\n"
"ds_read_b64_tr_b16 a[30:31], v232 offset:14336\n"
"v_accvgpr_read_b32 v203, a195\n"
"v_mfma_f32_16x16x32_bf16 v[20:23], a[4:7], a[172:175], v[20:23]\n"
"ds_read_b64_tr_b16 a[32:33], v229 offset:4608\n"
"ds_read_b64_tr_b16 a[34:35], v229 offset:6656\n"
"v_mfma_f32_16x16x32_bf16 v[20:23], a[0:3], a[164:167], v[20:23]\n"
"s_waitcnt lgkmcnt(10)\n"
"buffer_load_dwordx4 v235, s[36:39], s67 offen offset:1024 sc1 lds\n"
"v_mfma_f32_16x16x32_bf16 v[24:27], a[8:11], a[160:163], v[24:27]\n"
"s_add_u32 s13, s92, 18432\n"
"ds_read_b64_tr_b16 a[36:37], v229 offset:12800\n"
"v_accvgpr_read_b32 v204, a196\n"
"v_mfma_f32_16x16x32_bf16 v[24:27], a[12:15], a[168:171], v[24:27]\n"
"ds_read_b64_tr_b16 a[38:39], v229 offset:14848\n"
"v_accvgpr_read_b32 v205, a197\n"
"v_mfma_f32_16x16x32_bf16 v[28:31], a[12:15], a[172:175], v[28:31]\n"
"ds_read_b64_tr_b16 a[40:41], v232 offset:4608\n"
"ds_read_b64_tr_b16 a[42:43], v232 offset:6656\n"
"v_mfma_f32_16x16x32_bf16 v[28:31], a[8:11], a[164:167], v[28:31]\n"
"s_waitcnt lgkmcnt(10)\n"
"buffer_load_dwordx4 v236, s[36:39], s67 offen offset:2048 sc1 lds\n"
"v_mfma_f32_16x16x32_bf16 v[32:35], a[16:19], a[160:163], v[32:35]\n"
"buffer_load_dwordx4 a[136:139], v224, s[48:51], s13 offen offset:0 sc1\n"
"v_mfma_f32_16x16x32_bf16 v[32:35], a[20:23], a[168:171], v[32:35]\n"
"ds_read_b64_tr_b16 a[44:45], v232 offset:12800\n"
"v_accvgpr_read_b32 v206, a198\n"
"v_mfma_f32_16x16x32_bf16 v[36:39], a[20:23], a[172:175], v[36:39]\n"
"ds_read_b64_tr_b16 a[46:47], v232 offset:14848\n"
"v_accvgpr_read_b32 v207, a199\n"
"v_mfma_f32_16x16x32_bf16 v[36:39], a[16:19], a[164:167], v[36:39]\n"
"s_waitcnt lgkmcnt(8)\n"
"ds_read_b64_tr_b16 a[0:1], v229 offset:16384\n"
"ds_read_b64_tr_b16 a[2:3], v229 offset:18432\n"
"v_mfma_f32_16x16x32_bf16 v[40:43], a[24:27], a[160:163], v[40:43]\n"
"buffer_load_dwordx4 a[140:143], v224, s[48:51], s13 offen offset:1024 sc1\n"
"v_mfma_f32_16x16x32_bf16 v[40:43], a[28:31], a[168:171], v[40:43]\n"
"buffer_load_dwordx4 v237, s[36:39], s67 offen offset:3072 sc1 lds\n"
"v_mfma_f32_16x16x32_bf16 v[44:47], a[28:31], a[172:175], v[44:47]\n"
"s_add_u32 m0, s65, 32768\n"
"ds_read_b64_tr_b16 a[4:5], v229 offset:24576\n"
"v_cvt_pk_bf16_f32 v128, v200, v201\n"
"v_mfma_f32_16x16x32_bf16 v[44:47], a[24:27], a[164:167], v[44:47]\n"
"s_waitcnt lgkmcnt(7)\n"
"ds_read_b64_tr_b16 a[6:7], v229 offset:26624\n"
"v_cvt_pk_bf16_f32 v129, v202, v203\n"
"v_mfma_f32_16x16x32_bf16 v[48:51], a[32:35], a[160:163], v[48:51]\n"
"s_add_u32 s67, s10, 3145728\n"
"ds_read_b64_tr_b16 a[8:9], v232 offset:16384\n"
"ds_read_b64_tr_b16 a[10:11], v232 offset:18432\n"
"v_mfma_f32_16x16x32_bf16 v[48:51], a[36:39], a[168:171], v[48:51]\n"
"ds_read_b64_tr_b16 a[12:13], v232 offset:24576\n"
"v_cvt_pk_bf16_f32 v130, v204, v205\n"
"v_mfma_f32_16x16x32_bf16 v[52:55], a[36:39], a[172:175], v[52:55]\n"
"ds_read_b64_tr_b16 a[14:15], v232 offset:26624\n"
"v_cvt_pk_bf16_f32 v131, v206, v207\n"
"v_mfma_f32_16x16x32_bf16 v[52:55], a[32:35], a[164:167], v[52:55]\n"
"s_waitcnt lgkmcnt(8)\n"
"ds_read_b64_tr_b16 a[16:17], v229 offset:16896\n"
"ds_read_b64_tr_b16 a[18:19], v229 offset:18944\n"
"v_mfma_f32_16x16x32_bf16 v[56:59], a[40:43], a[160:163], v[56:59]\n"
"ds_read_b64_tr_b16 a[20:21], v229 offset:25088\n"
"v_accvgpr_read_b32 v208, a200\n"
"v_mfma_f32_16x16x32_bf16 v[56:59], a[44:47], a[168:171], v[56:59]\n"
"ds_read_b64_tr_b16 a[22:23], v229 offset:27136\n"
"v_accvgpr_read_b32 v209, a201\n"
"v_mfma_f32_16x16x32_bf16 v[60:63], a[44:47], a[172:175], v[60:63]\n"
"buffer_load_dwordx4 v234, s[36:39], s67 offen offset:0 sc1 lds\n"
"v_mfma_f32_16x16x32_bf16 v[60:63], a[40:43], a[164:167], v[60:63]\n"
"s_waitcnt lgkmcnt(8)\n"
"ds_read_b64_tr_b16 a[24:25], v232 offset:16896\n"
"ds_read_b64_tr_b16 a[26:27], v232 offset:18944\n"
"v_mfma_f32_16x16x32_bf16 v[64:67], a[0:3], a[160:163], v[64:67]\n"
"ds_read_b64_tr_b16 a[28:29], v232 offset:25088\n"
"v_accvgpr_read_b32 v210, a202\n"
"v_mfma_f32_16x16x32_bf16 v[64:67], a[4:7], a[168:171], v[64:67]\n"
"ds_read_b64_tr_b16 a[30:31], v232 offset:27136\n"
"v_accvgpr_read_b32 v211, a203\n"
"v_mfma_f32_16x16x32_bf16 v[68:71], a[4:7], a[172:175], v[68:71]\n"
"ds_read_b64_tr_b16 a[32:33], v229 offset:20480\n"
"v_accvgpr_read_b32 v200, a204\n"
"v_mfma_f32_16x16x32_bf16 v[68:71], a[0:3], a[164:167], v[68:71]\n"
"s_waitcnt lgkmcnt(9)\n"
"buffer_load_dwordx4 v235, s[36:39], s67 offen offset:1024 sc1 lds\n"
"v_mfma_f32_16x16x32_bf16 v[72:75], a[8:11], a[160:163], v[72:75]\n"
"ds_read_b64_tr_b16 a[34:35], v229 offset:22528\n"
"ds_read_b64_tr_b16 a[36:37], v229 offset:28672\n"
"v_mfma_f32_16x16x32_bf16 v[72:75], a[12:15], a[168:171], v[72:75]\n"
"ds_read_b64_tr_b16 a[38:39], v229 offset:30720\n"
"v_accvgpr_read_b32 v201, a205\n"
"v_mfma_f32_16x16x32_bf16 v[76:79], a[12:15], a[172:175], v[76:79]\n"
"ds_read_b64_tr_b16 a[40:41], v232 offset:20480\n"
"v_accvgpr_read_b32 v202, a206\n"
"v_mfma_f32_16x16x32_bf16 v[76:79], a[8:11], a[164:167], v[76:79]\n"
"s_waitcnt lgkmcnt(9)\n"
"ds_read_b64_tr_b16 a[42:43], v232 offset:22528\n"
"ds_read_b64_tr_b16 a[44:45], v232 offset:28672\n"
"v_mfma_f32_16x16x32_bf16 v[80:83], a[16:19], a[160:163], v[80:83]\n"
"buffer_load_dwordx4 v236, s[36:39], s67 offen offset:2048 sc1 lds\n"
"v_mfma_f32_16x16x32_bf16 v[80:83], a[20:23], a[168:171], v[80:83]\n"
"ds_read_b64_tr_b16 a[46:47], v232 offset:30720\n"
"v_accvgpr_read_b32 v203, a207\n"
"v_mfma_f32_16x16x32_bf16 v[84:87], a[20:23], a[172:175], v[84:87]\n"
"ds_read_b64_tr_b16 a[0:1], v229 offset:20992\n"
"v_cvt_pk_bf16_f32 v132, v208, v209\n"
"v_mfma_f32_16x16x32_bf16 v[84:87], a[16:19], a[164:167], v[84:87]\n"
"s_waitcnt lgkmcnt(9)\n"
"ds_read_b64_tr_b16 a[2:3], v229 offset:23040\n"
"ds_read_b64_tr_b16 a[4:5], v229 offset:29184\n"
"v_mfma_f32_16x16x32_bf16 v[88:91], a[24:27], a[160:163], v[88:91]\n"
"buffer_load_dwordx4 v237, s[36:39], s67 offen offset:3072 sc1 lds\n"
"v_mfma_f32_16x16x32_bf16 v[88:91], a[28:31], a[168:171], v[88:91]\n"
"ds_read_b64_tr_b16 a[6:7], v229 offset:31232\n"
"v_cvt_pk_bf16_f32 v133, v210, v211\n"
"v_mfma_f32_16x16x32_bf16 v[92:95], a[28:31], a[172:175], v[92:95]\n"
"ds_read_b64_tr_b16 a[8:9], v232 offset:20992\n"
"v_cvt_pk_bf16_f32 v134, v200, v201\n"
"v_mfma_f32_16x16x32_bf16 v[92:95], a[24:27], a[164:167], v[92:95]\n"
"s_waitcnt lgkmcnt(9)\n"
"ds_read_b64_tr_b16 a[10:11], v232 offset:23040\n"
"ds_read_b64_tr_b16 a[12:13], v232 offset:29184\n"
"v_mfma_f32_16x16x32_bf16 v[96:99], a[32:35], a[160:163], v[96:99]\n"
"s_waitcnt vmcnt(30)\n"
"s_barrier\n"
"v_cvt_pk_bf16_f32 v135, v202, v203\n"
"v_mfma_f32_16x16x32_bf16 v[96:99], a[36:39], a[168:171], v[96:99]\n"
"s_add_u32 m0, s65, 49152\n"
"s_add_u32 s67, s10, 3145984\n"
"ds_read_b64_tr_b16 a[14:15], v232 offset:31232\n"
"v_mfma_f32_16x16x32_bf16 v[100:103], a[36:39], a[172:175], v[100:103]\n"
"ds_read_b64_tr_b16 a[16:17], v229 offset:32768\n"
"v_accvgpr_read_b32 v204, a208\n"
"v_mfma_f32_16x16x32_bf16 v[100:103], a[32:35], a[164:167], v[100:103]\n"
"s_waitcnt lgkmcnt(9)\n"
"ds_read_b64_tr_b16 a[18:19], v229 offset:34816\n"
"ds_read_b64_tr_b16 a[20:21], v229 offset:40960\n"
"v_mfma_f32_16x16x32_bf16 v[104:107], a[40:43], a[160:163], v[104:107]\n"
"buffer_load_dwordx4 v234, s[36:39], s67 offen offset:0 sc1 lds\n"
"v_mfma_f32_16x16x32_bf16 v[104:107], a[44:47], a[168:171], v[104:107]\n"
"ds_read_b64_tr_b16 a[22:23], v229 offset:43008\n"
"v_accvgpr_read_b32 v205, a209\n"
"v_mfma_f32_16x16x32_bf16 v[108:111], a[44:47], a[172:175], v[108:111]\n"
"ds_read_b64_tr_b16 a[24:25], v232 offset:32768\n"
"v_accvgpr_read_b32 v206, a210\n"
"v_mfma_f32_16x16x32_bf16 v[108:111], a[40:43], a[164:167], v[108:111]\n"
"s_waitcnt lgkmcnt(9)\n"
"ds_read_b64_tr_b16 a[26:27], v232 offset:34816\n"
"ds_read_b64_tr_b16 a[28:29], v232 offset:40960\n"
"v_mfma_f32_16x16x32_bf16 v[112:115], a[0:3], a[160:163], v[112:115]\n"
"buffer_load_dwordx4 v235, s[36:39], s67 offen offset:1024 sc1 lds\n"
"v_mfma_f32_16x16x32_bf16 v[112:115], a[4:7], a[168:171], v[112:115]\n"
"ds_read_b64_tr_b16 a[30:31], v232 offset:43008\n"
"v_accvgpr_read_b32 v207, a211\n"
"v_mfma_f32_16x16x32_bf16 v[116:119], a[4:7], a[172:175], v[116:119]\n"
"ds_read_b64_tr_b16 a[32:33], v229 offset:33280\n"
"v_accvgpr_read_b32 v208, a212\n"
"v_mfma_f32_16x16x32_bf16 v[116:119], a[0:3], a[164:167], v[116:119]\n"
"s_waitcnt lgkmcnt(9)\n"
"ds_read_b64_tr_b16 a[34:35], v229 offset:35328\n"
"ds_read_b64_tr_b16 a[36:37], v229 offset:41472\n"
"v_mfma_f32_16x16x32_bf16 v[120:123], a[8:11], a[160:163], v[120:123]\n"
"buffer_load_dwordx4 v236, s[36:39], s67 offen offset:2048 sc1 lds\n"
"v_mfma_f32_16x16x32_bf16 v[120:123], a[12:15], a[168:171], v[120:123]\n"
"ds_read_b64_tr_b16 a[38:39], v229 offset:43520\n"
"v_accvgpr_read_b32 v209, a213\n"
"v_mfma_f32_16x16x32_bf16 v[124:127], a[12:15], a[172:175], v[124:127]\n"
"ds_read_b64_tr_b16 a[40:41], v232 offset:33280\n"
"v_accvgpr_read_b32 v210, a214\n"
"v_mfma_f32_16x16x32_bf16 v[124:127], a[8:11], a[164:167], v[124:127]\n"
"s_waitcnt vmcnt(27)\n"
"s_waitcnt lgkmcnt(9)\n"
"ds_read_b64_tr_b16 a[42:43], v232 offset:35328\n"
"v_mfma_f32_16x16x32_bf16 v[0:3], a[16:19], a[176:179], v[0:3]\n"
"s_add_u32 s13, s92, 20480\n"
"buffer_load_dwordx4 v237, s[36:39], s67 offen offset:3072 sc1 lds\n"
"v_mfma_f32_16x16x32_bf16 v[0:3], a[20:23], a[184:187], v[0:3]\n"
"s_add_u32 m0, s65, 65536\n"
"ds_read_b64_tr_b16 a[44:45], v232 offset:41472\n"
"v_accvgpr_read_b32 v211, a215\n"
"v_mfma_f32_16x16x32_bf16 v[4:7], a[20:23], a[188:191], v[4:7]\n"
"s_add_u32 s67, s64, 0\n"
"ds_read_b64_tr_b16 a[46:47], v232 offset:43520\n"
"v_cvt_pk_bf16_f32 v136, v204, v205\n"
"v_mfma_f32_16x16x32_bf16 v[4:7], a[16:19], a[180:183], v[4:7]\n"
"s_waitcnt lgkmcnt(8)\n"
"ds_read_b64_tr_b16 a[0:1], v229 offset:36864\n"
"ds_read_b64_tr_b16 a[2:3], v229 offset:38912\n"
"v_mfma_f32_16x16x32_bf16 v[8:11], a[24:27], a[176:179], v[8:11]\n"
"buffer_load_dwordx4 a[144:147], v224, s[48:51], s13 offen offset:0 sc1\n"
"v_mfma_f32_16x16x32_bf16 v[8:11], a[28:31], a[184:187], v[8:11]\n"
"ds_read_b64_tr_b16 a[4:5], v229 offset:45056\n"
"v_cvt_pk_bf16_f32 v137, v206, v207\n"
"v_mfma_f32_16x16x32_bf16 v[12:15], a[28:31], a[188:191], v[12:15]\n"
"ds_read_b64_tr_b16 a[6:7], v229 offset:47104\n"
"v_cvt_pk_bf16_f32 v138, v208, v209\n"
"v_mfma_f32_16x16x32_bf16 v[12:15], a[24:27], a[180:183], v[12:15]\n"
"s_waitcnt lgkmcnt(8)\n"
"ds_read_b64_tr_b16 a[8:9], v232 offset:36864\n"
"ds_read_b64_tr_b16 a[10:11], v232 offset:38912\n"
"v_mfma_f32_16x16x32_bf16 v[16:19], a[32:35], a[176:179], v[16:19]\n"
"buffer_load_dwordx4 a[148:151], v224, s[48:51], s13 offen offset:1024 sc1\n"
"v_mfma_f32_16x16x32_bf16 v[16:19], a[36:39], a[184:187], v[16:19]\n"
"v_lshrrev_b32 v185, 9, v224\n"
"v_cvt_pk_bf16_f32 v139, v210, v211\n"
"v_mfma_f32_16x16x32_bf16 v[20:23], a[36:39], a[188:191], v[20:23]\n"
"ds_read_b64_tr_b16 a[12:13], v232 offset:45056\n"
"v_lshl_add_u32 v184, v184, 1, v185\n"
"v_mfma_f32_16x16x32_bf16 v[20:23], a[32:35], a[180:183], v[20:23]\n"
"s_waitcnt lgkmcnt(7)\n"
"s_and_b32 s70, s30, 7\n"
"ds_read_b64_tr_b16 a[14:15], v232 offset:47104\n"
"v_mfma_f32_16x16x32_bf16 v[24:27], a[40:43], a[176:179], v[24:27]\n"
"s_mul_i32 s70, s70, 7\n"
"buffer_load_dwordx4 v238, s[40:43], s67 offen offset:0 sc1 lds\n"
"v_mfma_f32_16x16x32_bf16 v[24:27], a[44:47], a[184:187], v[24:27]\n"
"ds_read_b64_tr_b16 a[16:17], v229 offset:37376\n"
"v_accvgpr_read_b32 v200, a216\n"
"v_add_u32 v184, s70, v184\n"
"v_mfma_f32_16x16x32_bf16 v[28:31], a[44:47], a[188:191], v[28:31]\n"
"s_add_u32 s13, s92, 22528\n"
"ds_read_b64_tr_b16 a[18:19], v229 offset:39424\n"
"v_lshrrev_b32 v185, 2, v184\n"
"v_mfma_f32_16x16x32_bf16 v[28:31], a[40:43], a[180:183], v[28:31]\n"
"s_waitcnt lgkmcnt(6)\n"
"v_accvgpr_read_b32 v201, a217\n"
"v_bfe_u32 v186, v184, 1, 1\n"
"v_and_b32 v184, 1, v184\n"
"v_mfma_f32_16x16x32_bf16 v[32:35], a[0:3], a[176:179], v[32:35]\n"
"ds_read_b64_tr_b16 a[20:21], v229 offset:45568\n"
"ds_read_b64_tr_b16 a[22:23], v229 offset:47616\n"
"v_lshl_add_u32 v184, v184, 1, v186\n"
"v_mfma_f32_16x16x32_bf16 v[32:35], a[4:7], a[184:187], v[32:35]\n"
"buffer_load_dwordx4 v238, s[40:43], s67 offen offset:1024 sc1 lds\n"
"v_accvgpr_read_b32 v202, a218\n"
"v_mfma_f32_16x16x32_bf16 v[36:39], a[4:7], a[188:191], v[36:39]\n"
"ds_read_b64_tr_b16 a[24:25], v232 offset:37376\n"
"v_lshlrev_b32 v184, 8, v184\n"
"v_lshl_add_u32 v184, v185, 11, v184\n"
"v_mfma_f32_16x16x32_bf16 v[36:39], a[0:3], a[180:183], v[36:39]\n"
"s_waitcnt lgkmcnt(5)\n"
"ds_read_b64_tr_b16 a[26:27], v232 offset:39424\n"
"v_accvgpr_read_b32 v203, a219\n"
"v_add_u32 v185, 256, v224\n"
"v_mfma_f32_16x16x32_bf16 v[40:43], a[8:11], a[176:179], v[40:43]\n"
"ds_read_b64_tr_b16 a[28:29], v232 offset:45568\n"
"ds_read_b64_tr_b16 a[30:31], v232 offset:47616\n"
"v_lshrrev_b32 v185, 10, v185\n"
"v_mfma_f32_16x16x32_bf16 v[40:43], a[12:15], a[184:187], v[40:43]\n"
"buffer_load_dwordx4 a[152:155], v224, s[48:51], s13 offen offset:0 sc1\n"
"v_accvgpr_read_b32 v204, a220\n"
"v_mfma_f32_16x16x32_bf16 v[44:47], a[12:15], a[188:191], v[44:47]\n"
"buffer_load_dwordx4 a[156:159], v224, s[48:51], s13 offen offset:1024 sc1\n"
"buffer_load_dwordx4 v238, s[40:43], s67 offen offset:2048 sc1 lds\n"
"v_mfma_f32_16x16x32_bf16 v[44:47], a[8:11], a[180:183], v[44:47]\n"
"s_waitcnt lgkmcnt(4)\n"
"ds_read_b64_tr_b16 a[32:33], v229 offset:49152\n"
"v_accvgpr_read_b32 v205, a221\n"
"v_lshlrev_b32 v185, 30, v185\n"
"v_mfma_f32_16x16x32_bf16 v[48:51], a[16:19], a[176:179], v[48:51]\n"
"s_lshl_b32 s70, s31, 5\n"
"ds_read_b64_tr_b16 a[34:35], v229 offset:51200\n"
"ds_read_b64_tr_b16 a[36:37], v229 offset:57344\n"
"ds_read_b64_tr_b16 a[38:39], v229 offset:59392\n"
"v_mfma_f32_16x16x32_bf16 v[48:51], a[20:23], a[184:187], v[48:51]\n"
"s_and_b32 s70, s70, 32\n"
"ds_read_b64_tr_b16 a[40:41], v232 offset:49152\n"
"v_accvgpr_read_b32 v206, a222\n"
"v_xor_b32 v186, s70, v228\n"
"v_mfma_f32_16x16x32_bf16 v[52:55], a[20:23], a[188:191], v[52:55]\n"
"s_lshl_b32 s70, s31, 8\n"
"s_and_b32 s70, s70, 512\n"
"s_add_u32 s70, s70, 98304\n"
"v_add_u32 v186, s70, v186\n"
"v_mfma_f32_16x16x32_bf16 v[52:55], a[16:19], a[180:183], v[52:55]\n"
"s_waitcnt lgkmcnt(5)\n"
"buffer_load_dwordx4 v238, s[40:43], s67 offen offset:3072 sc1 lds\n"
"v_accvgpr_read_b32 v207, a223\n"
"v_mfma_f32_16x16x32_bf16 v[56:59], a[24:27], a[176:179], v[56:59]\n"
"ds_read_b64_tr_b16 a[42:43], v232 offset:51200\n"
"ds_read_b64_tr_b16 a[44:45], v232 offset:57344\n"
"ds_read_b64_tr_b16 a[46:47], v232 offset:59392\n"
"v_mfma_f32_16x16x32_bf16 v[56:59], a[28:31], a[184:187], v[56:59]\n"
"v_cvt_pk_bf16_f32 v140, v200, v201\n"
"v_bfe_u32 v187, v224, 5, 3\n"
"v_cmp_eq_u32_e64 s[74:75], 1, v187\n"
"v_mfma_f32_16x16x32_bf16 v[60:63], a[28:31], a[188:191], v[60:63]\n"
"v_cmp_eq_u32_e64 s[76:77], 2, v187\n"
"v_cmp_eq_u32_e64 s[78:79], 3, v187\n"
"v_lshrrev_b32 v188, 8, v224\n"
"v_mfma_f32_16x16x32_bf16 v[60:63], a[24:27], a[180:183], v[60:63]\n"
"s_waitcnt lgkmcnt(4)\n"
"v_cvt_pk_bf16_f32 v141, v202, v203\n"
"v_lshlrev_b32 v188, 10, v188\n"
"v_bfe_u32 v190, v224, 4, 3\n"
"v_mfma_f32_16x16x32_bf16 v[64:67], a[32:35], a[176:179], v[64:67]\n"
"ds_read_b64_tr_b16 a[0:1], v229 offset:49664\n"
"ds_read_b64_tr_b16 a[2:3], v229 offset:51712\n"
"ds_read_b64_tr_b16 a[4:5], v229 offset:57856\n"
"ds_read_b64_tr_b16 a[6:7], v229 offset:59904\n"
"v_lshl_add_u32 v188, v190, 7, v188\n"
"v_bfe_u32 v190, v224, 7, 1\n"
"v_lshl_add_u32 v188, v190, 30, v188\n"
"v_lshrrev_b32 v190, 2, v224\n"
"s_lshl_b32 s70, s31, 8\n"
"v_add_u32 v190, s70, v190\n"
"v_mfma_f32_16x16x32_bf16 v[64:67], a[36:39], a[184:187], v[64:67]\n"
"s_movk_i32 s68, 16384\n"
"ds_read_b64_tr_b16 a[8:9], v232 offset:49664\n"
"v_cvt_pk_bf16_f32 v142, v204, v205\n"
"v_mfma_f32_16x16x32_bf16 v[68:71], a[36:39], a[188:191], v[68:71]\n"
"ds_read_b64_tr_b16 a[10:11], v232 offset:51712\n"
"v_cvt_pk_bf16_f32 v143, v206, v207\n"
"v_mfma_f32_16x16x32_bf16 v[68:71], a[32:35], a[180:183], v[68:71]\n"
"s_waitcnt lgkmcnt(6)\n"
"ds_read_b64_tr_b16 a[12:13], v232 offset:57856\n"
"v_accvgpr_read_b32 v208, a224\n"
"v_mfma_f32_16x16x32_bf16 v[72:75], a[40:43], a[176:179], v[72:75]\n"
"ds_read_b64_tr_b16 a[14:15], v232 offset:59904\n"
"ds_read_b64_tr_b16 a[16:17], v229 offset:53248\n"
"v_mfma_f32_16x16x32_bf16 v[72:75], a[44:47], a[184:187], v[72:75]\n"
"ds_read_b64_tr_b16 a[18:19], v229 offset:55296\n"
"v_accvgpr_read_b32 v209, a225\n"
"v_mfma_f32_16x16x32_bf16 v[76:79], a[44:47], a[188:191], v[76:79]\n"
"buffer_load_dwordx4 v[160:163], v224, s[48:51], s68 offen offset:0 sc1\n"
"v_mfma_f32_16x16x32_bf16 v[76:79], a[40:43], a[180:183], v[76:79]\n"
"s_waitcnt lgkmcnt(6)\n"
"ds_read_b64_tr_b16 a[20:21], v229 offset:61440\n"
"v_accvgpr_read_b32 v210, a226\n"
"v_mfma_f32_16x16x32_bf16 v[80:83], a[0:3], a[176:179], v[80:83]\n"
"ds_read_b64_tr_b16 a[22:23], v229 offset:63488\n"
"ds_read_b64_tr_b16 a[24:25], v232 offset:53248\n"
"v_mfma_f32_16x16x32_bf16 v[80:83], a[4:7], a[184:187], v[80:83]\n"
"ds_read_b64_tr_b16 a[26:27], v232 offset:55296\n"
"v_accvgpr_read_b32 v211, a227\n"
"v_mfma_f32_16x16x32_bf16 v[84:87], a[4:7], a[188:191], v[84:87]\n"
"ds_read_b64_tr_b16 a[28:29], v232 offset:61440\n"
"v_accvgpr_read_b32 v200, a228\n"
"v_mfma_f32_16x16x32_bf16 v[84:87], a[0:3], a[180:183], v[84:87]\n"
"s_waitcnt lgkmcnt(7)\n"
"ds_read_b64_tr_b16 a[30:31], v232 offset:63488\n"
"ds_read_b64_tr_b16 a[32:33], v229 offset:53760\n"
"v_mfma_f32_16x16x32_bf16 v[88:91], a[8:11], a[176:179], v[88:91]\n"
"ds_read_b64_tr_b16 a[34:35], v229 offset:55808\n"
"v_accvgpr_read_b32 v201, a229\n"
"v_mfma_f32_16x16x32_bf16 v[88:91], a[12:15], a[184:187], v[88:91]\n"
"buffer_load_dwordx4 v[164:167], v224, s[48:51], s68 offen offset:1024 sc1\n"
"v_mfma_f32_16x16x32_bf16 v[92:95], a[12:15], a[188:191], v[92:95]\n"
"ds_read_b64_tr_b16 a[36:37], v229 offset:61952\n"
"v_accvgpr_read_b32 v202, a230\n"
"v_mfma_f32_16x16x32_bf16 v[92:95], a[8:11], a[180:183], v[92:95]\n"
"s_waitcnt lgkmcnt(7)\n"
"ds_read_b64_tr_b16 a[38:39], v229 offset:64000\n"
"ds_read_b64_tr_b16 a[40:41], v232 offset:53760\n"
"v_mfma_f32_16x16x32_bf16 v[96:99], a[16:19], a[176:179], v[96:99]\n"
"s_waitcnt vmcnt(32)\n"
"s_barrier\n"
"v_accvgpr_read_b32 v203, a231\n"
"v_mfma_f32_16x16x32_bf16 v[96:99], a[20:23], a[184:187], v[96:99]\n"
"s_add_u32 m0, s65, 81920\n"
"ds_read_b64_tr_b16 a[42:43], v232 offset:55808\n"
"ds_read_b64_tr_b16 a[44:45], v232 offset:61952\n"
"v_mfma_f32_16x16x32_bf16 v[100:103], a[20:23], a[188:191], v[100:103]\n"
"s_add_u32 s67, s64, 16384\n"
"ds_read_b64_tr_b16 a[46:47], v232 offset:64000\n"
"v_cvt_pk_bf16_f32 v144, v208, v209\n"
"v_mfma_f32_16x16x32_bf16 v[100:103], a[16:19], a[180:183], v[100:103]\n"
"s_waitcnt lgkmcnt(8)\n"
"ds_read_b64_tr_b16 a[0:1], v230 offset:0\n"
"v_cvt_pk_bf16_f32 v145, v210, v211\n"
"v_mfma_f32_16x16x32_bf16 v[104:107], a[24:27], a[176:179], v[104:107]\n"
"ds_read_b64_tr_b16 a[2:3], v230 offset:2048\n"
"ds_read_b64_tr_b16 a[4:5], v230 offset:8192\n"
"v_mfma_f32_16x16x32_bf16 v[104:107], a[28:31], a[184:187], v[104:107]\n"
"buffer_load_dwordx4 v[168:171], v224, s[48:51], s68 offen offset:2048 sc1\n"
"v_mfma_f32_16x16x32_bf16 v[108:111], a[28:31], a[188:191], v[108:111]\n"
"buffer_load_dwordx4 v238, s[40:43], s67 offen offset:0 sc1 lds\n"
"v_mfma_f32_16x16x32_bf16 v[108:111], a[24:27], a[180:183], v[108:111]\n"
"s_waitcnt lgkmcnt(7)\n"
"ds_read_b64_tr_b16 a[6:7], v230 offset:10240\n"
"v_cvt_pk_bf16_f32 v146, v200, v201\n"
"v_mfma_f32_16x16x32_bf16 v[112:115], a[32:35], a[176:179], v[112:115]\n"
"ds_read_b64_tr_b16 a[8:9], v233 offset:0\n"
"v_cvt_pk_bf16_f32 v147, v202, v203\n"
"v_mfma_f32_16x16x32_bf16 v[112:115], a[36:39], a[184:187], v[112:115]\n"
"ds_read_b64_tr_b16 a[10:11], v233 offset:2048\n"
"ds_read_b64_tr_b16 a[12:13], v233 offset:8192\n"
"v_mfma_f32_16x16x32_bf16 v[116:119], a[36:39], a[188:191], v[116:119]\n"
"buffer_load_dwordx4 v238, s[40:43], s67 offen offset:1024 sc1 lds\n"
"v_mfma_f32_16x16x32_bf16 v[116:119], a[32:35], a[180:183], v[116:119]\n"
"s_waitcnt lgkmcnt(7)\n"
"ds_read_b64_tr_b16 a[14:15], v233 offset:10240\n"
"v_accvgpr_read_b32 v204, a232\n"
"v_mfma_f32_16x16x32_bf16 v[120:123], a[40:43], a[176:179], v[120:123]\n"
"ds_read_b64_tr_b16 a[16:17], v230 offset:512\n"
"v_accvgpr_read_b32 v205, a233\n"
"v_mfma_f32_16x16x32_bf16 v[120:123], a[44:47], a[184:187], v[120:123]\n"
"ds_read_b64_tr_b16 a[18:19], v230 offset:2560\n"
"ds_read_b64_tr_b16 a[20:21], v230 offset:8704\n"
"v_mfma_f32_16x16x32_bf16 v[124:127], a[44:47], a[188:191], v[124:127]\n"
"buffer_load_dwordx4 v[172:175], v224, s[48:51], s68 offen offset:3072 sc1\n"
"v_mfma_f32_16x16x32_bf16 v[124:127], a[40:43], a[180:183], v[124:127]\n"
"s_waitcnt vmcnt(23)\n"
"s_waitcnt lgkmcnt(7)\n"
"buffer_load_dwordx4 v238, s[40:43], s67 offen offset:2048 sc1 lds\n"
"v_mfma_f32_16x16x32_bf16 v[0:3], a[0:3], a[128:131], v[0:3]\n"
"s_add_u32 s13, s92, 24576\n"
"ds_read_b64_tr_b16 a[22:23], v230 offset:10752\n"
"v_accvgpr_read_b32 v206, a234\n"
"v_mfma_f32_16x16x32_bf16 v[0:3], a[4:7], a[136:139], v[0:3]\n"
"ds_read_b64_tr_b16 a[24:25], v233 offset:512\n"
"v_accvgpr_read_b32 v207, a235\n"
"v_mfma_f32_16x16x32_bf16 v[4:7], a[4:7], a[140:143], v[4:7]\n"
"ds_read_b64_tr_b16 a[26:27], v233 offset:2560\n"
"v_accvgpr_read_b32 v208, a236\n"
"v_mfma_f32_16x16x32_bf16 v[4:7], a[0:3], a[132:135], v[4:7]\n"
"s_waitcnt lgkmcnt(6)\n"
"buffer_load_dwordx4 v238, s[40:43], s67 offen offset:3072 sc1 lds\n"
"ds_read_b64_tr_b16 a[28:29], v233 offset:8704\n"
"v_mfma_f32_16x16x32_bf16 v[8:11], a[8:11], a[128:131], v[8:11]\n"
"IWB2_%=:\n"
"s_cbranch_execz IWB6_%=\n"
"s_add_u32 m0, s65, 98304\n"
"ds_read_b64_tr_b16 a[30:31], v233 offset:10752\n"
"v_accvgpr_read_b32 v209, a237\n"
"v_mfma_f32_16x16x32_bf16 v[8:11], a[12:15], a[136:139], v[8:11]\n"
"s_mov_b32 s67, s11\n"
"ds_read_b64_tr_b16 a[32:33], v230 offset:4096\n"
"ds_read_b64_tr_b16 a[34:35], v230 offset:6144\n"
"v_mfma_f32_16x16x32_bf16 v[12:15], a[12:15], a[140:143], v[12:15]\n"
"s_mov_b32 s60, s24\n"
"s_and_b32 s61, s25, 0xffff\n"
"v_accvgpr_read_b32 v210, a238\n"
"v_mfma_f32_16x16x32_bf16 v[12:15], a[8:11], a[132:135], v[12:15]\n"
"s_waitcnt lgkmcnt(6)\n"
"buffer_load_dwordx4 a[160:163], v224, s[48:51], s13 offen offset:0 sc1\n"
"ds_read_b64_tr_b16 a[36:37], v230 offset:12288\n"
"v_mfma_f32_16x16x32_bf16 v[16:19], a[16:19], a[128:131], v[16:19]\n"
"s_mov_b32 s62, 1605632\n"
"ds_read_b64_tr_b16 a[38:39], v230 offset:14336\n"
"v_accvgpr_read_b32 v211, a239\n"
"v_mfma_f32_16x16x32_bf16 v[16:19], a[20:23], a[136:139], v[16:19]\n"
"s_mov_b32 s63, 0x20000\n"
"ds_read_b64_tr_b16 a[40:41], v233 offset:4096\n"
"ds_read_b64_tr_b16 a[42:43], v233 offset:6144\n"
"v_mfma_f32_16x16x32_bf16 v[20:23], a[20:23], a[140:143], v[20:23]\n"
"s_mul_i32 s66, s5, 7168\n"
"buffer_load_dwordx4 a[164:167], v224, s[48:51], s13 offen offset:1024 sc1\n"
"v_mfma_f32_16x16x32_bf16 v[20:23], a[16:19], a[132:135], v[20:23]\n"
"s_waitcnt lgkmcnt(6)\n"
"ds_read_b64_tr_b16 a[44:45], v233 offset:12288\n"
"v_cvt_pk_bf16_f32 v148, v204, v205\n"
"v_mfma_f32_16x16x32_bf16 v[24:27], a[24:27], a[128:131], v[24:27]\n"
"ds_read_b64_tr_b16 a[46:47], v233 offset:14336\n"
"buffer_load_dwordx4 v176, s[56:59], s67 offen offset:0 nt lds\n"
"v_mfma_f32_16x16x32_bf16 v[24:27], a[28:31], a[136:139], v[24:27]\n"
"s_add_u32 s13, s92, 26624\n"
"ds_read_b64_tr_b16 a[0:1], v230 offset:4608\n"
"v_cvt_pk_bf16_f32 v149, v206, v207\n"
"v_mfma_f32_16x16x32_bf16 v[28:31], a[28:31], a[140:143], v[28:31]\n"
"ds_read_b64_tr_b16 a[2:3], v230 offset:6656\n"
"v_cvt_pk_bf16_f32 v150, v208, v209\n"
"v_mfma_f32_16x16x32_bf16 v[28:31], a[24:27], a[132:135], v[28:31]\n"
"s_waitcnt lgkmcnt(6)\n"
"ds_read_b64_tr_b16 a[4:5], v230 offset:12800\n"
"ds_read_b64_tr_b16 a[6:7], v230 offset:14848\n"
"v_mfma_f32_16x16x32_bf16 v[32:35], a[32:35], a[128:131], v[32:35]\n"
"buffer_load_dword v248, v190, s[60:63], s66 offen offset:0 nt\n"
"v_mfma_f32_16x16x32_bf16 v[32:35], a[36:39], a[136:139], v[32:35]\n"
"buffer_load_dwordx4 v177, s[56:59], s67 offen offset:1024 nt lds\n"
"v_cvt_pk_bf16_f32 v151, v210, v211\n"
"v_mfma_f32_16x16x32_bf16 v[36:39], a[36:39], a[140:143], v[36:39]\n"
"ds_read_b64_tr_b16 a[8:9], v233 offset:4608\n"
"ds_read_b64_tr_b16 a[10:11], v233 offset:6656\n"
"v_mfma_f32_16x16x32_bf16 v[36:39], a[32:35], a[132:135], v[36:39]\n"
"s_waitcnt lgkmcnt(6)\n"
"buffer_load_dwordx4 a[168:171], v224, s[48:51], s13 offen offset:0 sc1\n"
"v_accvgpr_read_b32 v200, a240\n"
"v_mfma_f32_16x16x32_bf16 v[40:43], a[40:43], a[128:131], v[40:43]\n"
"ds_read_b64_tr_b16 a[12:13], v233 offset:12800\n"
"buffer_load_dword v249, v190, s[60:63], s66 offen offset:896 nt\n"
"ds_read_b64_tr_b16 a[14:15], v233 offset:14848\n"
"v_mfma_f32_16x16x32_bf16 v[40:43], a[44:47], a[136:139], v[40:43]\n"
"ds_read_b64_tr_b16 a[16:17], v230 offset:16384\n"
"v_accvgpr_read_b32 v201, a241\n"
"v_mfma_f32_16x16x32_bf16 v[44:47], a[44:47], a[140:143], v[44:47]\n"
"buffer_load_dwordx4 a[172:175], v224, s[48:51], s13 offen offset:1024 sc1\n"
"buffer_load_dwordx4 v178, s[56:59], s67 offen offset:2048 nt lds\n"
"v_mfma_f32_16x16x32_bf16 v[44:47], a[40:43], a[132:135], v[44:47]\n"
"s_waitcnt lgkmcnt(5)\n"
"buffer_load_dword v250, v190, s[60:63], s66 offen offset:1792 nt\n"
"v_accvgpr_read_b32 v202, a242\n"
"v_mfma_f32_16x16x32_bf16 v[48:51], a[0:3], a[128:131], v[48:51]\n"
"ds_read_b64_tr_b16 a[18:19], v230 offset:18432\n"
"ds_read_b64_tr_b16 a[20:21], v230 offset:24576\n"
"ds_read_b64_tr_b16 a[22:23], v230 offset:26624\n"
"v_mfma_f32_16x16x32_bf16 v[48:51], a[4:7], a[136:139], v[48:51]\n"
"ds_read_b64_tr_b16 a[24:25], v233 offset:16384\n"
"v_accvgpr_read_b32 v203, a243\n"
"v_mfma_f32_16x16x32_bf16 v[52:55], a[4:7], a[140:143], v[52:55]\n"
"buffer_load_dword v251, v190, s[60:63], s66 offen offset:2688 nt\n"
"ds_read_b64_tr_b16 a[26:27], v233 offset:18432\n"
"v_mfma_f32_16x16x32_bf16 v[52:55], a[0:3], a[132:135], v[52:55]\n"
"s_waitcnt lgkmcnt(6)\n"
"buffer_load_dwordx4 v179, s[56:59], s67 offen offset:3072 nt lds\n"
"v_accvgpr_read_b32 v204, a244\n"
"v_mfma_f32_16x16x32_bf16 v[56:59], a[8:11], a[128:131], v[56:59]\n"
"s_add_u32 s66, s66, 3584\n"
"ds_read_b64_tr_b16 a[28:29], v233 offset:24576\n"
"ds_read_b64_tr_b16 a[30:31], v233 offset:26624\n"
"v_mfma_f32_16x16x32_bf16 v[56:59], a[12:15], a[136:139], v[56:59]\n"
"ds_read_b64_tr_b16 a[32:33], v230 offset:16896\n"
"v_accvgpr_read_b32 v205, a245\n"
"v_mfma_f32_16x16x32_bf16 v[60:63], a[12:15], a[140:143], v[60:63]\n"
"ds_read_b64_tr_b16 a[34:35], v230 offset:18944\n"
"v_accvgpr_read_b32 v206, a246\n"
"v_mfma_f32_16x16x32_bf16 v[60:63], a[8:11], a[132:135], v[60:63]\n"
"s_waitcnt lgkmcnt(6)\n"
"buffer_load_dword v252, v190, s[60:63], s66 offen offset:0 nt\n"
"ds_read_b64_tr_b16 a[36:37], v230 offset:25088\n"
"v_mfma_f32_16x16x32_bf16 v[64:67], a[16:19], a[128:131], v[64:67]\n"
"ds_read_b64_tr_b16 a[38:39], v230 offset:27136\n"
"v_accvgpr_read_b32 v207, a247\n"
"v_mfma_f32_16x16x32_bf16 v[64:67], a[20:23], a[136:139], v[64:67]\n"
"ds_read_b64_tr_b16 a[40:41], v233 offset:16896\n"
"v_cvt_pk_bf16_f32 v152, v200, v201\n"
"v_mfma_f32_16x16x32_bf16 v[68:71], a[20:23], a[140:143], v[68:71]\n"
"buffer_load_dword v253, v190, s[60:63], s66 offen offset:896 nt\n"
"ds_read_b64_tr_b16 a[42:43], v233 offset:18944\n"
"v_mfma_f32_16x16x32_bf16 v[68:71], a[16:19], a[132:135], v[68:71]\n"
"s_waitcnt lgkmcnt(6)\n"
"ds_read_b64_tr_b16 a[44:45], v233 offset:25088\n"
"v_cvt_pk_bf16_f32 v153, v202, v203\n"
"v_mfma_f32_16x16x32_bf16 v[72:75], a[24:27], a[128:131], v[72:75]\n"
"ds_read_b64_tr_b16 a[46:47], v233 offset:27136\n"
"ds_read_b64_tr_b16 a[0:1], v230 offset:20480\n"
"v_mfma_f32_16x16x32_bf16 v[72:75], a[28:31], a[136:139], v[72:75]\n"
"buffer_load_dword v254, v190, s[60:63], s66 offen offset:1792 nt\n"
"v_mfma_f32_16x16x32_bf16 v[76:79], a[28:31], a[140:143], v[76:79]\n"
"ds_read_b64_tr_b16 a[2:3], v230 offset:22528\n"
"v_cvt_pk_bf16_f32 v154, v204, v205\n"
"v_mfma_f32_16x16x32_bf16 v[76:79], a[24:27], a[132:135], v[76:79]\n"
"s_waitcnt lgkmcnt(6)\n"
"ds_read_b64_tr_b16 a[4:5], v230 offset:28672\n"
"v_cvt_pk_bf16_f32 v155, v206, v207\n"
"v_mfma_f32_16x16x32_bf16 v[80:83], a[32:35], a[128:131], v[80:83]\n"
"ds_read_b64_tr_b16 a[6:7], v230 offset:30720\n"
"buffer_load_dword v255, v190, s[60:63], s66 offen offset:2688 nt\n"
"v_mfma_f32_16x16x32_bf16 v[80:83], a[36:39], a[136:139], v[80:83]\n"
"ds_read_b64_tr_b16 a[8:9], v233 offset:20480\n"
"v_accvgpr_read_b32 v208, a248\n"
"v_mfma_f32_16x16x32_bf16 v[84:87], a[36:39], a[140:143], v[84:87]\n"
"ds_read_b64_tr_b16 a[10:11], v233 offset:22528\n"
"ds_read_b64_tr_b16 a[12:13], v233 offset:28672\n"
"v_mfma_f32_16x16x32_bf16 v[84:87], a[32:35], a[132:135], v[84:87]\n"
"s_waitcnt lgkmcnt(7)\n"
"ds_read_b64_tr_b16 a[14:15], v233 offset:30720\n"
"v_accvgpr_read_b32 v209, a249\n"
"v_mfma_f32_16x16x32_bf16 v[88:91], a[40:43], a[128:131], v[88:91]\n"
"ds_read_b64_tr_b16 a[16:17], v230 offset:20992\n"
"v_accvgpr_read_b32 v210, a250\n"
"v_mfma_f32_16x16x32_bf16 v[88:91], a[44:47], a[136:139], v[88:91]\n"
"ds_read_b64_tr_b16 a[18:19], v230 offset:23040\n"
"ds_read_b64_tr_b16 a[20:21], v230 offset:29184\n"
"v_mfma_f32_16x16x32_bf16 v[92:95], a[44:47], a[140:143], v[92:95]\n"
"ds_read_b64_tr_b16 a[22:23], v230 offset:31232\n"
"v_accvgpr_read_b32 v211, a251\n"
"v_mfma_f32_16x16x32_bf16 v[92:95], a[40:43], a[132:135], v[92:95]\n"
"s_waitcnt lgkmcnt(8)\n"
"ds_read_b64_tr_b16 a[24:25], v233 offset:20992\n"
"v_accvgpr_read_b32 v200, a252\n"
"v_mfma_f32_16x16x32_bf16 v[96:99], a[0:3], a[128:131], v[96:99]\n"
"s_waitcnt vmcnt(40)\n"
"s_barrier\n"
"ds_read_b64_tr_b16 a[26:27], v233 offset:23040\n"
"v_mfma_f32_16x16x32_bf16 v[96:99], a[4:7], a[136:139], v[96:99]\n"
"s_add_u32 m0, s65, 114688\n"
"s_add_u32 s67, s64, 32768\n"
"ds_read_b64_tr_b16 a[28:29], v233 offset:29184\n"
"v_mfma_f32_16x16x32_bf16 v[100:103], a[4:7], a[140:143], v[100:103]\n"
"ds_read_b64_tr_b16 a[30:31], v233 offset:31232\n"
"v_accvgpr_read_b32 v201, a253\n"
"v_mfma_f32_16x16x32_bf16 v[100:103], a[0:3], a[132:135], v[100:103]\n"
"s_waitcnt lgkmcnt(8)\n"
"ds_read_b64_tr_b16 a[32:33], v228 offset:0\n"
"v_accvgpr_read_b32 v202, a254\n"
"v_mfma_f32_16x16x32_bf16 v[104:107], a[8:11], a[128:131], v[104:107]\n"
"ds_read_b64_tr_b16 a[34:35], v228 offset:2048\n"
"ds_read_b64_tr_b16 a[36:37], v228 offset:8192\n"
"v_mfma_f32_16x16x32_bf16 v[104:107], a[12:15], a[136:139], v[104:107]\n"
"buffer_load_dwordx4 v238, s[40:43], s67 offen offset:0 sc1 lds\n"
"v_mfma_f32_16x16x32_bf16 v[108:111], a[12:15], a[140:143], v[108:111]\n"
"ds_read_b64_tr_b16 a[38:39], v228 offset:10240\n"
"v_accvgpr_read_b32 v203, a255\n"
"v_mfma_f32_16x16x32_bf16 v[108:111], a[8:11], a[132:135], v[108:111]\n"
"s_waitcnt lgkmcnt(8)\n"
"ds_read_b64_tr_b16 a[40:41], v231 offset:0\n"
"v_cvt_pk_bf16_f32 v156, v208, v209\n"
"v_mfma_f32_16x16x32_bf16 v[112:115], a[16:19], a[128:131], v[112:115]\n"
"ds_read_b64_tr_b16 a[42:43], v231 offset:2048\n"
"ds_read_b64_tr_b16 a[44:45], v231 offset:8192\n"
"v_mfma_f32_16x16x32_bf16 v[112:115], a[20:23], a[136:139], v[112:115]\n"
"buffer_load_dwordx4 v238, s[40:43], s67 offen offset:1024 sc1 lds\n"
"v_mfma_f32_16x16x32_bf16 v[116:119], a[20:23], a[140:143], v[116:119]\n"
"ds_read_b64_tr_b16 a[46:47], v231 offset:10240\n"
"v_cvt_pk_bf16_f32 v157, v210, v211\n"
"v_mfma_f32_16x16x32_bf16 v[116:119], a[16:19], a[132:135], v[116:119]\n"
"s_waitcnt lgkmcnt(8)\n"
"ds_read_b64_tr_b16 a[0:1], v228 offset:512\n"
"v_cvt_pk_bf16_f32 v158, v200, v201\n"
"v_mfma_f32_16x16x32_bf16 v[120:123], a[24:27], a[128:131], v[120:123]\n"
"ds_read_b64_tr_b16 a[2:3], v228 offset:2560\n"
"ds_read_b64_tr_b16 a[4:5], v228 offset:8704\n"
"v_mfma_f32_16x16x32_bf16 v[120:123], a[28:31], a[136:139], v[120:123]\n"
"buffer_load_dwordx4 v238, s[40:43], s67 offen offset:2048 sc1 lds\n"
"v_mfma_f32_16x16x32_bf16 v[124:127], a[28:31], a[140:143], v[124:127]\n"
"ds_read_b64_tr_b16 a[6:7], v228 offset:10752\n"
"v_cvt_pk_bf16_f32 v159, v202, v203\n"
"v_mfma_f32_16x16x32_bf16 v[124:127], a[24:27], a[132:135], v[124:127]\n"
"s_waitcnt vmcnt(29)\n"
"s_waitcnt lgkmcnt(8)\n"
"ds_read_b64_tr_b16 a[8:9], v231 offset:512\n"
"v_mfma_f32_16x16x32_bf16 v[0:3], a[32:35], a[144:147], v[0:3]\n"
"s_waitcnt vmcnt(21)\n"
"ds_read_b64_tr_b16 a[10:11], v231 offset:2560\n"
"ds_read_b64_tr_b16 a[12:13], v231 offset:8704\n"
"v_mfma_f32_16x16x32_bf16 v[0:3], a[36:39], a[152:155], v[0:3]\n"
"buffer_load_dwordx4 v238, s[40:43], s67 offen offset:3072 sc1 lds\n"
"v_mfma_f32_16x16x32_bf16 v[4:7], a[36:39], a[156:159], v[4:7]\n"
"s_add_u32 m0, s65, 131072\n"
"ds_read_b64_tr_b16 a[14:15], v231 offset:10752\n"
"ds_read_b64_tr_b16 a[16:17], v228 offset:4096\n"
"v_mfma_f32_16x16x32_bf16 v[4:7], a[32:35], a[148:151], v[4:7]\n"
"s_waitcnt lgkmcnt(9)\n"
"ds_read_b64_tr_b16 a[18:19], v228 offset:6144\n"
"ds_read_b64_tr_b16 a[20:21], v228 offset:12288\n"
"v_mfma_f32_16x16x32_bf16 v[8:11], a[40:43], a[144:147], v[8:11]\n"
"s_add_u32 s67, s64, 49152\n"
"ds_read_b64_tr_b16 a[22:23], v228 offset:14336\n"
"ds_read_b64_tr_b16 a[24:25], v231 offset:4096\n"
"v_mfma_f32_16x16x32_bf16 v[8:11], a[44:47], a[152:155], v[8:11]\n"
"ds_read_b64_tr_b16 a[26:27], v231 offset:6144\n"
"ds_read_b64_tr_b16 a[28:29], v231 offset:12288\n"
"v_mfma_f32_16x16x32_bf16 v[12:15], a[44:47], a[156:159], v[12:15]\n"
"ds_read_b64_tr_b16 a[30:31], v231 offset:14336\n"
"v_cmp_eq_u32_e64 s[84:85], s86, v161\n"
"v_mfma_f32_16x16x32_bf16 v[12:15], a[40:43], a[148:151], v[12:15]\n"
"s_waitcnt lgkmcnt(12)\n"
"buffer_load_dwordx4 v238, s[40:43], s67 offen offset:0 sc1 lds\n"
"v_mfma_f32_16x16x32_bf16 v[16:19], a[0:3], a[144:147], v[16:19]\n"
"ds_read_b64_tr_b16 a[32:33], v228 offset:4608\n"
"v_cmp_eq_u32_e64 s[82:83], s87, v163\n"
"v_mfma_f32_16x16x32_bf16 v[16:19], a[4:7], a[152:155], v[16:19]\n"
"s_and_b64 s[84:85], s[84:85], s[82:83]\n"
"ds_read_b64_tr_b16 a[34:35], v228 offset:6656\n"
"v_cmp_eq_u32_e64 s[82:83], s86, v165\n"
"v_mfma_f32_16x16x32_bf16 v[20:23], a[4:7], a[156:159], v[20:23]\n"
"s_and_b64 s[84:85], s[84:85], s[82:83]\n"
"ds_read_b64_tr_b16 a[36:37], v228 offset:12800\n"
"ds_read_b64_tr_b16 a[38:39], v228 offset:14848\n"
"v_mfma_f32_16x16x32_bf16 v[20:23], a[0:3], a[148:151], v[20:23]\n"
"s_waitcnt lgkmcnt(12)\n"
"ds_read_b64_tr_b16 a[40:41], v231 offset:4608\n"
"v_cmp_eq_u32_e64 s[82:83], s87, v167\n"
"v_mfma_f32_16x16x32_bf16 v[24:27], a[8:11], a[144:147], v[24:27]\n"
"s_and_b64 s[84:85], s[84:85], s[82:83]\n"
"buffer_load_dwordx4 v238, s[40:43], s67 offen offset:1024 sc1 lds\n"
"v_mfma_f32_16x16x32_bf16 v[24:27], a[12:15], a[152:155], v[24:27]\n"
"ds_read_b64_tr_b16 a[42:43], v231 offset:6656\n"
"v_cmp_eq_u32_e64 s[82:83], s86, v169\n"
"v_mfma_f32_16x16x32_bf16 v[28:31], a[12:15], a[156:159], v[28:31]\n"
"s_and_b64 s[84:85], s[84:85], s[82:83]\n"
"ds_read_b64_tr_b16 a[44:45], v231 offset:12800\n"
"ds_read_b64_tr_b16 a[46:47], v231 offset:14848\n"
"v_mfma_f32_16x16x32_bf16 v[28:31], a[8:11], a[148:151], v[28:31]\n"
"s_waitcnt lgkmcnt(12)\n"
"ds_read_b64_tr_b16 a[0:1], v228 offset:16384\n"
"v_cmp_eq_u32_e64 s[82:83], s87, v171\n"
"v_mfma_f32_16x16x32_bf16 v[32:35], a[16:19], a[144:147], v[32:35]\n"
"s_and_b64 s[84:85], s[84:85], s[82:83]\n"
"ds_read_b64_tr_b16 a[2:3], v228 offset:18432\n"
"v_cmp_eq_u32_e64 s[82:83], s86, v173\n"
"v_mfma_f32_16x16x32_bf16 v[32:35], a[20:23], a[152:155], v[32:35]\n"
"s_and_b64 s[84:85], s[84:85], s[82:83]\n"
"buffer_load_dwordx4 v238, s[40:43], s67 offen offset:2048 sc1 lds\n"
"v_mfma_f32_16x16x32_bf16 v[36:39], a[20:23], a[156:159], v[36:39]\n"
"ds_read_b64_tr_b16 a[4:5], v228 offset:24576\n"
"ds_read_b64_tr_b16 a[6:7], v228 offset:26624\n"
"v_mfma_f32_16x16x32_bf16 v[36:39], a[16:19], a[148:151], v[36:39]\n"
"s_waitcnt lgkmcnt(12)\n"
"ds_read_b64_tr_b16 a[8:9], v231 offset:16384\n"
"v_cmp_eq_u32_e64 s[82:83], s87, v175\n"
"v_mfma_f32_16x16x32_bf16 v[40:43], a[24:27], a[144:147], v[40:43]\n"
"s_and_b64 s[84:85], s[84:85], s[82:83]\n"
"ds_read_b64_tr_b16 a[10:11], v231 offset:18432\n"
"ds_read_b64_tr_b16 a[12:13], v231 offset:24576\n"
"v_mfma_f32_16x16x32_bf16 v[40:43], a[28:31], a[152:155], v[40:43]\n"
"s_cmp_eq_u64 s[84:85], -1\n"
"s_cbranch_scc0 SPA_%=\n"
"SBA_%=:\n"
"v_mov_b32 v180, s6\n"
"v_mfma_f32_16x16x32_bf16 v[44:47], a[28:31], a[156:159], v[44:47]\n"
"buffer_load_dwordx4 v238, s[40:43], s67 offen offset:3072 sc1 lds\n"
"v_mfma_f32_16x16x32_bf16 v[44:47], a[24:27], a[148:151], v[44:47]\n"
"s_waitcnt lgkmcnt(11)\n"
"ds_read_b64_tr_b16 a[14:15], v231 offset:26624\n"
"v_mov_b32 v181, s7\n"
"v_mfma_f32_16x16x32_bf16 v[48:51], a[32:35], a[144:147], v[48:51]\n"
"s_or_b32 s68, s30, s31\n"
"s_cselect_b32 s68, 1073741824, 0\n"
"ds_read_b64_tr_b16 a[16:17], v228 offset:16896\n"
"v_mfma_f32_16x16x32_bf16 v[48:51], a[36:39], a[152:155], v[48:51]\n"
"buffer_store_dwordx2 v[180:181], v241, s[48:51], s68 offen offset:0 sc1\n"
"v_mfma_f32_16x16x32_bf16 v[52:55], a[36:39], a[156:159], v[52:55]\n"
"ds_read_b64_tr_b16 a[18:19], v228 offset:18944\n"
"v_min_u32 v161, v160, v164\n"
"v_mfma_f32_16x16x32_bf16 v[52:55], a[32:35], a[148:151], v[52:55]\n"
"s_waitcnt lgkmcnt(10)\n"
"ds_read_b64_tr_b16 a[20:21], v228 offset:25088\n"
"v_max_u32 v160, v160, v164\n"
"v_mfma_f32_16x16x32_bf16 v[56:59], a[40:43], a[144:147], v[56:59]\n"
"ds_read_b64_tr_b16 a[22:23], v228 offset:27136\n"
"v_max3_u32 v162, v162, v166, v161\n"
"v_mfma_f32_16x16x32_bf16 v[56:59], a[44:47], a[152:155], v[56:59]\n"
"ds_read_b64_tr_b16 a[24:25], v231 offset:16896\n"
"v_min_u32 v161, v160, v168\n"
"v_mfma_f32_16x16x32_bf16 v[60:63], a[44:47], a[156:159], v[60:63]\n"
"ds_read_b64_tr_b16 a[26:27], v231 offset:18944\n"
"v_max_u32 v160, v160, v168\n"
"v_mfma_f32_16x16x32_bf16 v[60:63], a[40:43], a[148:151], v[60:63]\n"
"s_waitcnt lgkmcnt(10)\n"
"ds_read_b64_tr_b16 a[28:29], v231 offset:25088\n"
"v_max3_u32 v162, v162, v170, v161\n"
"v_mfma_f32_16x16x32_bf16 v[64:67], a[0:3], a[144:147], v[64:67]\n"
"ds_read_b64_tr_b16 a[30:31], v231 offset:27136\n"
"v_min_u32 v161, v160, v172\n"
"v_mfma_f32_16x16x32_bf16 v[64:67], a[4:7], a[152:155], v[64:67]\n"
"ds_read_b64_tr_b16 a[32:33], v228 offset:20480\n"
"v_max_u32 v160, v160, v172\n"
"v_mfma_f32_16x16x32_bf16 v[68:71], a[4:7], a[156:159], v[68:71]\n"
"ds_read_b64_tr_b16 a[34:35], v228 offset:22528\n"
"v_max3_u32 v162, v162, v174, v161\n"
"v_mfma_f32_16x16x32_bf16 v[68:71], a[0:3], a[148:151], v[68:71]\n"
"s_waitcnt lgkmcnt(10)\n"
"ds_read_b64_tr_b16 a[36:37], v228 offset:28672\n"
"v_min_u32_dpp v164, v160, v160 quad_perm:[1,0,3,2] row_mask:0xf bank_mask:0xf\n"
"v_mfma_f32_16x16x32_bf16 v[72:75], a[8:11], a[144:147], v[72:75]\n"
"ds_read_b64_tr_b16 a[38:39], v228 offset:30720\n"
"v_max_u32_dpp v165, v162, v162 quad_perm:[1,0,3,2] row_mask:0xf bank_mask:0xf\n"
"v_mfma_f32_16x16x32_bf16 v[72:75], a[12:15], a[152:155], v[72:75]\n"
"ds_read_b64_tr_b16 a[40:41], v231 offset:20480\n"
"v_max_u32_dpp v160, v160, v160 quad_perm:[1,0,3,2] row_mask:0xf bank_mask:0xf\n"
"v_mfma_f32_16x16x32_bf16 v[76:79], a[12:15], a[156:159], v[76:79]\n"
"ds_read_b64_tr_b16 a[42:43], v231 offset:22528\n"
"v_max_u32 v162, v164, v165\n"
"v_mfma_f32_16x16x32_bf16 v[76:79], a[8:11], a[148:151], v[76:79]\n"
"s_waitcnt lgkmcnt(10)\n"
"ds_read_b64_tr_b16 a[44:45], v231 offset:28672\n"
"v_min_u32_dpp v164, v160, v160 quad_perm:[2,3,0,1] row_mask:0xf bank_mask:0xf\n"
"v_mfma_f32_16x16x32_bf16 v[80:83], a[16:19], a[144:147], v[80:83]\n"
"ds_read_b64_tr_b16 a[46:47], v231 offset:30720\n"
"v_max_u32_dpp v165, v162, v162 quad_perm:[2,3,0,1] row_mask:0xf bank_mask:0xf\n"
"v_mfma_f32_16x16x32_bf16 v[80:83], a[20:23], a[152:155], v[80:83]\n"
"ds_read_b64_tr_b16 a[0:1], v228 offset:20992\n"
"v_max_u32_dpp v160, v160, v160 quad_perm:[2,3,0,1] row_mask:0xf bank_mask:0xf\n"
"v_mfma_f32_16x16x32_bf16 v[84:87], a[20:23], a[156:159], v[84:87]\n"
"ds_read_b64_tr_b16 a[2:3], v228 offset:23040\n"
"v_max_u32 v162, v164, v165\n"
"v_mfma_f32_16x16x32_bf16 v[84:87], a[16:19], a[148:151], v[84:87]\n"
"s_waitcnt lgkmcnt(10)\n"
"ds_read_b64_tr_b16 a[4:5], v228 offset:29184\n"
"v_min_u32_dpp v164, v160, v160 row_half_mirror row_mask:0xf bank_mask:0xf\n"
"v_mfma_f32_16x16x32_bf16 v[88:91], a[24:27], a[144:147], v[88:91]\n"
"ds_read_b64_tr_b16 a[6:7], v228 offset:31232\n"
"v_max_u32_dpp v165, v162, v162 row_half_mirror row_mask:0xf bank_mask:0xf\n"
"v_mfma_f32_16x16x32_bf16 v[88:91], a[28:31], a[152:155], v[88:91]\n"
"ds_read_b64_tr_b16 a[8:9], v231 offset:20992\n"
"v_max_u32_dpp v160, v160, v160 row_half_mirror row_mask:0xf bank_mask:0xf\n"
"v_mfma_f32_16x16x32_bf16 v[92:95], a[28:31], a[156:159], v[92:95]\n"
"ds_read_b64_tr_b16 a[10:11], v231 offset:23040\n"
"v_max_u32 v162, v164, v165\n"
"v_mfma_f32_16x16x32_bf16 v[92:95], a[24:27], a[148:151], v[92:95]\n"
"s_waitcnt lgkmcnt(10)\n"
"ds_read_b64_tr_b16 a[12:13], v231 offset:29184\n"
"v_min_u32_dpp v164, v160, v160 row_mirror row_mask:0xf bank_mask:0xf\n"
"v_mfma_f32_16x16x32_bf16 v[96:99], a[32:35], a[144:147], v[96:99]\n"
"s_barrier\n"
"v_max_u32_dpp v165, v162, v162 row_mirror row_mask:0xf bank_mask:0xf\n"
"v_mfma_f32_16x16x32_bf16 v[96:99], a[36:39], a[152:155], v[96:99]\n"
"s_add_u32 m0, s65, 147456\n"
"s_add_u32 s67, s64, 65536\n"
"ds_read_b64_tr_b16 a[14:15], v231 offset:31232\n"
"v_mfma_f32_16x16x32_bf16 v[100:103], a[36:39], a[156:159], v[100:103]\n"
"v_max_u32_dpp v160, v160, v160 row_mirror row_mask:0xf bank_mask:0xf\n"
"v_max_u32 v162, v164, v165\n"
"v_mfma_f32_16x16x32_bf16 v[100:103], a[32:35], a[148:151], v[100:103]\n"
"s_waitcnt lgkmcnt(8)\n"
"ds_read_b64_tr_b16 a[16:17], v228 offset:32768\n"
"v_mov_b32 v161, v160\n"
"v_mfma_f32_16x16x32_bf16 v[104:107], a[40:43], a[144:147], v[104:107]\n"
"ds_read_b64_tr_b16 a[18:19], v228 offset:34816\n"
"v_mov_b32 v163, v162\n"
"v_mfma_f32_16x16x32_bf16 v[104:107], a[44:47], a[152:155], v[104:107]\n"
"buffer_load_dwordx4 v238, s[40:43], s67 offen offset:0 sc1 lds\n"
"v_mfma_f32_16x16x32_bf16 v[108:111], a[44:47], a[156:159], v[108:111]\n"
"ds_read_b64_tr_b16 a[20:21], v228 offset:40960\n"
"ds_read_b64_tr_b16 a[22:23], v228 offset:43008\n"
"v_mfma_f32_16x16x32_bf16 v[108:111], a[40:43], a[148:151], v[108:111]\n"
"s_waitcnt lgkmcnt(8)\n"
"v_permlane16_swap_b32 v160, v161\n"
"v_mfma_f32_16x16x32_bf16 v[112:115], a[0:3], a[144:147], v[112:115]\n"
"v_permlane16_swap_b32 v162, v163\n"
"v_mfma_f32_16x16x32_bf16 v[112:115], a[4:7], a[152:155], v[112:115]\n"
"ds_read_b64_tr_b16 a[24:25], v231 offset:32768\n"
"ds_read_b64_tr_b16 a[26:27], v231 offset:34816\n"
"v_mfma_f32_16x16x32_bf16 v[116:119], a[4:7], a[156:159], v[116:119]\n"
"ds_read_b64_tr_b16 a[28:29], v231 offset:40960\n"
"v_min_u32 v164, v160, v161\n"
"v_mfma_f32_16x16x32_bf16 v[116:119], a[0:3], a[148:151], v[116:119]\n"
"s_waitcnt lgkmcnt(7)\n"
"buffer_load_dwordx4 v238, s[40:43], s67 offen offset:1024 sc1 lds\n"
"v_max_u32 v160, v160, v161\n"
"v_mfma_f32_16x16x32_bf16 v[120:123], a[8:11], a[144:147], v[120:123]\n"
"ds_read_b64_tr_b16 a[30:31], v231 offset:43008\n"
"v_max3_u32 v162, v162, v163, v164\n"
"v_mfma_f32_16x16x32_bf16 v[120:123], a[12:15], a[152:155], v[120:123]\n"
"ds_read_b64_tr_b16 a[32:33], v228 offset:33280\n"
"v_mov_b32 v161, v160\n"
"v_mfma_f32_16x16x32_bf16 v[124:127], a[12:15], a[156:159], v[124:127]\n"
"ds_read_b64_tr_b16 a[34:35], v228 offset:35328\n"
"v_mov_b32 v163, v162\n"
"v_mfma_f32_16x16x32_bf16 v[124:127], a[8:11], a[148:151], v[124:127]\n"
"s_waitcnt vmcnt(19)\n"
"s_waitcnt lgkmcnt(6)\n"
"v_permlane32_swap_b32 v160, v161\n"
"v_mfma_f32_16x16x32_bf16 v[0:3], a[16:19], a[160:163], v[0:3]\n"
"ds_read_b64_tr_b16 a[36:37], v228 offset:41472\n"
"ds_read_b64_tr_b16 a[38:39], v228 offset:43520\n"
"v_permlane32_swap_b32 v162, v163\n"
"v_mfma_f32_16x16x32_bf16 v[0:3], a[20:23], a[168:171], v[0:3]\n"
"buffer_load_dwordx4 v238, s[40:43], s67 offen offset:2048 sc1 lds\n"
"v_min_u32 v164, v160, v161\n"
"v_mfma_f32_16x16x32_bf16 v[4:7], a[20:23], a[172:175], v[4:7]\n"
"ds_read_b64_tr_b16 a[40:41], v231 offset:33280\n"
"ds_read_b64_tr_b16 a[42:43], v231 offset:35328\n"
"v_max_u32 v160, v160, v161\n"
"v_mfma_f32_16x16x32_bf16 v[4:7], a[16:19], a[164:167], v[4:7]\n"
"s_waitcnt lgkmcnt(6)\n"
"ds_read_b64_tr_b16 a[44:45], v231 offset:41472\n"
"v_max3_u32 v162, v162, v163, v164\n"
"v_mfma_f32_16x16x32_bf16 v[8:11], a[24:27], a[160:163], v[8:11]\n"
"ds_read_b64_tr_b16 a[46:47], v231 offset:43520\n"
"v_readfirstlane_b32 s88, v160\n"
"v_mfma_f32_16x16x32_bf16 v[8:11], a[28:31], a[168:171], v[8:11]\n"
"buffer_load_dwordx4 v238, s[40:43], s67 offen offset:3072 sc1 lds\n"
"v_readfirstlane_b32 s89, v162\n"
"v_mfma_f32_16x16x32_bf16 v[12:15], a[28:31], a[172:175], v[12:15]\n"
"s_andn2_b32 s90, 0x7ff, s88\n"
"ds_read_b64_tr_b16 a[0:1], v228 offset:36864\n"
"ds_read_b64_tr_b16 a[2:3], v228 offset:38912\n"
"v_mfma_f32_16x16x32_bf16 v[12:15], a[24:27], a[164:167], v[12:15]\n"
"s_waitcnt lgkmcnt(6)\n"
"s_add_u32 m0, s65, 0\n"
"s_add_u32 s67, s64, 81920\n"
"s_lshr_b32 s68, s90, 5\n"
"v_mfma_f32_16x16x32_bf16 v[16:19], a[32:35], a[160:163], v[16:19]\n"
"s_mul_i32 s68, s68, 28672\n"
"ds_read_b64_tr_b16 a[4:5], v228 offset:45056\n"
"ds_read_b64_tr_b16 a[6:7], v228 offset:47104\n"
"v_cvt_pk_bf16_f32 v0, v0, v1\n"
"v_mfma_f32_16x16x32_bf16 v[16:19], a[36:39], a[168:171], v[16:19]\n"
"s_bfe_u32 s71, s90, 0x10004\n"
"ds_read_b64_tr_b16 a[8:9], v231 offset:36864\n"
"v_cvt_pk_bf16_f32 v1, v2, v3\n"
"v_cvt_pk_bf16_f32 v4, v4, v5\n"
"v_mfma_f32_16x16x32_bf16 v[20:23], a[36:39], a[172:175], v[20:23]\n"
"s_lshl_b32 s71, s71, 10\n"
"s_add_u32 s68, s68, s71\n"
"ds_read_b64_tr_b16 a[10:11], v231 offset:38912\n"
"v_cvt_pk_bf16_f32 v5, v6, v7\n"
"v_mfma_f32_16x16x32_bf16 v[20:23], a[32:35], a[164:167], v[20:23]\n"
"s_waitcnt lgkmcnt(6)\n"
"s_and_b32 s71, s90, 15\n"
"ds_read_b64_tr_b16 a[12:13], v231 offset:45056\n"
"v_cvt_pk_bf16_f32 v2, v8, v9\n"
"v_mfma_f32_16x16x32_bf16 v[24:27], a[40:43], a[160:163], v[24:27]\n"
"s_lshl_b32 s71, s71, 4\n"
"ds_read_b64_tr_b16 a[14:15], v231 offset:47104\n"
"buffer_load_dwordx4 v238, s[40:43], s67 offen offset:0 sc1 lds\n"
"v_cvt_pk_bf16_f32 v3, v10, v11\n"
"v_cvt_pk_bf16_f32 v6, v12, v13\n"
"v_mfma_f32_16x16x32_bf16 v[24:27], a[44:47], a[168:171], v[24:27]\n"
"s_add_u32 s68, s68, s71\n"
"ds_read_b64_tr_b16 a[16:17], v228 offset:37376\n"
"v_cvt_pk_bf16_f32 v7, v14, v15\n"
"v_mfma_f32_16x16x32_bf16 v[28:31], a[44:47], a[172:175], v[28:31]\n"
"s_andn2_b32 s91, 0x7ff, s89\n"
"ds_read_b64_tr_b16 a[18:19], v228 offset:39424\n"
"v_permlane16_swap_b32 v0, v2\n"
"v_mfma_f32_16x16x32_bf16 v[28:31], a[40:43], a[164:167], v[28:31]\n"
"s_waitcnt lgkmcnt(6)\n"
"s_lshr_b32 s70, s91, 5\n"
"v_permlane16_swap_b32 v4, v6\n"
"v_permlane16_swap_b32 v1, v3\n"
"v_mfma_f32_16x16x32_bf16 v[32:35], a[0:3], a[160:163], v[32:35]\n"
"s_mul_i32 s70, s70, 28672\n"
"ds_read_b64_tr_b16 a[20:21], v228 offset:45568\n"
"ds_read_b64_tr_b16 a[22:23], v228 offset:47616\n"
"v_permlane16_swap_b32 v5, v7\n"
"v_mfma_f32_16x16x32_bf16 v[32:35], a[4:7], a[168:171], v[32:35]\n"
"s_bfe_u32 s71, s91, 0x10004\n"
"buffer_load_dwordx4 v238, s[40:43], s67 offen offset:1024 sc1 lds\n"
"v_cvt_pk_bf16_f32 v16, v16, v17\n"
"v_mfma_f32_16x16x32_bf16 v[36:39], a[4:7], a[172:175], v[36:39]\n"
"s_lshl_b32 s71, s71, 10\n"
"ds_read_b64_tr_b16 a[24:25], v231 offset:37376\n"
"v_cvt_pk_bf16_f32 v17, v18, v19\n"
"v_cvt_pk_bf16_f32 v20, v20, v21\n"
"v_mfma_f32_16x16x32_bf16 v[36:39], a[0:3], a[164:167], v[36:39]\n"
"s_waitcnt lgkmcnt(5)\n"
"s_add_u32 s70, s70, s71\n"
"ds_read_b64_tr_b16 a[26:27], v231 offset:39424\n"
"v_cvt_pk_bf16_f32 v21, v22, v23\n"
"v_mfma_f32_16x16x32_bf16 v[40:43], a[8:11], a[160:163], v[40:43]\n"
"s_and_b32 s71, s91, 15\n"
"ds_read_b64_tr_b16 a[28:29], v231 offset:45568\n"
"ds_read_b64_tr_b16 a[30:31], v231 offset:47616\n"
"v_cvt_pk_bf16_f32 v18, v24, v25\n"
"v_mfma_f32_16x16x32_bf16 v[40:43], a[12:15], a[168:171], v[40:43]\n"
"s_lshl_b32 s71, s71, 4\n"
"ds_read_b64_tr_b16 a[32:33], v228 offset:49152\n"
"v_cvt_pk_bf16_f32 v19, v26, v27\n"
"v_cvt_pk_bf16_f32 v22, v28, v29\n"
"v_mfma_f32_16x16x32_bf16 v[44:47], a[12:15], a[172:175], v[44:47]\n"
"s_add_u32 s70, s70, s71\n"
"buffer_load_dwordx4 v238, s[40:43], s67 offen offset:2048 sc1 lds\n"
"v_cvt_pk_bf16_f32 v23, v30, v31\n"
"v_mfma_f32_16x16x32_bf16 v[44:47], a[8:11], a[164:167], v[44:47]\n"
"s_waitcnt lgkmcnt(5)\n"
"s_mov_b32 s82, 0xaaaaaaaa\n"
"v_permlane16_swap_b32 v16, v18\n"
"v_mfma_f32_16x16x32_bf16 v[48:51], a[16:19], a[160:163], v[48:51]\n"
"s_mov_b32 s83, 0xaaaaaaaa\n"
"ds_read_b64_tr_b16 a[34:35], v228 offset:51200\n"
"ds_read_b64_tr_b16 a[36:37], v228 offset:57344\n"
"ds_read_b64_tr_b16 a[38:39], v228 offset:59392\n"
"v_permlane16_swap_b32 v20, v22\n"
"v_permlane16_swap_b32 v17, v19\n"
"v_mfma_f32_16x16x32_bf16 v[48:51], a[20:23], a[168:171], v[48:51]\n"
"v_permlane16_swap_b32 v21, v23\n"
"v_mov_b32 v164, s68\n"
"v_mfma_f32_16x16x32_bf16 v[52:55], a[20:23], a[172:175], v[52:55]\n"
"ds_read_b64_tr_b16 a[40:41], v231 offset:49152\n"
"v_cvt_pk_bf16_f32 v32, v32, v33\n"
"v_mov_b32 v165, s70\n"
"v_mfma_f32_16x16x32_bf16 v[52:55], a[16:19], a[164:167], v[52:55]\n"
"s_waitcnt lgkmcnt(5)\n"
"buffer_load_dwordx4 v238, s[40:43], s67 offen offset:3072 sc1 lds\n"
"v_cvt_pk_bf16_f32 v33, v34, v35\n"
"v_cvt_pk_bf16_f32 v36, v36, v37\n"
"v_cndmask_b32_e64 v164, v164, v165, s[82:83]\n"
"v_mfma_f32_16x16x32_bf16 v[56:59], a[24:27], a[160:163], v[56:59]\n"
"ds_read_b64_tr_b16 a[42:43], v231 offset:51200\n"
"ds_read_b64_tr_b16 a[44:45], v231 offset:57344\n"
"ds_read_b64_tr_b16 a[46:47], v231 offset:59392\n"
"v_cvt_pk_bf16_f32 v37, v38, v39\n"
"v_add_u32 v164, v164, v184\n"
"v_mfma_f32_16x16x32_bf16 v[56:59], a[28:31], a[168:171], v[56:59]\n"
"ds_read_b64_tr_b16 a[0:1], v228 offset:49664\n"
"v_add_u32 v165, v164, v185\n"
"v_cvt_pk_bf16_f32 v34, v40, v41\n"
"v_mfma_f32_16x16x32_bf16 v[60:63], a[28:31], a[172:175], v[60:63]\n"
"s_mov_b32 s68, 2097152\n"
"ds_read_b64_tr_b16 a[2:3], v228 offset:51712\n"
"v_cvt_pk_bf16_f32 v35, v42, v43\n"
"v_cvt_pk_bf16_f32 v38, v44, v45\n"
"v_mfma_f32_16x16x32_bf16 v[60:63], a[24:27], a[164:167], v[60:63]\n"
"s_waitcnt lgkmcnt(6)\n"
"buffer_load_dwordx4 a[56:59], v164, s[48:51], s68 offen offset:0 sc1\n"
"v_cvt_pk_bf16_f32 v39, v46, v47\n"
"v_mfma_f32_16x16x32_bf16 v[64:67], a[32:35], a[160:163], v[64:67]\n"
"s_add_u32 s44, s26, s97\n"
"s_addc_u32 s45, s27, 0\n"
"s_and_b32 s45, s45, 0xffff\n"
"s_mov_b32 s46, 458752\n"
"s_mov_b32 s47, 0x20000\n"
"s_and_b32 s97, s96, 3\n"
"s_lshl_b32 s97, s97, 17\n"
"s_lshr_b32 s98, s12, 2\n"
"s_add_u32 s97, s97, s98\n"
"ds_read_b64_tr_b16 a[4:5], v228 offset:57856\n"
"ds_read_b64_tr_b16 a[6:7], v228 offset:59904\n"
"buffer_load_dwordx4 a[60:63], v165, s[48:51], s68 offen offset:2048 sc1\n"
"v_permlane16_swap_b32 v32, v34\n"
"v_mfma_f32_16x16x32_bf16 v[64:67], a[36:39], a[168:171], v[64:67]\n"
"IWB3_%=:\n"
"s_cbranch_execz IWB7_%=\n"
"v_permlane16_swap_b32 v36, v38\n"
"v_permlane16_swap_b32 v33, v35\n"
"v_mfma_f32_16x16x32_bf16 v[68:71], a[36:39], a[172:175], v[68:71]\n"
"ds_read_b64_tr_b16 a[8:9], v231 offset:49664\n"
"v_permlane16_swap_b32 v37, v39\n"
"v_mfma_f32_16x16x32_bf16 v[68:71], a[32:35], a[164:167], v[68:71]\n"
"s_waitcnt lgkmcnt(5)\n"
"ds_read_b64_tr_b16 a[10:11], v231 offset:51712\n"
"ds_read_b64_tr_b16 a[12:13], v231 offset:57856\n"
"v_cvt_pk_bf16_f32 v48, v48, v49\n"
"v_mfma_f32_16x16x32_bf16 v[72:75], a[40:43], a[160:163], v[72:75]\n"
"ds_read_b64_tr_b16 a[14:15], v231 offset:59904\n"
"v_cvt_pk_bf16_f32 v49, v50, v51\n"
"v_cvt_pk_bf16_f32 v52, v52, v53\n"
"v_mfma_f32_16x16x32_bf16 v[72:75], a[44:47], a[168:171], v[72:75]\n"
"ds_read_b64_tr_b16 a[16:17], v228 offset:53248\n"
"v_cvt_pk_bf16_f32 v53, v54, v55\n"
"v_mfma_f32_16x16x32_bf16 v[76:79], a[44:47], a[172:175], v[76:79]\n"
"buffer_store_dwordx4 v[128:131], v224, s[44:47], s97 offen offset:0 sc1\n"
"v_cvt_pk_bf16_f32 v50, v56, v57\n"
"v_mfma_f32_16x16x32_bf16 v[76:79], a[40:43], a[164:167], v[76:79]\n"
"s_waitcnt lgkmcnt(5)\n"
"ds_read_b64_tr_b16 a[18:19], v228 offset:55296\n"
"v_cvt_pk_bf16_f32 v51, v58, v59\n"
"v_cvt_pk_bf16_f32 v54, v60, v61\n"
"v_mfma_f32_16x16x32_bf16 v[80:83], a[0:3], a[160:163], v[80:83]\n"
"ds_read_b64_tr_b16 a[20:21], v228 offset:61440\n"
"ds_read_b64_tr_b16 a[22:23], v228 offset:63488\n"
"v_cvt_pk_bf16_f32 v55, v62, v63\n"
"v_mfma_f32_16x16x32_bf16 v[80:83], a[4:7], a[168:171], v[80:83]\n"
"ds_read_b64_tr_b16 a[24:25], v231 offset:53248\n"
"v_permlane16_swap_b32 v48, v50\n"
"v_mfma_f32_16x16x32_bf16 v[84:87], a[4:7], a[172:175], v[84:87]\n"
"v_permlane16_swap_b32 v52, v54\n"
"v_permlane16_swap_b32 v49, v51\n"
"v_mfma_f32_16x16x32_bf16 v[84:87], a[0:3], a[164:167], v[84:87]\n"
"s_waitcnt lgkmcnt(5)\n"
"v_permlane16_swap_b32 v53, v55\n"
"v_cvt_pk_bf16_f32 v64, v64, v65\n"
"v_mfma_f32_16x16x32_bf16 v[88:91], a[8:11], a[160:163], v[88:91]\n"
"ds_read_b64_tr_b16 a[26:27], v231 offset:55296\n"
"ds_read_b64_tr_b16 a[28:29], v231 offset:61440\n"
"ds_read_b64_tr_b16 a[30:31], v231 offset:63488\n"
"v_cvt_pk_bf16_f32 v68, v68, v69\n"
"v_mfma_f32_16x16x32_bf16 v[88:91], a[12:15], a[168:171], v[88:91]\n"
"ds_read_b64_tr_b16 a[32:33], v228 offset:53760\n"
"v_cvt_pk_bf16_f32 v65, v66, v67\n"
"v_mfma_f32_16x16x32_bf16 v[92:95], a[12:15], a[172:175], v[92:95]\n"
"ds_read_b64_tr_b16 a[34:35], v228 offset:55808\n"
"v_cvt_pk_bf16_f32 v66, v72, v73\n"
"v_cvt_pk_bf16_f32 v69, v70, v71\n"
"v_mfma_f32_16x16x32_bf16 v[92:95], a[8:11], a[164:167], v[92:95]\n"
"s_waitcnt lgkmcnt(6)\n"
"ds_read_b64_tr_b16 a[36:37], v228 offset:61952\n"
"v_cvt_pk_bf16_f32 v70, v76, v77\n"
"v_mfma_f32_16x16x32_bf16 v[96:99], a[16:19], a[160:163], v[96:99]\n"
"s_barrier\n"
"ds_read_b64_tr_b16 a[38:39], v228 offset:64000\n"
"v_cvt_pk_bf16_f32 v67, v74, v75\n"
"v_mfma_f32_16x16x32_bf16 v[96:99], a[20:23], a[168:171], v[96:99]\n"
"v_permlane16_swap_b32 v64, v66\n"
"v_cvt_pk_bf16_f32 v71, v78, v79\n"
"v_mfma_f32_16x16x32_bf16 v[100:103], a[20:23], a[172:175], v[100:103]\n"
"ds_read_b64_tr_b16 a[40:41], v231 offset:53760\n"
"v_permlane16_swap_b32 v68, v70\n"
"v_mfma_f32_16x16x32_bf16 v[100:103], a[16:19], a[164:167], v[100:103]\n"
"s_waitcnt lgkmcnt(5)\n"
"ds_read_b64_tr_b16 a[42:43], v231 offset:55808\n"
"v_permlane16_swap_b32 v65, v67\n"
"v_mfma_f32_16x16x32_bf16 v[104:107], a[24:27], a[160:163], v[104:107]\n"
"ds_read_b64_tr_b16 a[44:45], v231 offset:61952\n"
"ds_read_b64_tr_b16 a[46:47], v231 offset:64000\n"
"v_permlane16_swap_b32 v69, v71\n"
"v_cvt_pk_bf16_f32 v80, v80, v81\n"
"v_mfma_f32_16x16x32_bf16 v[104:107], a[28:31], a[168:171], v[104:107]\n"
"ds_read_b128 a[0:3], v226 offset:0\n"
"v_cvt_pk_bf16_f32 v84, v84, v85\n"
"v_mfma_f32_16x16x32_bf16 v[108:111], a[28:31], a[172:175], v[108:111]\n"
"buffer_store_dwordx4 v[132:135], v224, s[44:47], s97 offen offset:1024 sc1\n"
"v_cvt_pk_bf16_f32 v81, v82, v83\n"
"v_mfma_f32_16x16x32_bf16 v[108:111], a[24:27], a[164:167], v[108:111]\n"
"s_waitcnt lgkmcnt(5)\n"
"ds_read_b128 a[4:7], v226 offset:512\n"
"v_cvt_pk_bf16_f32 v82, v88, v89\n"
"v_cvt_pk_bf16_f32 v85, v86, v87\n"
"v_mfma_f32_16x16x32_bf16 v[112:115], a[32:35], a[160:163], v[112:115]\n"
"ds_read_b128 a[8:11], v226 offset:4096\n"
"ds_read_b128 a[12:15], v226 offset:4608\n"
"v_cvt_pk_bf16_f32 v86, v92, v93\n"
"v_mfma_f32_16x16x32_bf16 v[112:115], a[36:39], a[168:171], v[112:115]\n"
"v_cvt_pk_bf16_f32 v83, v90, v91\n"
"v_cvt_pk_bf16_f32 v87, v94, v95\n"
"v_mfma_f32_16x16x32_bf16 v[116:119], a[36:39], a[172:175], v[116:119]\n"
"v_permlane16_swap_b32 v80, v82\n"
"v_mfma_f32_16x16x32_bf16 v[116:119], a[32:35], a[164:167], v[116:119]\n"
"s_waitcnt lgkmcnt(4)\n"
"v_permlane16_swap_b32 v84, v86\n"
"v_mfma_f32_16x16x32_bf16 v[120:123], a[40:43], a[160:163], v[120:123]\n"
"v_permlane16_swap_b32 v81, v83\n"
"v_mfma_f32_16x16x32_bf16 v[120:123], a[44:47], a[168:171], v[120:123]\n"
"v_permlane16_swap_b32 v85, v87\n"
"v_cvt_pk_bf16_f32 v96, v96, v97\n"
"v_mfma_f32_16x16x32_bf16 v[124:127], a[44:47], a[172:175], v[124:127]\n"
"ds_read_b128 a[16:19], v226 offset:2048\n"
"ds_read_b128 a[20:23], v226 offset:2560\n"
"v_cvt_pk_bf16_f32 v100, v100, v101\n"
"v_mfma_f32_16x16x32_bf16 v[124:127], a[40:43], a[164:167], v[124:127]\n"
"s_waitcnt lgkmcnt(2)\n"
"ds_read_b128 a[24:27], v226 offset:6144\n"
"v_cvt_pk_bf16_f32 v97, v98, v99\n"
"v_mfma_f32_16x16x32_bf16 v[192:195], a[0:3], v[0:3], 0\n"
"ds_read_b128 a[28:31], v226 offset:6656\n"
"v_cvt_pk_bf16_f32 v98, v104, v105\n"
"v_cvt_pk_bf16_f32 v101, v102, v103\n"
"v_mfma_f32_16x16x32_bf16 v[192:195], a[4:7], v[16:19], v[192:195]\n"
"v_cvt_pk_bf16_f32 v102, v108, v109\n"
"v_cvt_pk_bf16_f32 v99, v106, v107\n"
"v_mfma_f32_16x16x32_bf16 v[192:195], a[8:11], v[32:35], v[192:195]\n"
"v_permlane16_swap_b32 v96, v98\n"
"v_mfma_f32_16x16x32_bf16 v[192:195], a[12:15], v[48:51], v[192:195]\n"
"ds_read_b128 a[32:35], v226 offset:8192\n"
"v_cvt_pk_bf16_f32 v103, v110, v111\n"
"v_mfma_f32_16x16x32_bf16 v[196:199], a[12:15], v[52:55], 0\n"
"v_permlane16_swap_b32 v100, v102\n"
"v_mfma_f32_16x16x32_bf16 v[196:199], a[8:11], v[36:39], v[196:199]\n"
"v_permlane16_swap_b32 v97, v99\n"
"v_mfma_f32_16x16x32_bf16 v[196:199], a[4:7], v[20:23], v[196:199]\n"
"buffer_store_dwordx4 v[136:139], v224, s[44:47], s97 offen offset:2048 sc1\n"
"v_permlane16_swap_b32 v101, v103\n"
"v_cvt_pk_bf16_f32 v112, v112, v113\n"
"v_mfma_f32_16x16x32_bf16 v[196:199], a[0:3], v[4:7], v[196:199]\n"
"s_waitcnt lgkmcnt(1)\n"
"ds_read_b128 a[36:39], v226 offset:8704\n"
"ds_read_b128 a[40:43], v226 offset:12288\n"
"v_cvt_pk_bf16_f32 v116, v116, v117\n"
"v_mfma_f32_16x16x32_bf16 v[200:203], a[16:19], v[0:3], 0\n"
"ds_read_b128 a[44:47], v226 offset:12800\n"
"v_cvt_pk_bf16_f32 v113, v114, v115\n"
"v_mfma_f32_16x16x32_bf16 v[200:203], a[20:23], v[16:19], v[200:203]\n"
"v_cvt_pk_bf16_f32 v114, v120, v121\n"
"v_cvt_pk_bf16_f32 v117, v118, v119\n"
"v_mfma_f32_16x16x32_bf16 v[200:203], a[24:27], v[32:35], v[200:203]\n"
"v_cvt_pk_bf16_f32 v118, v124, v125\n"
"v_cvt_pk_bf16_f32 v115, v122, v123\n"
"v_mfma_f32_16x16x32_bf16 v[200:203], a[28:31], v[48:51], v[200:203]\n"
"ds_read_b128 a[0:3], v226 offset:10240\n"
"v_cvt_pk_bf16_f32 v119, v126, v127\n"
"v_mfma_f32_16x16x32_bf16 v[204:207], a[28:31], v[52:55], 0\n"
"v_permlane16_swap_b32 v112, v114\n"
"v_mfma_f32_16x16x32_bf16 v[204:207], a[24:27], v[36:39], v[204:207]\n"
"v_permlane16_swap_b32 v116, v118\n"
"v_mfma_f32_16x16x32_bf16 v[204:207], a[20:23], v[20:23], v[204:207]\n"
"v_permlane16_swap_b32 v113, v115\n"
"v_permlane16_swap_b32 v117, v119\n"
"v_mfma_f32_16x16x32_bf16 v[204:207], a[16:19], v[4:7], v[204:207]\n"
"s_waitcnt lgkmcnt(1)\n"
"ds_read_b128 a[4:7], v226 offset:10752\n"
"ds_read_b128 a[8:11], v226 offset:14336\n"
"v_mfma_f32_16x16x32_bf16 v[192:195], a[32:35], v[64:67], v[192:195]\n"
"ds_read_b128 a[12:15], v226 offset:14848\n"
"ds_read_b128 a[16:19], v226 offset:16384\n"
"v_mfma_f32_16x16x32_bf16 v[192:195], a[36:39], v[80:83], v[192:195]\n"
"buffer_store_dwordx4 v[140:143], v224, s[44:47], s97 offen offset:3072 sc1\n"
"v_mfma_f32_16x16x32_bf16 v[192:195], a[40:43], v[96:99], v[192:195]\n"
"ds_read_b128 a[20:23], v226 offset:16896\n"
"ds_read_b128 a[24:27], v226 offset:20480\n"
"v_mfma_f32_16x16x32_bf16 v[192:195], a[44:47], v[112:115], v[192:195]\n"
"ds_read_b128 a[28:31], v226 offset:20992\n"
"v_mfma_f32_16x16x32_bf16 v[196:199], a[44:47], v[116:119], v[196:199]\n"
"v_mfma_f32_16x16x32_bf16 v[196:199], a[40:43], v[100:103], v[196:199]\n"
"v_mfma_f32_16x16x32_bf16 v[196:199], a[36:39], v[84:87], v[196:199]\n"
"v_mfma_f32_16x16x32_bf16 v[196:199], a[32:35], v[68:71], v[196:199]\n"
"s_waitcnt lgkmcnt(4)\n"
"ds_read_b128 a[32:35], v226 offset:18432\n"
"ds_read_b128 a[36:39], v226 offset:18944\n"
"v_mfma_f32_16x16x32_bf16 v[200:203], a[0:3], v[64:67], v[200:203]\n"
"ds_read_b128 a[40:43], v226 offset:22528\n"
"ds_read_b128 a[44:47], v226 offset:23040\n"
"v_mfma_f32_16x16x32_bf16 v[200:203], a[4:7], v[80:83], v[200:203]\n"
"v_mfma_f32_16x16x32_bf16 v[200:203], a[8:11], v[96:99], v[200:203]\n"
"s_add_u32 s97, s97, 65536\n"
"v_mfma_f32_16x16x32_bf16 v[200:203], a[12:15], v[112:115], v[200:203]\n"
"v_mfma_f32_16x16x32_bf16 v[204:207], a[12:15], v[116:119], v[204:207]\n"
"v_mfma_f32_16x16x32_bf16 v[204:207], a[8:11], v[100:103], v[204:207]\n"
"v_mfma_f32_16x16x32_bf16 v[204:207], a[4:7], v[84:87], v[204:207]\n"
"v_mfma_f32_16x16x32_bf16 v[204:207], a[0:3], v[68:71], v[204:207]\n"
"s_waitcnt lgkmcnt(4)\n"
"v_max_f32 v244, v192, v193\n"
"v_max_f32 v245, v194, v195\n"
"v_max_f32 v246, v196, v197\n"
"v_mfma_f32_16x16x32_bf16 v[208:211], a[16:19], v[0:3], 0\n"
"v_max_f32 v247, v198, v199\n"
"v_max3_f32 v244, v244, v200, v201\n"
"v_max3_f32 v245, v245, v202, v203\n"
"v_mfma_f32_16x16x32_bf16 v[208:211], a[20:23], v[16:19], v[208:211]\n"
"v_max3_f32 v246, v246, v204, v205\n"
"v_max3_f32 v247, v247, v206, v207\n"
"v_max_f32 v244, v244, v245\n"
"v_mfma_f32_16x16x32_bf16 v[208:211], a[24:27], v[32:35], v[208:211]\n"
"v_max_f32 v246, v246, v247\n"
"v_mov_b32 v245, v244\n"
"v_mov_b32 v247, v246\n"
"v_mfma_f32_16x16x32_bf16 v[208:211], a[28:31], v[48:51], v[208:211]\n"
"v_permlane16_swap_b32 v244, v245\n"
"v_mfma_f32_16x16x32_bf16 v[212:215], a[28:31], v[52:55], 0\n"
"v_permlane16_swap_b32 v246, v247\n"
"v_max_f32 v244, v244, v245\n"
"v_mfma_f32_16x16x32_bf16 v[212:215], a[24:27], v[36:39], v[212:215]\n"
"ds_read_b128 a[0:3], v226 offset:24576\n"
"v_max_f32 v246, v246, v247\n"
"v_mov_b32 v245, v244\n"
"v_mfma_f32_16x16x32_bf16 v[212:215], a[20:23], v[20:23], v[212:215]\n"
"buffer_store_dwordx4 v[144:147], v224, s[44:47], s97 offen offset:0 sc1\n"
"v_mov_b32 v247, v246\n"
"v_mfma_f32_16x16x32_bf16 v[212:215], a[16:19], v[4:7], v[212:215]\n"
"s_waitcnt lgkmcnt(1)\n"
"ds_read_b128 a[4:7], v226 offset:25088\n"
"v_permlane32_swap_b32 v244, v245\n"
"v_mfma_f32_16x16x32_bf16 v[216:219], a[32:35], v[0:3], 0\n"
"ds_read_b128 a[8:11], v226 offset:28672\n"
"ds_read_b128 a[12:15], v226 offset:29184\n"
"v_mfma_f32_16x16x32_bf16 v[216:219], a[36:39], v[16:19], v[216:219]\n"
"v_permlane32_swap_b32 v246, v247\n"
"v_max_f32 v244, v244, v245\n"
"v_mfma_f32_16x16x32_bf16 v[216:219], a[40:43], v[32:35], v[216:219]\n"
"ds_read_b128 a[16:19], v226 offset:26624\n"
"v_max_f32 v246, v246, v247\n"
"v_mfma_f32_16x16x32_bf16 v[216:219], a[44:47], v[48:51], v[216:219]\n"
"v_sub_f32 v192, v192, v244\n"
"v_sub_f32 v193, v193, v244\n"
"v_sub_f32 v196, v196, v246\n"
"v_mfma_f32_16x16x32_bf16 v[220:223], a[44:47], v[52:55], 0\n"
"v_exp_f32 v192, v192\n"
"v_sub_f32 v197, v197, v246\n"
"v_mfma_f32_16x16x32_bf16 v[220:223], a[40:43], v[36:39], v[220:223]\n"
"v_exp_f32 v196, v196\n"
"v_sub_f32 v194, v194, v244\n"
"v_mfma_f32_16x16x32_bf16 v[220:223], a[36:39], v[20:23], v[220:223]\n"
"v_exp_f32 v193, v193\n"
"v_sub_f32 v198, v198, v246\n"
"v_mfma_f32_16x16x32_bf16 v[220:223], a[32:35], v[4:7], v[220:223]\n"
"s_waitcnt lgkmcnt(1)\n"
"ds_read_b128 a[20:23], v226 offset:27136\n"
"v_exp_f32 v197, v197\n"
"v_mfma_f32_16x16x32_bf16 v[208:211], a[0:3], v[64:67], v[208:211]\n"
"s_waitcnt vmcnt(16)\n"
"s_barrier\n"
"ds_read_b128 a[24:27], v226 offset:30720\n"
"ds_read_b128 a[28:31], v226 offset:31232\n"
"v_cvt_pk_bf16_f32 v8, v192, v193\n"
"v_mfma_f32_16x16x32_bf16 v[208:211], a[4:7], v[80:83], v[208:211]\n"
"v_sub_f32 v195, v195, v244\n"
"v_cvt_pk_bf16_f32 v12, v196, v197\n"
"v_sub_f32 v199, v199, v246\n"
"v_mfma_f32_16x16x32_bf16 v[208:211], a[8:11], v[96:99], v[208:211]\n"
"v_exp_f32 v194, v194\n"
"v_sub_f32 v200, v200, v244\n"
"v_mfma_f32_16x16x32_bf16 v[208:211], a[12:15], v[112:115], v[208:211]\n"
"v_exp_f32 v198, v198\n"
"v_sub_f32 v204, v204, v246\n"
"v_mfma_f32_16x16x32_bf16 v[212:215], a[12:15], v[116:119], v[212:215]\n"
"v_exp_f32 v195, v195\n"
"v_mfma_f32_16x16x32_bf16 v[212:215], a[8:11], v[100:103], v[212:215]\n"
"v_exp_f32 v199, v199\n"
"v_cvt_pk_bf16_f32 v9, v194, v195\n"
"v_mfma_f32_16x16x32_bf16 v[212:215], a[4:7], v[84:87], v[212:215]\n"
"buffer_store_dwordx4 v[148:151], v224, s[44:47], s97 offen offset:1024 sc1\n"
"v_sub_f32 v201, v201, v244\n"
"v_cvt_pk_bf16_f32 v13, v198, v199\n"
"v_mfma_f32_16x16x32_bf16 v[212:215], a[0:3], v[68:71], v[212:215]\n"
"s_waitcnt lgkmcnt(0)\n"
"v_exp_f32 v200, v200\n"
"v_sub_f32 v205, v205, v246\n"
"v_mfma_f32_16x16x32_bf16 v[216:219], a[16:19], v[64:67], v[216:219]\n"
"v_exp_f32 v204, v204\n"
"v_sub_f32 v202, v202, v244\n"
"v_mfma_f32_16x16x32_bf16 v[216:219], a[20:23], v[80:83], v[216:219]\n"
"v_exp_f32 v201, v201\n"
"v_sub_f32 v206, v206, v246\n"
"v_mfma_f32_16x16x32_bf16 v[216:219], a[24:27], v[96:99], v[216:219]\n"
"ds_read_b128 a[32:35], v226 offset:49152\n"
"ds_read_b128 a[36:39], v226 offset:49664\n"
"ds_read_b128 a[40:43], v226 offset:53248\n"
"ds_read_b128 a[44:47], v226 offset:53760\n"
"v_exp_f32 v205, v205\n"
"v_cvt_pk_bf16_f32 v10, v200, v201\n"
"v_mfma_f32_16x16x32_bf16 v[216:219], a[28:31], v[112:115], v[216:219]\n"
"v_sub_f32 v203, v203, v244\n"
"v_cvt_pk_bf16_f32 v14, v204, v205\n"
"v_sub_f32 v228, 0, v244\n"
"v_mfma_f32_16x16x32_bf16 v[220:223], a[28:31], v[116:119], v[220:223]\n"
"v_exp_f32 v202, v202\n"
"v_sub_f32 v207, v207, v246\n"
"v_mfma_f32_16x16x32_bf16 v[220:223], a[24:27], v[100:103], v[220:223]\n"
"v_exp_f32 v206, v206\n"
"v_exp_f32 v203, v203\n"
"v_mfma_f32_16x16x32_bf16 v[220:223], a[20:23], v[84:87], v[220:223]\n"
"v_exp_f32 v207, v207\n"
"v_cvt_pk_bf16_f32 v11, v202, v203\n"
"v_mfma_f32_16x16x32_bf16 v[220:223], a[16:19], v[68:71], v[220:223]\n"
"ds_read_b64_tr_b16 a[80:81], v186 offset:0\n"
"v_cvt_pk_bf16_f32 v15, v206, v207\n"
"v_sub_f32 v229, 0, v244\n"
"v_mfma_f32_16x16x32_bf16 a[112:115], v[8:11], a[120:123], 0\n"
"v_sub_f32 v230, 0, v244\n"
"v_sub_f32 v231, 0, v244\n"
"v_sub_f32 v232, 0, v246\n"
"v_sub_f32 v233, 0, v246\n"
"v_sub_f32 v234, 0, v246\n"
"v_sub_f32 v235, 0, v246\n"
"v_mfma_f32_16x16x32_bf16 a[116:119], v[12:15], a[120:123], 0\n"
"s_waitcnt lgkmcnt(1)\n"
"ds_read_b64_tr_b16 a[82:83], v186 offset:2048\n"
"ds_read_b128 a[0:3], v226 offset:51200\n"
"ds_read_b128 a[4:7], v226 offset:51712\n"
"v_mfma_f32_16x16x32_bf16 v[192:195], a[32:35], v[0:3], v[228:231]\n"
"ds_read_b128 a[8:11], v226 offset:55296\n"
"ds_read_b128 a[12:15], v226 offset:55808\n"
"v_mfma_f32_16x16x32_bf16 v[192:195], a[36:39], v[16:19], v[192:195]\n"
"ds_read_b64_tr_b16 a[84:85], v186 offset:4096\n"
"ds_read_b64_tr_b16 a[86:87], v186 offset:6144\n"
"v_mfma_f32_16x16x32_bf16 v[192:195], a[40:43], v[32:35], v[192:195]\n"
"buffer_store_dwordx4 v[152:155], v224, s[44:47], s97 offen offset:2048 sc1\n"
"v_mfma_f32_16x16x32_bf16 v[192:195], a[44:47], v[48:51], v[192:195]\n"
"ds_read_b64_tr_b16 a[88:89], v186 offset:8192\n"
"ds_read_b128 a[16:19], v226 offset:57344\n"
"v_mfma_f32_16x16x32_bf16 v[196:199], a[44:47], v[52:55], v[232:235]\n"
"ds_read_b64_tr_b16 a[90:91], v186 offset:10240\n"
"v_sub_f32 v208, v208, v244\n"
"v_mfma_f32_16x16x32_bf16 v[196:199], a[40:43], v[36:39], v[196:199]\n"
"ds_read_b128 a[20:23], v226 offset:57856\n"
"ds_read_b128 a[24:27], v226 offset:61440\n"
"v_mfma_f32_16x16x32_bf16 v[196:199], a[36:39], v[20:23], v[196:199]\n"
"ds_read_b64_tr_b16 a[92:93], v186 offset:12288\n"
"v_sub_f32 v209, v209, v244\n"
"v_mfma_f32_16x16x32_bf16 v[196:199], a[32:35], v[4:7], v[196:199]\n"
"s_waitcnt lgkmcnt(8)\n"
"ds_read_b128 a[28:31], v226 offset:61952\n"
"v_sub_f32 v212, v212, v246\n"
"v_mfma_f32_16x16x32_bf16 v[200:203], a[0:3], v[0:3], v[228:231]\n"
"v_exp_f32 v208, v208\n"
"v_mfma_f32_16x16x32_bf16 v[200:203], a[4:7], v[16:19], v[200:203]\n"
"ds_read_b64_tr_b16 a[94:95], v186 offset:14336\n"
"v_sub_f32 v210, v210, v244\n"
"v_mfma_f32_16x16x32_bf16 v[200:203], a[8:11], v[32:35], v[200:203]\n"
"ds_read_b128 a[32:35], v226 offset:59392\n"
"v_sub_f32 v213, v213, v246\n"
"v_mfma_f32_16x16x32_bf16 v[200:203], a[12:15], v[48:51], v[200:203]\n"
"v_exp_f32 v209, v209\n"
"v_mfma_f32_16x16x32_bf16 v[204:207], a[12:15], v[52:55], v[232:235]\n"
"v_exp_f32 v212, v212\n"
"v_mfma_f32_16x16x32_bf16 v[204:207], a[8:11], v[36:39], v[204:207]\n"
"v_cvt_pk_bf16_f32 v24, v208, v209\n"
"v_sub_f32 v214, v214, v246\n"
"v_mfma_f32_16x16x32_bf16 v[204:207], a[4:7], v[20:23], v[204:207]\n"
"ds_read_b128 a[36:39], v226 offset:59904\n"
"v_sub_f32 v211, v211, v244\n"
"v_mfma_f32_16x16x32_bf16 v[204:207], a[0:3], v[4:7], v[204:207]\n"
"s_waitcnt vmcnt(7) lgkmcnt(2)\n"
"v_exp_f32 v213, v213\n"
"v_mfma_f32_16x16x32_bf16 v[168:171], a[80:83], a[56:59], 0\n"
"ds_read_b128 a[40:43], v226 offset:63488\n"
"v_cvt_pk_bf16_f32 v28, v212, v213\n"
"v_mfma_f32_16x16x32_bf16 v[172:175], a[88:91], a[56:59], 0\n"
"v_exp_f32 v210, v210\n"
"v_mfma_f32_16x16x32_bf16 v[192:195], a[16:19], v[64:67], v[192:195]\n"
"buffer_store_dwordx4 v[156:159], v224, s[44:47], s97 offen offset:3072 sc1\n"
"v_mfma_f32_16x16x32_bf16 v[192:195], a[20:23], v[80:83], v[192:195]\n"
"ds_read_b128 a[44:47], v226 offset:64000\n"
"v_sub_f32 v215, v215, v246\n"
"v_mfma_f32_16x16x32_bf16 v[192:195], a[24:27], v[96:99], v[192:195]\n"
"ds_read_b128 a[0:3], v227 offset:0\n"
"v_sub_f32 v216, v216, v244\n"
"v_mfma_f32_16x16x32_bf16 v[192:195], a[28:31], v[112:115], v[192:195]\n"
"v_exp_f32 v214, v214\n"
"v_mfma_f32_16x16x32_bf16 v[196:199], a[28:31], v[116:119], v[196:199]\n"
"v_exp_f32 v211, v211\n"
"v_mfma_f32_16x16x32_bf16 v[196:199], a[24:27], v[100:103], v[196:199]\n"
"v_sub_f32 v220, v220, v246\n"
"v_cvt_pk_bf16_f32 v25, v210, v211\n"
"v_mfma_f32_16x16x32_bf16 v[196:199], a[20:23], v[84:87], v[196:199]\n"
"v_exp_f32 v215, v215\n"
"v_mfma_f32_16x16x32_bf16 v[196:199], a[16:19], v[68:71], v[196:199]\n"
"v_sub_f32 v217, v217, v244\n"
"v_cvt_pk_bf16_f32 v29, v214, v215\n"
"v_mfma_f32_16x16x32_bf16 v[168:171], a[84:87], a[60:63], v[168:171]\n"
"v_exp_f32 v216, v216\n"
"v_sub_f32 v221, v221, v246\n"
"v_mfma_f32_16x16x32_bf16 v[172:175], a[92:95], a[60:63], v[172:175]\n"
"s_waitcnt lgkmcnt(1)\n"
"v_exp_f32 v220, v220\n"
"v_mfma_f32_16x16x32_bf16 v[200:203], a[32:35], v[64:67], v[200:203]\n"
"ds_read_b128 a[4:7], v227 offset:512\n"
"v_sub_f32 v218, v218, v244\n"
"v_mfma_f32_16x16x32_bf16 v[200:203], a[36:39], v[80:83], v[200:203]\n"
"v_exp_f32 v217, v217\n"
"v_sub_f32 v222, v222, v246\n"
"v_mfma_f32_16x16x32_bf16 v[200:203], a[40:43], v[96:99], v[200:203]\n"
"ds_read_b128 a[8:11], v227 offset:4096\n"
"ds_read_b128 a[12:15], v227 offset:4608\n"
"v_cvt_pk_bf16_f32 v26, v216, v217\n"
"v_mfma_f32_16x16x32_bf16 v[200:203], a[44:47], v[112:115], v[200:203]\n"
"v_exp_f32 v221, v221\n"
"v_sub_f32 v219, v219, v244\n"
"v_mfma_f32_16x16x32_bf16 v[204:207], a[44:47], v[116:119], v[204:207]\n"
"v_cvt_pk_bf16_f32 v30, v220, v221\n"
"v_exp_f32 v218, v218\n"
"v_mfma_f32_16x16x32_bf16 v[204:207], a[40:43], v[100:103], v[204:207]\n"
"s_add_u32 m0, s65, 16384\n"
"s_add_u32 s67, s64, 98304\n"
"v_sub_f32 v223, v223, v246\n"
"v_mfma_f32_16x16x32_bf16 v[204:207], a[36:39], v[84:87], v[204:207]\n"
"v_exp_f32 v219, v219\n"
"s_nop 0\n"
"v_cvt_pk_bf16_f32 v27, v218, v219\n"
"v_cndmask_b32_e64 v168, v168, v169, s[74:75]\n"
"v_mfma_f32_16x16x32_bf16 v[204:207], a[32:35], v[68:71], v[204:207]\n"
"v_exp_f32 v222, v222\n"
"v_exp_f32 v223, v223\n"
"s_nop 0\n"
"v_cvt_pk_bf16_f32 v31, v222, v223\n"
"v_cndmask_b32_e64 v168, v168, v170, s[76:77]\n"
"v_mfma_f32_16x16x32_bf16 a[112:115], v[24:27], a[120:123], a[112:115]\n"
"ds_read_b128 a[16:19], v227 offset:2048\n"
"ds_read_b128 a[20:23], v227 offset:2560\n"
"v_cndmask_b32_e64 v168, v168, v171, s[78:79]\n"
"v_mfma_f32_16x16x32_bf16 a[116:119], v[28:31], a[120:123], a[116:119]\n"
"s_waitcnt lgkmcnt(2)\n"
"buffer_load_dwordx4 v238, s[40:43], s67 offen offset:0 sc1 lds\n"
"v_cndmask_b32_e64 v172, v172, v173, s[74:75]\n"
"v_mfma_f32_16x16x32_bf16 v[208:211], a[0:3], v[0:3], v[228:231]\n"
"ds_read_b128 a[24:27], v227 offset:6144\n"
"ds_read_b128 a[28:31], v227 offset:6656\n"
"v_cndmask_b32_e64 v172, v172, v174, s[76:77]\n"
"v_mfma_f32_16x16x32_bf16 v[208:211], a[4:7], v[16:19], v[208:211]\n"
"s_mul_i32 s97, s95, 1024\n"
"v_cndmask_b32_e64 v172, v172, v175, s[78:79]\n"
"v_mov_b32 v170, v172\n"
"v_mfma_f32_16x16x32_bf16 v[208:211], a[8:11], v[32:35], v[208:211]\n"
"s_add_u32 s97, s97, 5242880\n"
"v_mov_b32 v169, s86\n"
"v_mov_b32 v171, s87\n"
"v_mfma_f32_16x16x32_bf16 v[208:211], a[12:15], v[48:51], v[208:211]\n"
"s_lshr_b32 s68, s30, 3\n"
"buffer_load_dwordx4 v238, s[40:43], s67 offen offset:1024 sc1 lds\n"
"v_mfma_f32_16x16x32_bf16 v[212:215], a[12:15], v[52:55], v[232:235]\n"
"s_lshl_b32 s68, s68, 14\n"
"ds_read_b128 a[32:35], v227 offset:8192\n"
"ds_read_b128 a[36:39], v227 offset:8704\n"
"v_mfma_f32_16x16x32_bf16 v[212:215], a[8:11], v[36:39], v[212:215]\n"
"s_add_u32 s68, s68, s65\n"
"ds_read_b128 a[40:43], v227 offset:12288\n"
"ds_read_b128 a[44:47], v227 offset:12800\n"
"v_mfma_f32_16x16x32_bf16 v[212:215], a[4:7], v[20:23], v[212:215]\n"
"s_and_b32 s70, s30, 7\n"
"v_exp_f32 v192, v192\n"
"v_mfma_f32_16x16x32_bf16 v[212:215], a[0:3], v[4:7], v[212:215]\n"
"s_waitcnt lgkmcnt(4)\n"
"buffer_load_dwordx4 v238, s[40:43], s67 offen offset:2048 sc1 lds\n"
"v_mfma_f32_16x16x32_bf16 v[216:219], a[16:19], v[0:3], v[228:231]\n"
"s_lshl_b32 s70, s70, 4\n"
"s_add_u32 s68, s68, s70\n"
"s_add_u32 s68, s68, 524288\n"
"v_mfma_f32_16x16x32_bf16 v[216:219], a[20:23], v[16:19], v[216:219]\n"
"s_lshl_b32 s98, s3, 3\n"
"v_exp_f32 v193, v193\n"
"v_mfma_f32_16x16x32_bf16 v[216:219], a[24:27], v[32:35], v[216:219]\n"
"s_add_u32 s98, s98, s97\n"
"v_exp_f32 v196, v196\n"
"v_mfma_f32_16x16x32_bf16 v[216:219], a[28:31], v[48:51], v[216:219]\n"
"buffer_load_dwordx4 v238, s[40:43], s67 offen offset:3072 sc1 lds\n"
"v_mfma_f32_16x16x32_bf16 v[220:223], a[28:31], v[52:55], v[232:235]\n"
"s_add_u32 m0, s65, 32768\n"
"ds_read_b128 a[0:3], v227 offset:10240\n"
"v_cvt_pk_bf16_f32 v40, v192, v193\n"
"v_mfma_f32_16x16x32_bf16 v[220:223], a[24:27], v[36:39], v[220:223]\n"
"s_add_u32 s67, s64, 114688\n"
"buffer_store_dwordx4 v[168:171], v188, s[48:51], s68 offen offset:0 sc1\n"
"v_mfma_f32_16x16x32_bf16 v[220:223], a[20:23], v[20:23], v[220:223]\n"
"ds_read_b128 a[4:7], v227 offset:10752\n"
"v_exp_f32 v197, v197\n"
"v_mfma_f32_16x16x32_bf16 v[220:223], a[16:19], v[4:7], v[220:223]\n"
"s_waitcnt lgkmcnt(2)\n"
"ds_read_b128 a[8:11], v227 offset:14336\n"
"ds_read_b128 a[12:15], v227 offset:14848\n"
"v_mfma_f32_16x16x32_bf16 v[208:211], a[32:35], v[64:67], v[208:211]\n"
"s_barrier\n"
"v_exp_f32 v194, v194\n"
"v_cvt_pk_bf16_f32 v44, v196, v197\n"
"v_mfma_f32_16x16x32_bf16 v[208:211], a[36:39], v[80:83], v[208:211]\n"
"v_exp_f32 v195, v195\n"
"v_mfma_f32_16x16x32_bf16 v[208:211], a[40:43], v[96:99], v[208:211]\n"
"buffer_load_dwordx4 v238, s[40:43], s67 offen offset:0 sc1 lds\n"
"v_mfma_f32_16x16x32_bf16 v[208:211], a[44:47], v[112:115], v[208:211]\n"
"v_exp_f32 v198, v198\n"
"v_mfma_f32_16x16x32_bf16 v[212:215], a[44:47], v[116:119], v[212:215]\n"
"ds_read_b128 a[16:19], v227 offset:16384\n"
"v_cvt_pk_bf16_f32 v41, v194, v195\n"
"v_mfma_f32_16x16x32_bf16 v[212:215], a[40:43], v[100:103], v[212:215]\n"
"v_exp_f32 v199, v199\n"
"v_mfma_f32_16x16x32_bf16 v[212:215], a[36:39], v[84:87], v[212:215]\n"
"buffer_load_dwordx4 v238, s[40:43], s67 offen offset:1024 sc1 lds\n"
"v_mfma_f32_16x16x32_bf16 v[212:215], a[32:35], v[68:71], v[212:215]\n"
"s_waitcnt lgkmcnt(1)\n"
"v_exp_f32 v200, v200\n"
"v_mfma_f32_16x16x32_bf16 v[216:219], a[0:3], v[64:67], v[216:219]\n"
"ds_read_b128 a[20:23], v227 offset:16896\n"
"v_cvt_pk_bf16_f32 v45, v198, v199\n"
"v_mfma_f32_16x16x32_bf16 v[216:219], a[4:7], v[80:83], v[216:219]\n"
"ds_read_b128 a[24:27], v227 offset:20480\n"
"v_exp_f32 v204, v204\n"
"v_mfma_f32_16x16x32_bf16 v[216:219], a[8:11], v[96:99], v[216:219]\n"
"ds_read_b128 a[28:31], v227 offset:20992\n"
"v_exp_f32 v201, v201\n"
"v_mfma_f32_16x16x32_bf16 v[216:219], a[12:15], v[112:115], v[216:219]\n"
"v_exp_f32 v205, v205\n"
"v_cvt_pk_bf16_f32 v42, v200, v201\n"
"v_mfma_f32_16x16x32_bf16 v[220:223], a[12:15], v[116:119], v[220:223]\n"
"buffer_load_dwordx4 v238, s[40:43], s67 offen offset:2048 sc1 lds\n"
"v_cvt_pk_bf16_f32 v46, v204, v205\n"
"v_mfma_f32_16x16x32_bf16 v[220:223], a[8:11], v[100:103], v[220:223]\n"
"v_exp_f32 v202, v202\n"
"v_mfma_f32_16x16x32_bf16 v[220:223], a[4:7], v[84:87], v[220:223]\n"
"v_exp_f32 v203, v203\n"
"s_nop 0\n"
"v_cvt_pk_bf16_f32 v43, v202, v203\n"
"v_mfma_f32_16x16x32_bf16 v[220:223], a[0:3], v[68:71], v[220:223]\n"
"v_exp_f32 v206, v206\n"
"v_exp_f32 v207, v207\n"
"s_nop 0\n"
"v_cvt_pk_bf16_f32 v47, v206, v207\n"
"v_mfma_f32_16x16x32_bf16 a[112:115], v[40:43], a[120:123], a[112:115]\n"
"buffer_load_dwordx4 v238, s[40:43], s67 offen offset:3072 sc1 lds\n"
"ds_read_b128 a[32:35], v227 offset:18432\n"
"v_mfma_f32_16x16x32_bf16 a[116:119], v[44:47], a[120:123], a[116:119]\n"
"s_waitcnt lgkmcnt(1)\n"
"ds_read_b128 a[36:39], v227 offset:18944\n"
"ds_read_b128 a[40:43], v227 offset:22528\n"
"v_mfma_f32_16x16x32_bf16 v[192:195], a[16:19], v[0:3], v[228:231]\n"
"s_add_u32 m0, s65, 49152\n"
"s_add_u32 s67, s64, 131072\n"
"ds_read_b128 a[44:47], v227 offset:23040\n"
"v_mfma_f32_16x16x32_bf16 v[192:195], a[20:23], v[16:19], v[192:195]\n"
"ds_read_b128 a[0:3], v227 offset:24576\n"
"ds_read_b128 a[4:7], v227 offset:25088\n"
"v_mfma_f32_16x16x32_bf16 v[192:195], a[24:27], v[32:35], v[192:195]\n"
"buffer_load_dwordx4 v238, s[40:43], s67 offen offset:0 sc1 lds\n"
"v_mfma_f32_16x16x32_bf16 v[192:195], a[28:31], v[48:51], v[192:195]\n"
"ds_read_b128 a[8:11], v227 offset:28672\n"
"ds_read_b128 a[12:15], v227 offset:29184\n"
"v_mfma_f32_16x16x32_bf16 v[196:199], a[28:31], v[52:55], v[232:235]\n"
"v_exp_f32 v208, v208\n"
"v_mfma_f32_16x16x32_bf16 v[196:199], a[24:27], v[36:39], v[196:199]\n"
"v_exp_f32 v209, v209\n"
"v_mfma_f32_16x16x32_bf16 v[196:199], a[20:23], v[20:23], v[196:199]\n"
"buffer_load_dwordx4 v238, s[40:43], s67 offen offset:1024 sc1 lds\n"
"v_mfma_f32_16x16x32_bf16 v[196:199], a[16:19], v[4:7], v[196:199]\n"
"s_waitcnt lgkmcnt(4)\n"
"v_exp_f32 v212, v212\n"
"v_mfma_f32_16x16x32_bf16 v[200:203], a[32:35], v[0:3], v[228:231]\n"
"ds_read_b128 a[16:19], v227 offset:26624\n"
"v_cvt_pk_bf16_f32 v56, v208, v209\n"
"v_mfma_f32_16x16x32_bf16 v[200:203], a[36:39], v[16:19], v[200:203]\n"
"ds_read_b128 a[20:23], v227 offset:27136\n"
"ds_read_b128 a[24:27], v227 offset:30720\n"
"v_mfma_f32_16x16x32_bf16 v[200:203], a[40:43], v[32:35], v[200:203]\n"
"buffer_load_dwordx4 v238, s[40:43], s67 offen offset:2048 sc1 lds\n"
"v_mfma_f32_16x16x32_bf16 v[200:203], a[44:47], v[48:51], v[200:203]\n"
"v_exp_f32 v213, v213\n"
"v_mfma_f32_16x16x32_bf16 v[204:207], a[44:47], v[52:55], v[232:235]\n"
"ds_read_b128 a[28:31], v227 offset:31232\n"
"v_cvt_pk_bf16_f32 v60, v212, v213\n"
"v_mfma_f32_16x16x32_bf16 v[204:207], a[40:43], v[36:39], v[204:207]\n"
"v_exp_f32 v210, v210\n"
"v_mfma_f32_16x16x32_bf16 v[204:207], a[36:39], v[20:23], v[204:207]\n"
"buffer_load_dwordx4 v238, s[40:43], s67 offen offset:3072 sc1 lds\n"
"v_mfma_f32_16x16x32_bf16 v[204:207], a[32:35], v[4:7], v[204:207]\n"
"s_waitcnt lgkmcnt(4)\n"
"v_exp_f32 v211, v211\n"
"v_mfma_f32_16x16x32_bf16 v[192:195], a[0:3], v[64:67], v[192:195]\n"
"s_add_u32 m0, s65, 65536\n"
"v_exp_f32 v214, v214\n"
"v_mfma_f32_16x16x32_bf16 v[192:195], a[4:7], v[80:83], v[192:195]\n"
"s_add_u32 s67, s64, 147456\n"
"ds_read_b128 a[32:35], v225 offset:0\n"
"v_cvt_pk_bf16_f32 v57, v210, v211\n"
"v_mfma_f32_16x16x32_bf16 v[192:195], a[8:11], v[96:99], v[192:195]\n"
"v_exp_f32 v215, v215\n"
"v_mfma_f32_16x16x32_bf16 v[192:195], a[12:15], v[112:115], v[192:195]\n"
"v_exp_f32 v216, v216\n"
"v_mfma_f32_16x16x32_bf16 v[196:199], a[12:15], v[116:119], v[196:199]\n"
"ds_read_b128 a[36:39], v225 offset:512\n"
"v_cvt_pk_bf16_f32 v61, v214, v215\n"
"v_mfma_f32_16x16x32_bf16 v[196:199], a[8:11], v[100:103], v[196:199]\n"
"v_exp_f32 v220, v220\n"
"v_mfma_f32_16x16x32_bf16 v[196:199], a[4:7], v[84:87], v[196:199]\n"
"buffer_load_dwordx4 v238, s[40:43], s67 offen offset:0 sc1 lds\n"
"v_mfma_f32_16x16x32_bf16 v[196:199], a[0:3], v[68:71], v[196:199]\n"
"s_waitcnt lgkmcnt(2)\n"
"v_exp_f32 v217, v217\n"
"v_mfma_f32_16x16x32_bf16 v[200:203], a[16:19], v[64:67], v[200:203]\n"
"IWB4_%=:\n"
"s_cbranch_execz IWB8_%=\n"
"ds_read_b128 a[40:43], v225 offset:4096\n"
"ds_read_b128 a[44:47], v225 offset:4608\n"
"v_mfma_f32_16x16x32_bf16 v[200:203], a[20:23], v[80:83], v[200:203]\n"
"v_exp_f32 v221, v221\n"
"v_mfma_f32_16x16x32_bf16 v[200:203], a[24:27], v[96:99], v[200:203]\n"
"v_cvt_pk_bf16_f32 v58, v216, v217\n"
"v_cvt_pk_bf16_f32 v62, v220, v221\n"
"v_mfma_f32_16x16x32_bf16 v[200:203], a[28:31], v[112:115], v[200:203]\n"
"v_exp_f32 v218, v218\n"
"v_mfma_f32_16x16x32_bf16 v[204:207], a[28:31], v[116:119], v[204:207]\n"
"v_exp_f32 v222, v222\n"
"v_mfma_f32_16x16x32_bf16 v[204:207], a[24:27], v[100:103], v[204:207]\n"
"v_exp_f32 v219, v219\n"
"v_mfma_f32_16x16x32_bf16 v[204:207], a[20:23], v[84:87], v[204:207]\n"
"buffer_load_dwordx4 v238, s[40:43], s67 offen offset:1024 sc1 lds\n"
"v_cvt_pk_bf16_f32 v59, v218, v219\n"
"v_mfma_f32_16x16x32_bf16 v[204:207], a[16:19], v[68:71], v[204:207]\n"
"v_exp_f32 v223, v223\n"
"s_nop 0\n"
"v_cvt_pk_bf16_f32 v63, v222, v223\n"
"v_mfma_f32_16x16x32_bf16 a[112:115], v[56:59], a[120:123], a[112:115]\n"
"ds_read_b128 a[0:3], v225 offset:2048\n"
"ds_read_b128 a[4:7], v225 offset:2560\n"
"v_mfma_f32_16x16x32_bf16 a[116:119], v[60:63], a[120:123], a[116:119]\n"
"s_waitcnt lgkmcnt(2)\n"
"ds_read_b128 a[8:11], v225 offset:6144\n"
"ds_read_b128 a[12:15], v225 offset:6656\n"
"v_mfma_f32_16x16x32_bf16 v[208:211], a[32:35], v[0:3], v[228:231]\n"
"buffer_load_dwordx4 v238, s[40:43], s67 offen offset:2048 sc1 lds\n"
"v_mfma_f32_16x16x32_bf16 v[208:211], a[36:39], v[16:19], v[208:211]\n"
"ds_read_b128 a[16:19], v225 offset:8192\n"
"ds_read_b128 a[20:23], v225 offset:8704\n"
"v_mfma_f32_16x16x32_bf16 v[208:211], a[40:43], v[32:35], v[208:211]\n"
"ds_read_b128 a[24:27], v225 offset:12288\n"
"ds_read_b128 a[28:31], v225 offset:12800\n"
"v_mfma_f32_16x16x32_bf16 v[208:211], a[44:47], v[48:51], v[208:211]\n"
"v_exp_f32 v192, v192\n"
"v_mfma_f32_16x16x32_bf16 v[212:215], a[44:47], v[52:55], v[232:235]\n"
"buffer_load_dwordx4 v238, s[40:43], s67 offen offset:3072 sc1 lds\n"
"v_mfma_f32_16x16x32_bf16 v[212:215], a[40:43], v[36:39], v[212:215]\n"
"s_add_u32 m0, s65, 81920\n"
"s_add_u32 s67, s64, 163840\n"
"v_mov_b32 v152, s86\n"
"v_mfma_f32_16x16x32_bf16 v[212:215], a[36:39], v[20:23], v[212:215]\n"
"v_exp_f32 v193, v193\n"
"v_mfma_f32_16x16x32_bf16 v[212:215], a[32:35], v[4:7], v[212:215]\n"
"s_waitcnt lgkmcnt(4)\n"
"v_exp_f32 v196, v196\n"
"v_mfma_f32_16x16x32_bf16 v[216:219], a[0:3], v[0:3], v[228:231]\n"
"buffer_load_dwordx4 v238, s[40:43], s67 offen offset:0 sc1 lds\n"
"v_mfma_f32_16x16x32_bf16 v[216:219], a[4:7], v[16:19], v[216:219]\n"
"ds_read_b128 a[32:35], v225 offset:10240\n"
"v_cvt_pk_bf16_f32 v72, v192, v193\n"
"v_mfma_f32_16x16x32_bf16 v[216:219], a[8:11], v[32:35], v[216:219]\n"
"ds_read_b128 a[36:39], v225 offset:10752\n"
"ds_read_b128 a[40:43], v225 offset:14336\n"
"v_mfma_f32_16x16x32_bf16 v[216:219], a[12:15], v[48:51], v[216:219]\n"
"v_exp_f32 v197, v197\n"
"v_mfma_f32_16x16x32_bf16 v[220:223], a[12:15], v[52:55], v[232:235]\n"
"buffer_load_dwordx4 v238, s[40:43], s67 offen offset:1024 sc1 lds\n"
"v_mfma_f32_16x16x32_bf16 v[220:223], a[8:11], v[36:39], v[220:223]\n"
"ds_read_b128 a[44:47], v225 offset:14848\n"
"v_cvt_pk_bf16_f32 v76, v196, v197\n"
"v_mfma_f32_16x16x32_bf16 v[220:223], a[4:7], v[20:23], v[220:223]\n"
"v_exp_f32 v194, v194\n"
"v_mfma_f32_16x16x32_bf16 v[220:223], a[0:3], v[4:7], v[220:223]\n"
"s_waitcnt lgkmcnt(4)\n"
"v_exp_f32 v195, v195\n"
"v_mfma_f32_16x16x32_bf16 v[208:211], a[16:19], v[64:67], v[208:211]\n"
"s_waitcnt vmcnt(10)\n"
"s_barrier\n"
"v_cvt_pk_bf16_f32 v73, v194, v195\n"
"v_mfma_f32_16x16x32_bf16 v[208:211], a[20:23], v[80:83], v[208:211]\n"
"buffer_load_dwordx4 v238, s[40:43], s67 offen offset:2048 sc1 lds\n"
"v_mfma_f32_16x16x32_bf16 v[208:211], a[24:27], v[96:99], v[208:211]\n"
"v_exp_f32 v198, v198\n"
"v_mfma_f32_16x16x32_bf16 v[208:211], a[28:31], v[112:115], v[208:211]\n"
"v_exp_f32 v199, v199\n"
"v_mfma_f32_16x16x32_bf16 v[212:215], a[28:31], v[116:119], v[212:215]\n"
"ds_read_b128 a[0:3], v225 offset:16384\n"
"v_cvt_pk_bf16_f32 v77, v198, v199\n"
"v_mfma_f32_16x16x32_bf16 v[212:215], a[24:27], v[100:103], v[212:215]\n"
"buffer_load_dwordx4 v238, s[40:43], s67 offen offset:3072 sc1 lds\n"
"v_mfma_f32_16x16x32_bf16 v[212:215], a[20:23], v[84:87], v[212:215]\n"
"s_add_u32 m0, s65, 98304\n"
"v_exp_f32 v200, v200\n"
"v_mfma_f32_16x16x32_bf16 v[212:215], a[16:19], v[68:71], v[212:215]\n"
"s_waitcnt lgkmcnt(1)\n"
"ds_read_b128 a[4:7], v225 offset:16896\n"
"ds_read_b128 a[8:11], v225 offset:20480\n"
"v_mfma_f32_16x16x32_bf16 v[216:219], a[32:35], v[64:67], v[216:219]\n"
"s_add_u32 s67, s64, 180224\n"
"v_exp_f32 v204, v204\n"
"v_mfma_f32_16x16x32_bf16 v[216:219], a[36:39], v[80:83], v[216:219]\n"
"v_exp_f32 v201, v201\n"
"v_mfma_f32_16x16x32_bf16 v[216:219], a[40:43], v[96:99], v[216:219]\n"
"ds_read_b128 a[12:15], v225 offset:20992\n"
"v_exp_f32 v205, v205\n"
"v_mfma_f32_16x16x32_bf16 v[216:219], a[44:47], v[112:115], v[216:219]\n"
"v_cvt_pk_bf16_f32 v74, v200, v201\n"
"v_exp_f32 v202, v202\n"
"v_mfma_f32_16x16x32_bf16 v[220:223], a[44:47], v[116:119], v[220:223]\n"
"v_cvt_pk_bf16_f32 v78, v204, v205\n"
"v_exp_f32 v206, v206\n"
"v_mfma_f32_16x16x32_bf16 v[220:223], a[40:43], v[100:103], v[220:223]\n"
"v_exp_f32 v203, v203\n"
"v_mfma_f32_16x16x32_bf16 v[220:223], a[36:39], v[84:87], v[220:223]\n"
"v_cvt_pk_bf16_f32 v75, v202, v203\n"
"v_exp_f32 v207, v207\n"
"v_mfma_f32_16x16x32_bf16 v[220:223], a[32:35], v[68:71], v[220:223]\n"
"buffer_load_dwordx4 v238, s[40:43], s67 offen offset:0 sc1 lds\n"
"v_cvt_pk_bf16_f32 v79, v206, v207\n"
"v_mfma_f32_16x16x32_bf16 a[112:115], v[72:75], a[120:123], a[112:115]\n"
"ds_read_b128 a[16:19], v225 offset:18432\n"
"ds_read_b128 a[20:23], v225 offset:18944\n"
"v_mfma_f32_16x16x32_bf16 a[116:119], v[76:79], a[120:123], a[116:119]\n"
"s_waitcnt lgkmcnt(2)\n"
"ds_read_b128 a[24:27], v225 offset:22528\n"
"v_mov_b32 v153, s87\n"
"v_mfma_f32_16x16x32_bf16 v[192:195], a[0:3], v[0:3], v[228:231]\n"
"ds_read_b128 a[28:31], v225 offset:23040\n"
"v_lshrrev_b32 v156, 4, v224\n"
"v_lshlrev_b32 v156, 3, v156\n"
"v_mfma_f32_16x16x32_bf16 v[192:195], a[4:7], v[16:19], v[192:195]\n"
"buffer_load_dwordx4 v238, s[40:43], s67 offen offset:1024 sc1 lds\n"
"v_mfma_f32_16x16x32_bf16 v[192:195], a[8:11], v[32:35], v[192:195]\n"
"buffer_store_dwordx2 v[152:153], v241, s[48:51], s98 offen offset:0 sc1\n"
"v_mfma_f32_16x16x32_bf16 v[192:195], a[12:15], v[48:51], v[192:195]\n"
"ds_read_b128 a[32:35], v225 offset:24576\n"
"ds_read_b128 a[36:39], v225 offset:25088\n"
"v_mfma_f32_16x16x32_bf16 v[196:199], a[12:15], v[52:55], v[232:235]\n"
"ds_read_b128 a[40:43], v225 offset:28672\n"
"ds_read_b128 a[44:47], v225 offset:29184\n"
"v_mfma_f32_16x16x32_bf16 v[196:199], a[8:11], v[36:39], v[196:199]\n"
"buffer_load_dwordx4 v238, s[40:43], s67 offen offset:2048 sc1 lds\n"
"v_mfma_f32_16x16x32_bf16 v[196:199], a[4:7], v[20:23], v[196:199]\n"
"v_exp_f32 v208, v208\n"
"v_mfma_f32_16x16x32_bf16 v[196:199], a[0:3], v[4:7], v[196:199]\n"
"s_waitcnt lgkmcnt(4)\n"
"v_exp_f32 v209, v209\n"
"v_mfma_f32_16x16x32_bf16 v[200:203], a[16:19], v[0:3], v[228:231]\n"
"v_exp_f32 v212, v212\n"
"v_mfma_f32_16x16x32_bf16 v[200:203], a[20:23], v[16:19], v[200:203]\n"
"buffer_load_dwordx4 v238, s[40:43], s67 offen offset:3072 sc1 lds\n"
"v_mfma_f32_16x16x32_bf16 v[200:203], a[24:27], v[32:35], v[200:203]\n"
"s_add_u32 m0, s65, 114688\n"
"s_add_u32 s67, s64, 196608\n"
"ds_read_b128 a[0:3], v225 offset:26624\n"
"v_mfma_f32_16x16x32_bf16 v[200:203], a[28:31], v[48:51], v[200:203]\n"
"ds_read_b128 a[4:7], v225 offset:27136\n"
"v_cvt_pk_bf16_f32 v88, v208, v209\n"
"v_mfma_f32_16x16x32_bf16 v[204:207], a[28:31], v[52:55], v[232:235]\n"
"v_exp_f32 v213, v213\n"
"v_mfma_f32_16x16x32_bf16 v[204:207], a[24:27], v[36:39], v[204:207]\n"
"ds_read_b128 a[8:11], v225 offset:30720\n"
"ds_read_b128 a[12:15], v225 offset:31232\n"
"v_mfma_f32_16x16x32_bf16 v[204:207], a[20:23], v[20:23], v[204:207]\n"
"v_exp_f32 v210, v210\n"
"v_mfma_f32_16x16x32_bf16 v[204:207], a[16:19], v[4:7], v[204:207]\n"
"s_waitcnt lgkmcnt(4)\n"
"ds_read_b128 a[16:19], v225 offset:32768\n"
"v_cvt_pk_bf16_f32 v92, v212, v213\n"
"v_mfma_f32_16x16x32_bf16 v[192:195], a[32:35], v[64:67], v[192:195]\n"
"buffer_load_dwordx4 v238, s[40:43], s67 offen offset:0 sc1 lds\n"
"v_mfma_f32_16x16x32_bf16 v[192:195], a[36:39], v[80:83], v[192:195]\n"
"v_exp_f32 v211, v211\n"
"v_mfma_f32_16x16x32_bf16 v[192:195], a[40:43], v[96:99], v[192:195]\n"
"v_exp_f32 v214, v214\n"
"v_mfma_f32_16x16x32_bf16 v[192:195], a[44:47], v[112:115], v[192:195]\n"
"ds_read_b128 a[20:23], v225 offset:33280\n"
"v_cvt_pk_bf16_f32 v89, v210, v211\n"
"v_mfma_f32_16x16x32_bf16 v[196:199], a[44:47], v[116:119], v[196:199]\n"
"buffer_load_dwordx4 v238, s[40:43], s67 offen offset:1024 sc1 lds\n"
"v_mfma_f32_16x16x32_bf16 v[196:199], a[40:43], v[100:103], v[196:199]\n"
"v_exp_f32 v215, v215\n"
"v_mfma_f32_16x16x32_bf16 v[196:199], a[36:39], v[84:87], v[196:199]\n"
"v_exp_f32 v216, v216\n"
"v_mfma_f32_16x16x32_bf16 v[196:199], a[32:35], v[68:71], v[196:199]\n"
"s_waitcnt lgkmcnt(2)\n"
"ds_read_b128 a[24:27], v225 offset:36864\n"
"v_cvt_pk_bf16_f32 v93, v214, v215\n"
"v_mfma_f32_16x16x32_bf16 v[200:203], a[0:3], v[64:67], v[200:203]\n"
"v_exp_f32 v220, v220\n"
"v_mfma_f32_16x16x32_bf16 v[200:203], a[4:7], v[80:83], v[200:203]\n"
"buffer_load_dwordx4 v238, s[40:43], s67 offen offset:2048 sc1 lds\n"
"v_mfma_f32_16x16x32_bf16 v[200:203], a[8:11], v[96:99], v[200:203]\n"
"ds_read_b128 a[28:31], v225 offset:37376\n"
"v_exp_f32 v217, v217\n"
"v_mfma_f32_16x16x32_bf16 v[200:203], a[12:15], v[112:115], v[200:203]\n"
"v_exp_f32 v221, v221\n"
"v_cvt_pk_bf16_f32 v90, v216, v217\n"
"v_mfma_f32_16x16x32_bf16 v[204:207], a[12:15], v[116:119], v[204:207]\n"
"v_exp_f32 v218, v218\n"
"v_cvt_pk_bf16_f32 v94, v220, v221\n"
"v_mfma_f32_16x16x32_bf16 v[204:207], a[8:11], v[100:103], v[204:207]\n"
"v_exp_f32 v222, v222\n"
"v_mfma_f32_16x16x32_bf16 v[204:207], a[4:7], v[84:87], v[204:207]\n"
"v_exp_f32 v219, v219\n"
"s_nop 0\n"
"v_cvt_pk_bf16_f32 v91, v218, v219\n"
"v_mfma_f32_16x16x32_bf16 v[204:207], a[0:3], v[68:71], v[204:207]\n"
"v_exp_f32 v223, v223\n"
"s_nop 0\n"
"v_cvt_pk_bf16_f32 v95, v222, v223\n"
"v_mfma_f32_16x16x32_bf16 a[112:115], v[88:91], a[120:123], a[112:115]\n"
"buffer_load_dwordx4 v238, s[40:43], s67 offen offset:3072 sc1 lds\n"
"ds_read_b128 a[32:35], v225 offset:34816\n"
"v_mfma_f32_16x16x32_bf16 a[116:119], v[92:95], a[120:123], a[116:119]\n"
"s_waitcnt lgkmcnt(1)\n"
"s_add_u32 m0, s65, 131072\n"
"s_add_u32 s67, s64, 212992\n"
"ds_read_b128 a[36:39], v225 offset:35328\n"
"v_mfma_f32_16x16x32_bf16 v[208:211], a[16:19], v[0:3], v[228:231]\n"
"s_lshl_b32 s68, s5, 11\n"
"ds_read_b128 a[40:43], v225 offset:38912\n"
"ds_read_b128 a[44:47], v225 offset:39424\n"
"v_mfma_f32_16x16x32_bf16 v[208:211], a[20:23], v[16:19], v[208:211]\n"
"s_add_u32 s68, s68, 524288\n"
"ds_read_b128 a[0:3], v225 offset:40960\n"
"ds_read_b128 a[4:7], v225 offset:41472\n"
"v_mfma_f32_16x16x32_bf16 v[208:211], a[24:27], v[32:35], v[208:211]\n"
"buffer_load_dwordx4 v238, s[40:43], s67 offen offset:0 sc1 lds\n"
"v_mfma_f32_16x16x32_bf16 v[208:211], a[28:31], v[48:51], v[208:211]\n"
"ds_read_b128 a[8:11], v225 offset:45056\n"
"ds_read_b128 a[12:15], v225 offset:45568\n"
"v_mfma_f32_16x16x32_bf16 v[212:215], a[28:31], v[52:55], v[232:235]\n"
"v_exp_f32 v192, v192\n"
"v_mfma_f32_16x16x32_bf16 v[212:215], a[24:27], v[36:39], v[212:215]\n"
"v_exp_f32 v193, v193\n"
"v_mfma_f32_16x16x32_bf16 v[212:215], a[20:23], v[20:23], v[212:215]\n"
"buffer_load_dwordx4 v238, s[40:43], s67 offen offset:1024 sc1 lds\n"
"v_mfma_f32_16x16x32_bf16 v[212:215], a[16:19], v[4:7], v[212:215]\n"
"s_waitcnt lgkmcnt(4)\n"
"v_exp_f32 v196, v196\n"
"v_mfma_f32_16x16x32_bf16 v[216:219], a[32:35], v[0:3], v[228:231]\n"
"buffer_load_dwordx4 v[176:179], v224, s[48:51], s68 offen offset:0 sc1\n"
"v_mfma_f32_16x16x32_bf16 v[216:219], a[36:39], v[16:19], v[216:219]\n"
"ds_read_b128 a[16:19], v225 offset:43008\n"
"v_cvt_pk_bf16_f32 v104, v192, v193\n"
"v_mfma_f32_16x16x32_bf16 v[216:219], a[40:43], v[32:35], v[216:219]\n"
"buffer_load_dwordx4 v238, s[40:43], s67 offen offset:2048 sc1 lds\n"
"v_mfma_f32_16x16x32_bf16 v[216:219], a[44:47], v[48:51], v[216:219]\n"
"ds_read_b128 a[20:23], v225 offset:43520\n"
"ds_read_b128 a[24:27], v225 offset:47104\n"
"v_mfma_f32_16x16x32_bf16 v[220:223], a[44:47], v[52:55], v[232:235]\n"
"v_exp_f32 v197, v197\n"
"v_mfma_f32_16x16x32_bf16 v[220:223], a[40:43], v[36:39], v[220:223]\n"
"buffer_load_dwordx4 v[180:183], v224, s[48:51], s68 offen offset:1024 sc1\n"
"v_mfma_f32_16x16x32_bf16 v[220:223], a[36:39], v[20:23], v[220:223]\n"
"buffer_load_dwordx4 v238, s[40:43], s67 offen offset:3072 sc1 lds\n"
"v_mfma_f32_16x16x32_bf16 v[220:223], a[32:35], v[4:7], v[220:223]\n"
"s_waitcnt lgkmcnt(3)\n"
"ds_read_b128 a[28:31], v225 offset:47616\n"
"v_cvt_pk_bf16_f32 v108, v196, v197\n"
"v_mfma_f32_16x16x32_bf16 v[208:211], a[0:3], v[64:67], v[208:211]\n"
"s_waitcnt vmcnt(19)\n"
"s_barrier\n"
"v_exp_f32 v194, v194\n"
"v_mfma_f32_16x16x32_bf16 v[208:211], a[4:7], v[80:83], v[208:211]\n"
"s_add_u32 m0, s65, 147456\n"
"v_exp_f32 v195, v195\n"
"v_mfma_f32_16x16x32_bf16 v[208:211], a[8:11], v[96:99], v[208:211]\n"
"s_add_u32 s67, s64, 229376\n"
"v_exp_f32 v198, v198\n"
"v_mfma_f32_16x16x32_bf16 v[208:211], a[12:15], v[112:115], v[208:211]\n"
"ds_read_b128 a[32:35], v225 offset:49152\n"
"v_cvt_pk_bf16_f32 v105, v194, v195\n"
"v_mfma_f32_16x16x32_bf16 v[212:215], a[12:15], v[116:119], v[212:215]\n"
"v_exp_f32 v199, v199\n"
"v_mfma_f32_16x16x32_bf16 v[212:215], a[8:11], v[100:103], v[212:215]\n"
"buffer_load_dwordx4 v238, s[40:43], s67 offen offset:0 sc1 lds\n"
"v_mfma_f32_16x16x32_bf16 v[212:215], a[4:7], v[84:87], v[212:215]\n"
"v_exp_f32 v200, v200\n"
"v_mfma_f32_16x16x32_bf16 v[212:215], a[0:3], v[68:71], v[212:215]\n"
"s_waitcnt lgkmcnt(1)\n"
"ds_read_b128 a[36:39], v225 offset:49664\n"
"v_cvt_pk_bf16_f32 v109, v198, v199\n"
"v_mfma_f32_16x16x32_bf16 v[216:219], a[16:19], v[64:67], v[216:219]\n"
"v_exp_f32 v204, v204\n"
"v_mfma_f32_16x16x32_bf16 v[216:219], a[20:23], v[80:83], v[216:219]\n"
"ds_read_b128 a[40:43], v225 offset:53248\n"
"v_exp_f32 v201, v201\n"
"v_mfma_f32_16x16x32_bf16 v[216:219], a[24:27], v[96:99], v[216:219]\n"
"ds_read_b128 a[44:47], v225 offset:53760\n"
"v_exp_f32 v205, v205\n"
"v_mfma_f32_16x16x32_bf16 v[216:219], a[28:31], v[112:115], v[216:219]\n"
"buffer_load_dwordx4 v238, s[40:43], s67 offen offset:1024 sc1 lds\n"
"v_cvt_pk_bf16_f32 v106, v200, v201\n"
"v_mfma_f32_16x16x32_bf16 v[220:223], a[28:31], v[116:119], v[220:223]\n"
"v_exp_f32 v202, v202\n"
"v_cvt_pk_bf16_f32 v110, v204, v205\n"
"v_mfma_f32_16x16x32_bf16 v[220:223], a[24:27], v[100:103], v[220:223]\n"
"v_exp_f32 v206, v206\n"
"v_mfma_f32_16x16x32_bf16 v[220:223], a[20:23], v[84:87], v[220:223]\n"
"v_exp_f32 v203, v203\n"
"s_nop 0\n"
"v_cvt_pk_bf16_f32 v107, v202, v203\n"
"v_mfma_f32_16x16x32_bf16 v[220:223], a[16:19], v[68:71], v[220:223]\n"
"v_exp_f32 v207, v207\n"
"s_nop 0\n"
"v_cvt_pk_bf16_f32 v111, v206, v207\n"
"v_mfma_f32_16x16x32_bf16 a[112:115], v[104:107], a[120:123], a[112:115]\n"
"buffer_load_dwordx4 v238, s[40:43], s67 offen offset:2048 sc1 lds\n"
"ds_read_b128 a[0:3], v225 offset:51200\n"
"v_mfma_f32_16x16x32_bf16 a[116:119], v[108:111], a[120:123], a[116:119]\n"
"s_waitcnt lgkmcnt(1)\n"
"ds_read_b128 a[4:7], v225 offset:51712\n"
"ds_read_b128 a[8:11], v225 offset:55296\n"
"v_mfma_f32_16x16x32_bf16 v[192:195], a[32:35], v[0:3], v[228:231]\n"
"ds_read_b128 a[12:15], v225 offset:55808\n"
"ds_read_b128 a[16:19], v225 offset:57344\n"
"v_mfma_f32_16x16x32_bf16 v[192:195], a[36:39], v[16:19], v[192:195]\n"
"ds_read_b128 a[20:23], v225 offset:57856\n"
"ds_read_b128 a[24:27], v225 offset:61440\n"
"v_mfma_f32_16x16x32_bf16 v[192:195], a[40:43], v[32:35], v[192:195]\n"
"buffer_load_dwordx4 v238, s[40:43], s67 offen offset:3072 sc1 lds\n"
"v_mfma_f32_16x16x32_bf16 v[192:195], a[44:47], v[48:51], v[192:195]\n"
"s_add_u32 m0, s65, 0\n"
"s_add_u32 s67, s64, 245760\n"
"ds_read_b128 a[28:31], v225 offset:61952\n"
"v_mfma_f32_16x16x32_bf16 v[196:199], a[44:47], v[52:55], v[232:235]\n"
"v_exp_f32 v208, v208\n"
"v_mfma_f32_16x16x32_bf16 v[196:199], a[40:43], v[36:39], v[196:199]\n"
"v_exp_f32 v209, v209\n"
"v_mfma_f32_16x16x32_bf16 v[196:199], a[36:39], v[20:23], v[196:199]\n"
"buffer_load_dwordx4 v238, s[40:43], s67 offen offset:0 sc1 lds\n"
"v_mfma_f32_16x16x32_bf16 v[196:199], a[32:35], v[4:7], v[196:199]\n"
"s_waitcnt lgkmcnt(4)\n"
"v_exp_f32 v212, v212\n"
"v_mfma_f32_16x16x32_bf16 v[200:203], a[0:3], v[0:3], v[228:231]\n"
"ds_read_b128 a[32:35], v225 offset:59392\n"
"v_cvt_pk_bf16_f32 v120, v208, v209\n"
"v_mfma_f32_16x16x32_bf16 v[200:203], a[4:7], v[16:19], v[200:203]\n"
"ds_read_b128 a[36:39], v225 offset:59904\n"
"ds_read_b128 a[40:43], v225 offset:63488\n"
"v_mfma_f32_16x16x32_bf16 v[200:203], a[8:11], v[32:35], v[200:203]\n"
"v_exp_f32 v213, v213\n"
"v_mfma_f32_16x16x32_bf16 v[200:203], a[12:15], v[48:51], v[200:203]\n"
"buffer_load_dwordx4 v238, s[40:43], s67 offen offset:1024 sc1 lds\n"
"v_mfma_f32_16x16x32_bf16 v[204:207], a[12:15], v[52:55], v[232:235]\n"
"ds_read_b128 a[44:47], v225 offset:64000\n"
"v_cvt_pk_bf16_f32 v124, v212, v213\n"
"v_mfma_f32_16x16x32_bf16 v[204:207], a[8:11], v[36:39], v[204:207]\n"
"v_exp_f32 v210, v210\n"
"v_mfma_f32_16x16x32_bf16 v[204:207], a[4:7], v[20:23], v[204:207]\n"
"v_exp_f32 v211, v211\n"
"v_mfma_f32_16x16x32_bf16 v[204:207], a[0:3], v[4:7], v[204:207]\n"
"s_waitcnt lgkmcnt(4)\n"
"v_exp_f32 v214, v214\n"
"v_mfma_f32_16x16x32_bf16 v[192:195], a[16:19], v[64:67], v[192:195]\n"
"buffer_load_dwordx4 v238, s[40:43], s67 offen offset:2048 sc1 lds\n"
"v_mfma_f32_16x16x32_bf16 v[192:195], a[20:23], v[80:83], v[192:195]\n"
"ds_read_b128 a[0:3], v226 offset:0\n"
"v_cvt_pk_bf16_f32 v121, v210, v211\n"
"v_mfma_f32_16x16x32_bf16 v[192:195], a[24:27], v[96:99], v[192:195]\n"
"v_exp_f32 v215, v215\n"
"v_mfma_f32_16x16x32_bf16 v[192:195], a[28:31], v[112:115], v[192:195]\n"
"v_exp_f32 v216, v216\n"
"v_mfma_f32_16x16x32_bf16 v[196:199], a[28:31], v[116:119], v[196:199]\n"
"ds_read_b128 a[4:7], v226 offset:512\n"
"v_cvt_pk_bf16_f32 v125, v214, v215\n"
"v_mfma_f32_16x16x32_bf16 v[196:199], a[24:27], v[100:103], v[196:199]\n"
"v_exp_f32 v220, v220\n"
"v_mfma_f32_16x16x32_bf16 v[196:199], a[20:23], v[84:87], v[196:199]\n"
"v_exp_f32 v217, v217\n"
"v_mfma_f32_16x16x32_bf16 v[196:199], a[16:19], v[68:71], v[196:199]\n"
"s_waitcnt lgkmcnt(2)\n"
"ds_read_b128 a[8:11], v226 offset:4096\n"
"ds_read_b128 a[12:15], v226 offset:4608\n"
"v_mfma_f32_16x16x32_bf16 v[200:203], a[32:35], v[64:67], v[200:203]\n"
"buffer_load_dwordx4 v238, s[40:43], s67 offen offset:3072 sc1 lds\n"
"v_mfma_f32_16x16x32_bf16 v[200:203], a[36:39], v[80:83], v[200:203]\n"
"v_exp_f32 v221, v221\n"
"v_mfma_f32_16x16x32_bf16 v[200:203], a[40:43], v[96:99], v[200:203]\n"
"v_cvt_pk_bf16_f32 v122, v216, v217\n"
"v_cvt_pk_bf16_f32 v126, v220, v221\n"
"v_mfma_f32_16x16x32_bf16 v[200:203], a[44:47], v[112:115], v[200:203]\n"
"v_exp_f32 v218, v218\n"
"v_mfma_f32_16x16x32_bf16 v[204:207], a[44:47], v[116:119], v[204:207]\n"
"v_exp_f32 v222, v222\n"
"v_mfma_f32_16x16x32_bf16 v[204:207], a[40:43], v[100:103], v[204:207]\n"
"v_exp_f32 v219, v219\n"
"v_mfma_f32_16x16x32_bf16 v[204:207], a[36:39], v[84:87], v[204:207]\n"
"v_cvt_pk_bf16_f32 v123, v218, v219\n"
"v_exp_f32 v223, v223\n"
"v_mfma_f32_16x16x32_bf16 v[204:207], a[32:35], v[68:71], v[204:207]\n"
"ds_read_b128 a[16:19], v226 offset:2048\n"
"v_cvt_pk_bf16_f32 v127, v222, v223\n"
"v_mfma_f32_16x16x32_bf16 a[112:115], v[120:123], a[120:123], a[112:115]\n"
"ds_read_b128 a[20:23], v226 offset:2560\n"
"ds_read_b128 a[24:27], v226 offset:6144\n"
"v_mfma_f32_16x16x32_bf16 a[116:119], v[124:127], a[120:123], a[116:119]\n"
"s_waitcnt lgkmcnt(3)\n"
"ds_read_b128 a[28:31], v226 offset:6656\n"
"ds_read_b128 a[32:35], v226 offset:8192\n"
"v_mfma_f32_16x16x32_bf16 v[208:211], a[0:3], v[0:3], v[228:231]\n"
"s_waitcnt vmcnt(9)\n"
"buffer_load_dwordx2 v[154:155], v156, s[48:51], s97 offen offset:0 sc1\n"
"v_mfma_f32_16x16x32_bf16 v[208:211], a[4:7], v[16:19], v[208:211]\n"
"ds_read_b128 a[36:39], v226 offset:8704\n"
"ds_read_b128 a[40:43], v226 offset:12288\n"
"v_mfma_f32_16x16x32_bf16 v[208:211], a[8:11], v[32:35], v[208:211]\n"
"ds_read_b128 a[44:47], v226 offset:12800\n"
"v_cmp_eq_u32_e64 s[84:85], s86, v177\n"
"v_mfma_f32_16x16x32_bf16 v[208:211], a[12:15], v[48:51], v[208:211]\n"
"v_exp_f32 v192, v192\n"
"v_mfma_f32_16x16x32_bf16 v[212:215], a[12:15], v[52:55], v[232:235]\n"
"v_exp_f32 v193, v193\n"
"v_mfma_f32_16x16x32_bf16 v[212:215], a[8:11], v[36:39], v[212:215]\n"
"v_exp_f32 v196, v196\n"
"v_mfma_f32_16x16x32_bf16 v[212:215], a[4:7], v[20:23], v[212:215]\n"
"v_cvt_pk_bf16_f32 v128, v192, v193\n"
"v_cmp_eq_u32_e64 s[82:83], s87, v179\n"
"v_mfma_f32_16x16x32_bf16 v[212:215], a[0:3], v[4:7], v[212:215]\n"
"s_waitcnt lgkmcnt(4)\n"
"ds_read_b128 a[0:3], v226 offset:10240\n"
"ds_read_b128 a[4:7], v226 offset:10752\n"
"v_mfma_f32_16x16x32_bf16 v[216:219], a[16:19], v[0:3], v[228:231]\n"
"s_and_b64 s[84:85], s[84:85], s[82:83]\n"
"v_exp_f32 v197, v197\n"
"v_mfma_f32_16x16x32_bf16 v[216:219], a[20:23], v[16:19], v[216:219]\n"
"ds_read_b128 a[8:11], v226 offset:14336\n"
"ds_read_b128 a[12:15], v226 offset:14848\n"
"v_mfma_f32_16x16x32_bf16 v[216:219], a[24:27], v[32:35], v[216:219]\n"
"v_exp_f32 v194, v194\n"
"v_mfma_f32_16x16x32_bf16 v[216:219], a[28:31], v[48:51], v[216:219]\n"
"v_cvt_pk_bf16_f32 v132, v196, v197\n"
"v_cmp_eq_u32_e64 s[82:83], s86, v181\n"
"v_mfma_f32_16x16x32_bf16 v[220:223], a[28:31], v[52:55], v[232:235]\n"
"s_and_b64 s[84:85], s[84:85], s[82:83]\n"
"v_exp_f32 v195, v195\n"
"v_mfma_f32_16x16x32_bf16 v[220:223], a[24:27], v[36:39], v[220:223]\n"
"v_exp_f32 v198, v198\n"
"v_mfma_f32_16x16x32_bf16 v[220:223], a[20:23], v[20:23], v[220:223]\n"
"v_cvt_pk_bf16_f32 v129, v194, v195\n"
"v_cmp_eq_u32_e64 s[82:83], s87, v183\n"
"v_mfma_f32_16x16x32_bf16 v[220:223], a[16:19], v[4:7], v[220:223]\n"
"s_waitcnt lgkmcnt(4)\n"
"v_exp_f32 v199, v199\n"
"v_mfma_f32_16x16x32_bf16 v[208:211], a[32:35], v[64:67], v[208:211]\n"
"s_barrier\n"
"ds_read_b128 a[16:19], v226 offset:16384\n"
"v_mfma_f32_16x16x32_bf16 v[208:211], a[36:39], v[80:83], v[208:211]\n"
"s_and_b64 s[84:85], s[84:85], s[82:83]\n"
"v_exp_f32 v200, v200\n"
"v_mfma_f32_16x16x32_bf16 v[208:211], a[40:43], v[96:99], v[208:211]\n"
"ds_read_b128 a[20:23], v226 offset:16896\n"
"v_cvt_pk_bf16_f32 v133, v198, v199\n"
"v_mfma_f32_16x16x32_bf16 v[208:211], a[44:47], v[112:115], v[208:211]\n"
"v_exp_f32 v204, v204\n"
"v_mfma_f32_16x16x32_bf16 v[212:215], a[44:47], v[116:119], v[212:215]\n"
"v_exp_f32 v201, v201\n"
"v_mfma_f32_16x16x32_bf16 v[212:215], a[40:43], v[100:103], v[212:215]\n"
"ds_read_b128 a[24:27], v226 offset:20480\n"
"ds_read_b128 a[28:31], v226 offset:20992\n"
"v_mfma_f32_16x16x32_bf16 v[212:215], a[36:39], v[84:87], v[212:215]\n"
"v_exp_f32 v205, v205\n"
"v_mfma_f32_16x16x32_bf16 v[212:215], a[32:35], v[68:71], v[212:215]\n"
"s_waitcnt lgkmcnt(4)\n"
"v_cvt_pk_bf16_f32 v130, v200, v201\n"
"v_cvt_pk_bf16_f32 v134, v204, v205\n"
"v_mfma_f32_16x16x32_bf16 v[216:219], a[0:3], v[64:67], v[216:219]\n"
"v_exp_f32 v202, v202\n"
"v_mfma_f32_16x16x32_bf16 v[216:219], a[4:7], v[80:83], v[216:219]\n"
"v_exp_f32 v206, v206\n"
"v_mfma_f32_16x16x32_bf16 v[216:219], a[8:11], v[96:99], v[216:219]\n"
"v_exp_f32 v203, v203\n"
"v_mfma_f32_16x16x32_bf16 v[216:219], a[12:15], v[112:115], v[216:219]\n"
"v_exp_f32 v207, v207\n"
"v_mfma_f32_16x16x32_bf16 v[220:223], a[12:15], v[116:119], v[220:223]\n"
"ds_read_b128 a[32:35], v226 offset:18432\n"
"v_cvt_pk_bf16_f32 v131, v202, v203\n"
"v_mfma_f32_16x16x32_bf16 v[220:223], a[8:11], v[100:103], v[220:223]\n"
"ds_read_b128 a[36:39], v226 offset:18944\n"
"v_cvt_pk_bf16_f32 v135, v206, v207\n"
"v_mfma_f32_16x16x32_bf16 v[220:223], a[4:7], v[84:87], v[220:223]\n"
"ds_read_b128 a[40:43], v226 offset:22528\n"
"ds_read_b128 a[44:47], v226 offset:23040\n"
"v_mfma_f32_16x16x32_bf16 v[220:223], a[0:3], v[68:71], v[220:223]\n"
"s_cmp_eq_u64 s[84:85], -1\n"
"s_cbranch_scc0 SPH_%=\n"
"SBH_%=:\n"
"ds_read_b128 a[0:3], v226 offset:24576\n"
"v_mfma_f32_16x16x32_bf16 a[112:115], v[128:131], a[120:123], a[112:115]\n"
"ds_read_b128 a[4:7], v226 offset:25088\n"
"v_add_f32_dpp v176, v176, v176 quad_perm:[1,0,3,2] row_mask:0xf bank_mask:0xf\n"
"v_mfma_f32_16x16x32_bf16 a[116:119], v[132:135], a[120:123], a[116:119]\n"
"s_waitcnt lgkmcnt(6)\n"
"ds_read_b128 a[8:11], v226 offset:28672\n"
"v_add_f32_dpp v178, v178, v178 quad_perm:[1,0,3,2] row_mask:0xf bank_mask:0xf\n"
"v_mfma_f32_16x16x32_bf16 v[192:195], a[16:19], v[0:3], v[228:231]\n"
"s_waitcnt vmcnt(0)\n"
"v_cmp_eq_u32_e64 s[14:15], s86, v154\n"
"v_cmp_eq_u32_e64 s[16:17], s87, v155\n"
"v_mfma_f32_16x16x32_bf16 v[192:195], a[20:23], v[16:19], v[192:195]\n"
"s_and_b64 s[14:15], s[14:15], s[16:17]\n"
"s_cmp_eq_u64 s[14:15], -1\n"
"s_cbranch_scc0 SPV_%=\n"
"SBV_%=:\n"
"v_mfma_f32_16x16x32_bf16 v[192:195], a[24:27], v[32:35], v[192:195]\n"
"ds_read_b128 a[12:15], v226 offset:29184\n"
"v_add_f32_dpp v180, v180, v180 quad_perm:[1,0,3,2] row_mask:0xf bank_mask:0xf\n"
"v_mfma_f32_16x16x32_bf16 v[192:195], a[28:31], v[48:51], v[192:195]\n"
"v_exp_f32 v208, v208\n"
"v_mfma_f32_16x16x32_bf16 v[196:199], a[28:31], v[52:55], v[232:235]\n"
"v_exp_f32 v209, v209\n"
"v_mfma_f32_16x16x32_bf16 v[196:199], a[24:27], v[36:39], v[196:199]\n"
"v_exp_f32 v212, v212\n"
"v_mfma_f32_16x16x32_bf16 v[196:199], a[20:23], v[20:23], v[196:199]\n"
"v_add_f32_dpp v182, v182, v182 quad_perm:[1,0,3,2] row_mask:0xf bank_mask:0xf\n"
"v_cvt_pk_bf16_f32 v136, v208, v209\n"
"v_mfma_f32_16x16x32_bf16 v[196:199], a[16:19], v[4:7], v[196:199]\n"
"s_waitcnt lgkmcnt(4)\n"
"ds_read_b128 a[16:19], v226 offset:26624\n"
"v_add_f32_dpp v176, v176, v176 quad_perm:[2,3,0,1] row_mask:0xf bank_mask:0xf\n"
"v_mfma_f32_16x16x32_bf16 v[200:203], a[32:35], v[0:3], v[228:231]\n"
"ds_read_b128 a[20:23], v226 offset:27136\n"
"v_add_f32_dpp v178, v178, v178 quad_perm:[2,3,0,1] row_mask:0xf bank_mask:0xf\n"
"v_mfma_f32_16x16x32_bf16 v[200:203], a[36:39], v[16:19], v[200:203]\n"
"s_add_u32 m0, s65, 16384\n"
"s_add_u32 s67, s65, 0\n"
"ds_read_b128 a[24:27], v226 offset:30720\n"
"v_mfma_f32_16x16x32_bf16 v[200:203], a[40:43], v[32:35], v[200:203]\n"
"v_exp_f32 v213, v213\n"
"v_mfma_f32_16x16x32_bf16 v[200:203], a[44:47], v[48:51], v[200:203]\n"
"buffer_load_dwordx4 v224, s[44:47], s67 offen offset:0 sc1 lds\n"
"v_mfma_f32_16x16x32_bf16 v[204:207], a[44:47], v[52:55], v[232:235]\n"
"ds_read_b128 a[28:31], v226 offset:31232\n"
"v_cvt_pk_bf16_f32 v140, v212, v213\n"
"v_mfma_f32_16x16x32_bf16 v[204:207], a[40:43], v[36:39], v[204:207]\n"
"v_exp_f32 v210, v210\n"
"v_mfma_f32_16x16x32_bf16 v[204:207], a[36:39], v[20:23], v[204:207]\n"
"v_add_f32_dpp v180, v180, v180 quad_perm:[2,3,0,1] row_mask:0xf bank_mask:0xf\n"
"v_add_f32_dpp v182, v182, v182 quad_perm:[2,3,0,1] row_mask:0xf bank_mask:0xf\n"
"v_mfma_f32_16x16x32_bf16 v[204:207], a[32:35], v[4:7], v[204:207]\n"
"s_waitcnt lgkmcnt(4)\n"
"v_exp_f32 v211, v211\n"
"v_mfma_f32_16x16x32_bf16 v[192:195], a[0:3], v[64:67], v[192:195]\n"
"v_add_f32_dpp v176, v176, v176 row_half_mirror row_mask:0xf bank_mask:0xf\n"
"v_add_f32_dpp v178, v178, v178 row_half_mirror row_mask:0xf bank_mask:0xf\n"
"v_mfma_f32_16x16x32_bf16 v[192:195], a[4:7], v[80:83], v[192:195]\n"
"buffer_load_dwordx4 v224, s[44:47], s67 offen offset:1024 sc1 lds\n"
"v_mfma_f32_16x16x32_bf16 v[192:195], a[8:11], v[96:99], v[192:195]\n"
"v_exp_f32 v214, v214\n"
"v_mfma_f32_16x16x32_bf16 v[192:195], a[12:15], v[112:115], v[192:195]\n"
"v_cvt_pk_bf16_f32 v137, v210, v211\n"
"v_add_f32_dpp v180, v180, v180 row_half_mirror row_mask:0xf bank_mask:0xf\n"
"v_mfma_f32_16x16x32_bf16 v[196:199], a[12:15], v[116:119], v[196:199]\n"
"v_exp_f32 v215, v215\n"
"v_mfma_f32_16x16x32_bf16 v[196:199], a[8:11], v[100:103], v[196:199]\n"
"ds_read_b128 a[32:35], v226 offset:32768\n"
"v_add_f32_dpp v182, v182, v182 row_half_mirror row_mask:0xf bank_mask:0xf\n"
"v_mfma_f32_16x16x32_bf16 v[196:199], a[4:7], v[84:87], v[196:199]\n"
"buffer_load_dwordx4 v224, s[44:47], s67 offen offset:2048 sc1 lds\n"
"v_mfma_f32_16x16x32_bf16 v[196:199], a[0:3], v[68:71], v[196:199]\n"
"s_waitcnt lgkmcnt(1)\n"
"v_exp_f32 v216, v216\n"
"v_mul_f32 v177, 0xc1800000, v176\n"
"v_mfma_f32_16x16x32_bf16 v[200:203], a[16:19], v[64:67], v[200:203]\n"
"ds_read_b128 a[36:39], v226 offset:33280\n"
"v_cvt_pk_bf16_f32 v141, v214, v215\n"
"v_mul_f32 v181, 0xc1800000, v180\n"
"v_mfma_f32_16x16x32_bf16 v[200:203], a[20:23], v[80:83], v[200:203]\n"
"ds_read_b128 a[40:43], v226 offset:36864\n"
"v_exp_f32 v220, v220\n"
"v_mfma_f32_16x16x32_bf16 v[200:203], a[24:27], v[96:99], v[200:203]\n"
"ds_read_b128 a[44:47], v226 offset:37376\n"
"v_exp_f32 v217, v217\n"
"v_exp_f32 v177, v177\n"
"v_mfma_f32_16x16x32_bf16 v[200:203], a[28:31], v[112:115], v[200:203]\n"
"v_exp_f32 v181, v181\n"
"v_cvt_pk_bf16_f32 v138, v216, v217\n"
"v_mfma_f32_16x16x32_bf16 v[204:207], a[28:31], v[116:119], v[204:207]\n"
"v_mul_f32 v176, v176, v178\n"
"v_exp_f32 v221, v221\n"
"v_mfma_f32_16x16x32_bf16 v[204:207], a[24:27], v[100:103], v[204:207]\n"
"buffer_load_dwordx4 v224, s[44:47], s67 offen offset:3072 sc1 lds\n"
"v_mul_f32 v180, v180, v182\n"
"v_mfma_f32_16x16x32_bf16 v[204:207], a[20:23], v[84:87], v[204:207]\n"
"v_exp_f32 v218, v218\n"
"v_exp_f32 v219, v219\n"
"s_nop 0\n"
"v_cvt_pk_bf16_f32 v139, v218, v219\n"
"v_mfma_f32_16x16x32_bf16 v[204:207], a[16:19], v[68:71], v[204:207]\n"
"s_add_u32 m0, s65, 49152\n"
"s_add_u32 s67, s65, 16384\n"
"v_cvt_pk_bf16_f32 v142, v220, v221\n"
"v_exp_f32 v222, v222\n"
"v_exp_f32 v223, v223\n"
"s_nop 0\n"
"v_cvt_pk_bf16_f32 v143, v222, v223\n"
"v_add_f32 v177, 1.0, v177\n"
"v_mfma_f32_16x16x32_bf16 a[112:115], v[136:139], a[120:123], a[112:115]\n"
"IWB5_%=:\n"
"s_cbranch_execz IWB9_%=\n"
"ds_read_b128 a[0:3], v226 offset:34816\n"
"ds_read_b128 a[4:7], v226 offset:35328\n"
"v_add_f32 v181, 1.0, v181\n"
"v_mfma_f32_16x16x32_bf16 a[116:119], v[140:143], a[120:123], a[116:119]\n"
"s_waitcnt lgkmcnt(2)\n"
"ds_read_b128 a[8:11], v226 offset:38912\n"
"v_rcp_f32 v177, v177\n"
"v_mfma_f32_16x16x32_bf16 v[208:211], a[32:35], v[0:3], v[228:231]\n"
"ds_read_b128 a[12:15], v226 offset:39424\n"
"v_rcp_f32 v181, v181\n"
"v_mfma_f32_16x16x32_bf16 v[208:211], a[36:39], v[16:19], v[208:211]\n"
"buffer_load_dwordx4 v224, s[44:47], s67 offen offset:0 sc1 lds\n"
"v_mfma_f32_16x16x32_bf16 v[208:211], a[40:43], v[32:35], v[208:211]\n"
"v_mul_f32 v176, v176, v177\n"
"v_mul_f32 v180, v180, v181\n"
"v_mfma_f32_16x16x32_bf16 v[208:211], a[44:47], v[48:51], v[208:211]\n"
"ds_read_b128 a[16:19], v226 offset:40960\n"
"v_mul_f32 v176, 0x42f5fdf0, v176\n"
"v_mfma_f32_16x16x32_bf16 v[212:215], a[44:47], v[52:55], v[232:235]\n"
"ds_read_b128 a[20:23], v226 offset:41472\n"
"v_mul_f32 v180, 0x42f5fdf0, v180\n"
"v_mfma_f32_16x16x32_bf16 v[212:215], a[40:43], v[36:39], v[212:215]\n"
"buffer_load_dwordx4 v224, s[44:47], s67 offen offset:1024 sc1 lds\n"
"v_mfma_f32_16x16x32_bf16 v[212:215], a[36:39], v[20:23], v[212:215]\n"
"ds_read_b128 a[24:27], v226 offset:45056\n"
"v_readlane_b32 s72, v176, 0\n"
"v_mfma_f32_16x16x32_bf16 v[212:215], a[32:35], v[4:7], v[212:215]\n"
"s_waitcnt lgkmcnt(3)\n"
"ds_read_b128 a[28:31], v226 offset:45568\n"
"v_readlane_b32 s73, v176, 8\n"
"v_mfma_f32_16x16x32_bf16 v[216:219], a[0:3], v[0:3], v[228:231]\n"
"v_exp_f32 v192, v192\n"
"v_mfma_f32_16x16x32_bf16 v[216:219], a[4:7], v[16:19], v[216:219]\n"
"v_readlane_b32 s74, v176, 16\n"
"v_readlane_b32 s75, v176, 24\n"
"v_mfma_f32_16x16x32_bf16 v[216:219], a[8:11], v[32:35], v[216:219]\n"
"buffer_load_dwordx4 v224, s[44:47], s67 offen offset:2048 sc1 lds\n"
"v_mfma_f32_16x16x32_bf16 v[216:219], a[12:15], v[48:51], v[216:219]\n"
"v_exp_f32 v193, v193\n"
"v_exp_f32 v196, v196\n"
"v_mfma_f32_16x16x32_bf16 v[220:223], a[12:15], v[52:55], v[232:235]\n"
"ds_read_b128 a[32:35], v226 offset:43008\n"
"v_readlane_b32 s76, v176, 32\n"
"v_mfma_f32_16x16x32_bf16 v[220:223], a[8:11], v[36:39], v[220:223]\n"
"v_cvt_pk_bf16_f32 v144, v192, v193\n"
"v_readlane_b32 s77, v176, 40\n"
"v_mfma_f32_16x16x32_bf16 v[220:223], a[4:7], v[20:23], v[220:223]\n"
"v_exp_f32 v197, v197\n"
"v_readlane_b32 s78, v176, 48\n"
"v_mfma_f32_16x16x32_bf16 v[220:223], a[0:3], v[4:7], v[220:223]\n"
"s_waitcnt lgkmcnt(1)\n"
"ds_read_b128 a[36:39], v226 offset:43520\n"
"ds_read_b128 a[40:43], v226 offset:47104\n"
"v_readlane_b32 s79, v176, 56\n"
"v_mfma_f32_16x16x32_bf16 v[208:211], a[16:19], v[64:67], v[208:211]\n"
"s_barrier\n"
"ds_read_b128 a[44:47], v226 offset:47616\n"
"v_exp_f32 v194, v194\n"
"v_cvt_pk_bf16_f32 v148, v196, v197\n"
"v_mfma_f32_16x16x32_bf16 v[208:211], a[20:23], v[80:83], v[208:211]\n"
"buffer_load_dwordx4 v224, s[44:47], s67 offen offset:3072 sc1 lds\n"
"v_readlane_b32 s80, v180, 0\n"
"v_mfma_f32_16x16x32_bf16 v[208:211], a[24:27], v[96:99], v[208:211]\n"
"v_exp_f32 v195, v195\n"
"v_readlane_b32 s81, v180, 8\n"
"v_mfma_f32_16x16x32_bf16 v[208:211], a[28:31], v[112:115], v[208:211]\n"
"s_add_u32 m0, s65, 65536\n"
"s_add_u32 s67, s65, 32768\n"
"v_exp_f32 v198, v198\n"
"v_readlane_b32 s20, v180, 16\n"
"v_mfma_f32_16x16x32_bf16 v[212:215], a[28:31], v[116:119], v[212:215]\n"
"v_readlane_b32 s21, v180, 24\n"
"v_mul_u32_u24 v242, 1024, v224\n"
"v_mfma_f32_16x16x32_bf16 v[212:215], a[24:27], v[100:103], v[212:215]\n"
"v_cvt_pk_bf16_f32 v145, v194, v195\n"
"v_exp_f32 v199, v199\n"
"v_mfma_f32_16x16x32_bf16 v[212:215], a[20:23], v[84:87], v[212:215]\n"
"v_readlane_b32 s22, v180, 32\n"
"v_lshrrev_b32 v243, 7, v224\n"
"v_min_u32 v243, 1, v243\n"
"v_mfma_f32_16x16x32_bf16 v[212:215], a[16:19], v[68:71], v[212:215]\n"
"s_waitcnt lgkmcnt(0)\n"
"v_exp_f32 v200, v200\n"
"v_readlane_b32 s23, v180, 40\n"
"v_mfma_f32_16x16x32_bf16 v[216:219], a[32:35], v[64:67], v[216:219]\n"
"buffer_load_dwordx4 v224, s[44:47], s67 offen offset:0 sc1 lds\n"
"v_lshl_add_u32 v242, v243, 30, v242\n"
"v_not_b32 v243, s87\n"
"buffer_store_dword v243, v242, s[48:51], s0 offen offset:4 sc1\n"
"v_cvt_pk_bf16_f32 v149, v198, v199\n"
"v_readlane_b32 s84, v180, 48\n"
"v_mfma_f32_16x16x32_bf16 v[216:219], a[36:39], v[80:83], v[216:219]\n"
"ds_read_b128 a[0:3], v226 offset:49152\n"
"ds_read_b128 a[4:7], v226 offset:49664\n"
"v_readlane_b32 s85, v180, 56\n"
"v_mfma_f32_16x16x32_bf16 v[216:219], a[40:43], v[96:99], v[216:219]\n"
"ds_read_b128 a[8:11], v226 offset:53248\n"
"ds_read_b128 a[12:15], v226 offset:53760\n"
"v_exp_f32 v201, v201\n"
"v_mfma_f32_16x16x32_bf16 v[216:219], a[44:47], v[112:115], v[216:219]\n"
"v_exp_f32 v204, v204\n"
"v_cvt_pk_bf16_f32 v146, v200, v201\n"
"v_mfma_f32_16x16x32_bf16 v[220:223], a[44:47], v[116:119], v[220:223]\n"
"v_lshlrev_b32 v188, 16, v248\n"
"v_exp_f32 v205, v205\n"
"v_mfma_f32_16x16x32_bf16 v[220:223], a[40:43], v[100:103], v[220:223]\n"
"buffer_load_dwordx4 v224, s[44:47], s67 offen offset:1024 sc1 lds\n"
"v_and_b32 v189, 0xffff0000, v248\n"
"v_mfma_f32_16x16x32_bf16 v[220:223], a[36:39], v[84:87], v[220:223]\n"
"v_exp_f32 v202, v202\n"
"v_exp_f32 v203, v203\n"
"s_nop 0\n"
"v_cvt_pk_bf16_f32 v147, v202, v203\n"
"v_mul_f32 v184, s72, v188\n"
"v_mfma_f32_16x16x32_bf16 v[220:223], a[32:35], v[68:71], v[220:223]\n"
"v_cvt_pk_bf16_f32 v150, v204, v205\n"
"v_exp_f32 v206, v206\n"
"v_exp_f32 v207, v207\n"
"s_nop 0\n"
"v_cvt_pk_bf16_f32 v151, v206, v207\n"
"v_mfma_f32_16x16x32_bf16 a[112:115], v[144:147], a[120:123], a[112:115]\n"
"ds_read_b128 a[16:19], v226 offset:51200\n"
"ds_read_b128 a[20:23], v226 offset:51712\n"
"v_mul_f32 v185, s72, v189\n"
"v_mfma_f32_16x16x32_bf16 a[116:119], v[148:151], a[120:123], a[116:119]\n"
"s_waitcnt lgkmcnt(2)\n"
"buffer_load_dwordx4 v224, s[44:47], s67 offen offset:2048 sc1 lds\n"
"v_mul_f32 v186, s73, v188\n"
"v_mfma_f32_16x16x32_bf16 v[192:195], a[0:3], v[0:3], v[228:231]\n"
"ds_read_b128 a[24:27], v226 offset:55296\n"
"ds_read_b128 a[28:31], v226 offset:55808\n"
"v_mul_f32 v187, s73, v189\n"
"v_mfma_f32_16x16x32_bf16 v[192:195], a[4:7], v[16:19], v[192:195]\n"
"v_lshlrev_b32 v188, 16, v249\n"
"v_and_b32 v189, 0xffff0000, v249\n"
"v_mfma_f32_16x16x32_bf16 v[192:195], a[8:11], v[32:35], v[192:195]\n"
"ds_read_b128 a[32:35], v226 offset:57344\n"
"v_fmac_f32 v184, s74, v188\n"
"v_mfma_f32_16x16x32_bf16 v[192:195], a[12:15], v[48:51], v[192:195]\n"
"buffer_load_dwordx4 v224, s[44:47], s67 offen offset:3072 sc1 lds\n"
"v_mfma_f32_16x16x32_bf16 v[196:199], a[12:15], v[52:55], v[232:235]\n"
"s_add_u32 m0, s65, 81920\n"
"s_add_u32 s67, s65, 49152\n"
"v_fmac_f32 v185, s74, v189\n"
"v_mfma_f32_16x16x32_bf16 v[196:199], a[8:11], v[36:39], v[196:199]\n"
"ds_read_b128 a[36:39], v226 offset:57856\n"
"v_fmac_f32 v186, s75, v188\n"
"v_mfma_f32_16x16x32_bf16 v[196:199], a[4:7], v[20:23], v[196:199]\n"
"ds_read_b128 a[40:43], v226 offset:61440\n"
"ds_read_b128 a[44:47], v226 offset:61952\n"
"v_mfma_f32_16x16x32_bf16 v[196:199], a[0:3], v[4:7], v[196:199]\n"
"s_waitcnt lgkmcnt(4)\n"
"v_exp_f32 v208, v208\n"
"v_mfma_f32_16x16x32_bf16 v[200:203], a[16:19], v[0:3], v[228:231]\n"
"v_fmac_f32 v187, s75, v189\n"
"v_lshlrev_b32 v188, 16, v250\n"
"v_mfma_f32_16x16x32_bf16 v[200:203], a[20:23], v[16:19], v[200:203]\n"
"buffer_load_dwordx4 v224, s[44:47], s67 offen offset:0 sc1 lds\n"
"v_mfma_f32_16x16x32_bf16 v[200:203], a[24:27], v[32:35], v[200:203]\n"
"v_and_b32 v189, 0xffff0000, v250\n"
"v_fmac_f32 v184, s76, v188\n"
"v_mfma_f32_16x16x32_bf16 v[200:203], a[28:31], v[48:51], v[200:203]\n"
"v_exp_f32 v209, v209\n"
"v_exp_f32 v212, v212\n"
"v_mfma_f32_16x16x32_bf16 v[204:207], a[28:31], v[52:55], v[232:235]\n"
"ds_read_b128 a[0:3], v226 offset:59392\n"
"v_cvt_pk_bf16_f32 v152, v208, v209\n"
"v_mfma_f32_16x16x32_bf16 v[204:207], a[24:27], v[36:39], v[204:207]\n"
"ds_read_b128 a[4:7], v226 offset:59904\n"
"v_fmac_f32 v185, s76, v189\n"
"v_mfma_f32_16x16x32_bf16 v[204:207], a[20:23], v[20:23], v[204:207]\n"
"v_exp_f32 v213, v213\n"
"v_fmac_f32 v186, s77, v188\n"
"v_mfma_f32_16x16x32_bf16 v[204:207], a[16:19], v[4:7], v[204:207]\n"
"s_waitcnt lgkmcnt(2)\n"
"buffer_load_dwordx4 v224, s[44:47], s67 offen offset:1024 sc1 lds\n"
"v_fmac_f32 v187, s77, v189\n"
"v_mfma_f32_16x16x32_bf16 v[192:195], a[32:35], v[64:67], v[192:195]\n"
"ds_read_b128 a[8:11], v226 offset:63488\n"
"ds_read_b128 a[12:15], v226 offset:64000\n"
"v_exp_f32 v210, v210\n"
"v_cvt_pk_bf16_f32 v156, v212, v213\n"
"v_lshlrev_b32 v188, 16, v251\n"
"v_mfma_f32_16x16x32_bf16 v[192:195], a[36:39], v[80:83], v[192:195]\n"
"v_exp_f32 v211, v211\n"
"v_mfma_f32_16x16x32_bf16 v[192:195], a[40:43], v[96:99], v[192:195]\n"
"v_and_b32 v189, 0xffff0000, v251\n"
"v_fmac_f32 v184, s78, v188\n"
"v_mfma_f32_16x16x32_bf16 v[192:195], a[44:47], v[112:115], v[192:195]\n"
"v_exp_f32 v214, v214\n"
"v_mfma_f32_16x16x32_bf16 v[196:199], a[44:47], v[116:119], v[196:199]\n"
"buffer_load_dwordx4 v224, s[44:47], s67 offen offset:2048 sc1 lds\n"
"v_fmac_f32 v185, s78, v189\n"
"v_mfma_f32_16x16x32_bf16 v[196:199], a[40:43], v[100:103], v[196:199]\n"
"v_cvt_pk_bf16_f32 v153, v210, v211\n"
"v_exp_f32 v215, v215\n"
"v_fmac_f32 v186, s79, v188\n"
"v_mfma_f32_16x16x32_bf16 v[196:199], a[36:39], v[84:87], v[196:199]\n"
"ds_read_b128 a[16:19], v227 offset:0\n"
"v_fmac_f32 v187, s79, v189\n"
"v_mfma_f32_16x16x32_bf16 v[196:199], a[32:35], v[68:71], v[196:199]\n"
"s_waitcnt lgkmcnt(1)\n"
"v_exp_f32 v216, v216\n"
"v_mfma_f32_16x16x32_bf16 v[200:203], a[0:3], v[64:67], v[200:203]\n"
"ds_read_b128 a[20:23], v227 offset:512\n"
"v_cvt_pk_bf16_f32 v157, v214, v215\n"
"v_lshlrev_b32 v188, 16, v252\n"
"v_mfma_f32_16x16x32_bf16 v[200:203], a[4:7], v[80:83], v[200:203]\n"
"buffer_load_dwordx4 v224, s[44:47], s67 offen offset:3072 sc1 lds\n"
"v_and_b32 v189, 0xffff0000, v252\n"
"v_mfma_f32_16x16x32_bf16 v[200:203], a[8:11], v[96:99], v[200:203]\n"
"ds_read_b128 a[24:27], v227 offset:4096\n"
"ds_read_b128 a[28:31], v227 offset:4608\n"
"v_exp_f32 v217, v217\n"
"v_fmac_f32 v184, s80, v188\n"
"v_mfma_f32_16x16x32_bf16 v[200:203], a[12:15], v[112:115], v[200:203]\n"
"v_exp_f32 v220, v220\n"
"v_cvt_pk_bf16_f32 v154, v216, v217\n"
"v_mfma_f32_16x16x32_bf16 v[204:207], a[12:15], v[116:119], v[204:207]\n"
"v_fmac_f32 v185, s80, v189\n"
"v_exp_f32 v221, v221\n"
"v_mfma_f32_16x16x32_bf16 v[204:207], a[8:11], v[100:103], v[204:207]\n"
"v_fmac_f32 v186, s81, v188\n"
"v_exp_f32 v218, v218\n"
"v_mfma_f32_16x16x32_bf16 v[204:207], a[4:7], v[84:87], v[204:207]\n"
"v_exp_f32 v219, v219\n"
"s_nop 0\n"
"v_cvt_pk_bf16_f32 v155, v218, v219\n"
"v_fmac_f32 v187, s81, v189\n"
"v_mfma_f32_16x16x32_bf16 v[204:207], a[0:3], v[68:71], v[204:207]\n"
"v_cvt_pk_bf16_f32 v158, v220, v221\n"
"v_exp_f32 v222, v222\n"
"v_exp_f32 v223, v223\n"
"s_nop 0\n"
"v_cvt_pk_bf16_f32 v159, v222, v223\n"
"v_lshlrev_b32 v188, 16, v253\n"
"v_mfma_f32_16x16x32_bf16 a[112:115], v[152:155], a[120:123], a[112:115]\n"
"ds_read_b128 a[32:35], v227 offset:2048\n"
"ds_read_b128 a[36:39], v227 offset:2560\n"
"v_mfma_f32_16x16x32_bf16 a[116:119], v[156:159], a[120:123], a[116:119]\n"
"s_waitcnt lgkmcnt(2)\n"
"ds_read_b128 a[40:43], v227 offset:6144\n"
"v_and_b32 v189, 0xffff0000, v253\n"
"v_mfma_f32_16x16x32_bf16 v[208:211], a[16:19], v[0:3], v[228:231]\n"
"ds_read_b128 a[44:47], v227 offset:6656\n"
"v_fmac_f32 v184, s20, v188\n"
"v_mfma_f32_16x16x32_bf16 v[208:211], a[20:23], v[16:19], v[208:211]\n"
"v_fmac_f32 v185, s20, v189\n"
"v_fmac_f32 v186, s21, v188\n"
"v_mfma_f32_16x16x32_bf16 v[208:211], a[24:27], v[32:35], v[208:211]\n"
"ds_read_b128 a[0:3], v227 offset:8192\n"
"v_fmac_f32 v187, s21, v189\n"
"v_mfma_f32_16x16x32_bf16 v[208:211], a[28:31], v[48:51], v[208:211]\n"
"ds_read_b128 a[4:7], v227 offset:8704\n"
"v_lshlrev_b32 v188, 16, v254\n"
"v_mfma_f32_16x16x32_bf16 v[212:215], a[28:31], v[52:55], v[232:235]\n"
"ds_read_b128 a[8:11], v227 offset:12288\n"
"v_and_b32 v189, 0xffff0000, v254\n"
"v_mfma_f32_16x16x32_bf16 v[212:215], a[24:27], v[36:39], v[212:215]\n"
"ds_read_b128 a[12:15], v227 offset:12800\n"
"v_fmac_f32 v184, s22, v188\n"
"v_mfma_f32_16x16x32_bf16 v[212:215], a[20:23], v[20:23], v[212:215]\n"
"v_exp_f32 v192, v192\n"
"v_mfma_f32_16x16x32_bf16 v[212:215], a[16:19], v[4:7], v[212:215]\n"
"s_waitcnt lgkmcnt(4)\n"
"v_fmac_f32 v185, s22, v189\n"
"v_fmac_f32 v186, s23, v188\n"
"v_mfma_f32_16x16x32_bf16 v[216:219], a[32:35], v[0:3], v[228:231]\n"
"v_exp_f32 v193, v193\n"
"v_mfma_f32_16x16x32_bf16 v[216:219], a[36:39], v[16:19], v[216:219]\n"
"v_exp_f32 v196, v196\n"
"v_mfma_f32_16x16x32_bf16 v[216:219], a[40:43], v[32:35], v[216:219]\n"
"v_fmac_f32 v187, s23, v189\n"
"v_lshlrev_b32 v188, 16, v255\n"
"v_mfma_f32_16x16x32_bf16 v[216:219], a[44:47], v[48:51], v[216:219]\n"
"ds_read_b128 a[16:19], v227 offset:10240\n"
"v_cvt_pk_bf16_f32 v160, v192, v193\n"
"v_mfma_f32_16x16x32_bf16 v[220:223], a[44:47], v[52:55], v[232:235]\n"
"ds_read_b128 a[20:23], v227 offset:10752\n"
"v_and_b32 v189, 0xffff0000, v255\n"
"v_mfma_f32_16x16x32_bf16 v[220:223], a[40:43], v[36:39], v[220:223]\n"
"v_exp_f32 v197, v197\n"
"v_mfma_f32_16x16x32_bf16 v[220:223], a[36:39], v[20:23], v[220:223]\n"
"ds_read_b128 a[24:27], v227 offset:14336\n"
"v_fmac_f32 v184, s84, v188\n"
"v_mfma_f32_16x16x32_bf16 v[220:223], a[32:35], v[4:7], v[220:223]\n"
"s_waitcnt lgkmcnt(3)\n"
"ds_read_b128 a[28:31], v227 offset:14848\n"
"v_cvt_pk_bf16_f32 v164, v196, v197\n"
"v_mfma_f32_16x16x32_bf16 v[208:211], a[0:3], v[64:67], v[208:211]\n"
"s_waitcnt vmcnt(13)\n"
"s_barrier\n"
"v_exp_f32 v194, v194\n"
"v_fmac_f32 v185, s84, v189\n"
"v_mfma_f32_16x16x32_bf16 v[208:211], a[4:7], v[80:83], v[208:211]\n"
"s_add_u32 m0, s65, 98304\n"
"s_add_u32 s67, s65, 65536\n"
"v_fmac_f32 v186, s85, v188\n"
"v_mfma_f32_16x16x32_bf16 v[208:211], a[8:11], v[96:99], v[208:211]\n"
"v_exp_f32 v195, v195\n"
"v_mfma_f32_16x16x32_bf16 v[208:211], a[12:15], v[112:115], v[208:211]\n"
"v_exp_f32 v198, v198\n"
"v_fmac_f32 v187, s85, v189\n"
"v_mfma_f32_16x16x32_bf16 v[212:215], a[12:15], v[116:119], v[212:215]\n"
"v_cvt_pk_bf16_f32 v176, v184, v185\n"
"v_cvt_pk_bf16_f32 v161, v194, v195\n"
"v_mfma_f32_16x16x32_bf16 v[212:215], a[8:11], v[100:103], v[212:215]\n"
"v_exp_f32 v199, v199\n"
"v_cvt_pk_bf16_f32 v178, v186, v187\n"
"v_mfma_f32_16x16x32_bf16 v[212:215], a[4:7], v[84:87], v[212:215]\n"
"ds_read_b128 a[32:35], v227 offset:16384\n"
"v_mov_b32 v177, s86\n"
"v_mfma_f32_16x16x32_bf16 v[212:215], a[0:3], v[68:71], v[212:215]\n"
"s_waitcnt lgkmcnt(1)\n"
"v_exp_f32 v200, v200\n"
"v_mfma_f32_16x16x32_bf16 v[216:219], a[16:19], v[64:67], v[216:219]\n"
"buffer_load_dwordx4 v224, s[44:47], s67 offen offset:0 sc1 lds\n"
"v_cvt_pk_bf16_f32 v165, v198, v199\n"
"v_mov_b32 v179, s87\n"
"v_mfma_f32_16x16x32_bf16 v[216:219], a[20:23], v[80:83], v[216:219]\n"
"ds_read_b128 a[36:39], v227 offset:16896\n"
"ds_read_b128 a[40:43], v227 offset:20480\n"
"v_lshlrev_b32 v190, 2, v190\n"
"v_mfma_f32_16x16x32_bf16 v[216:219], a[24:27], v[96:99], v[216:219]\n"
"s_lshl_b32 s68, s5, 12\n"
"ds_read_b128 a[44:47], v227 offset:20992\n"
"v_exp_f32 v201, v201\n"
"v_mfma_f32_16x16x32_bf16 v[216:219], a[28:31], v[112:115], v[216:219]\n"
"s_add_u32 s68, s68, 1048576\n"
"v_exp_f32 v204, v204\n"
"v_cvt_pk_bf16_f32 v162, v200, v201\n"
"v_mfma_f32_16x16x32_bf16 v[220:223], a[28:31], v[116:119], v[220:223]\n"
"v_exp_f32 v205, v205\n"
"v_mfma_f32_16x16x32_bf16 v[220:223], a[24:27], v[100:103], v[220:223]\n"
"buffer_load_dwordx4 v224, s[44:47], s67 offen offset:1024 sc1 lds\n"
"buffer_store_dwordx4 v[176:179], v190, s[48:51], s68 offen offset:0 sc1\n"
"v_mfma_f32_16x16x32_bf16 v[220:223], a[20:23], v[84:87], v[220:223]\n"
"v_exp_f32 v202, v202\n"
"v_exp_f32 v203, v203\n"
"s_nop 0\n"
"v_cvt_pk_bf16_f32 v163, v202, v203\n"
"v_mfma_f32_16x16x32_bf16 v[220:223], a[16:19], v[68:71], v[220:223]\n"
"v_cvt_pk_bf16_f32 v166, v204, v205\n"
"v_exp_f32 v206, v206\n"
"v_exp_f32 v207, v207\n"
"s_nop 0\n"
"v_cvt_pk_bf16_f32 v167, v206, v207\n"
"v_mfma_f32_16x16x32_bf16 a[112:115], v[160:163], a[120:123], a[112:115]\n"
"ds_read_b128 a[0:3], v224 offset:16384\n"
"ds_read_b128 a[4:7], v224 offset:20480\n"
"v_mfma_f32_16x16x32_bf16 a[116:119], v[164:167], a[120:123], a[116:119]\n"
"s_waitcnt lgkmcnt(2)\n"
"buffer_load_dwordx4 v224, s[44:47], s67 offen offset:2048 sc1 lds\n"
"v_mfma_f32_16x16x32_bf16 v[192:195], a[32:35], v[0:3], v[228:231]\n"
"ds_read_b128 a[8:11], v227 offset:18432\n"
"v_bfe_u32 v248, v224, 4, 2\n"
"v_mfma_f32_16x16x32_bf16 v[192:195], a[36:39], v[16:19], v[192:195]\n"
"s_lshl_b32 s70, s31, 2\n"
"s_add_u32 s70, s70, s2\n"
"ds_read_b128 a[12:15], v227 offset:18944\n"
"v_mfma_f32_16x16x32_bf16 v[192:195], a[40:43], v[32:35], v[192:195]\n"
"ds_read_b128 a[16:19], v227 offset:22528\n"
"v_add_u32 v248, s70, v248\n"
"v_mfma_f32_16x16x32_bf16 v[192:195], a[44:47], v[48:51], v[192:195]\n"
"buffer_load_dwordx4 v224, s[44:47], s67 offen offset:3072 sc1 lds\n"
"v_mfma_f32_16x16x32_bf16 v[196:199], a[44:47], v[52:55], v[232:235]\n"
"ds_read_b128 a[20:23], v227 offset:23040\n"
"v_lshrrev_b32 v236, 8, v224\n"
"v_lshlrev_b32 v236, 3, v236\n"
"v_mfma_f32_16x16x32_bf16 v[196:199], a[40:43], v[36:39], v[196:199]\n"
"s_add_u32 m0, s65, 114688\n"
"s_add_u32 s67, s65, 81920\n"
"v_bfe_u32 v237, v224, 6, 1\n"
"v_mfma_f32_16x16x32_bf16 v[196:199], a[36:39], v[20:23], v[196:199]\n"
"v_lshl_add_u32 v236, v237, 5, v236\n"
"v_and_b32 v237, 128, v224\n"
"v_mfma_f32_16x16x32_bf16 v[196:199], a[32:35], v[4:7], v[196:199]\n"
"s_waitcnt lgkmcnt(4)\n"
"ds_read_b128 a[24:27], v224 offset:17408\n"
"v_add_u32 v236, v237, v236\n"
"v_mfma_f32_16x16x32_bf16 a[128:131], v[8:11], a[0:3], 0\n"
"s_and_b32 s68, s95, 1\n"
"s_lshl_b32 s68, s68, 6\n"
"v_add_u32 v236, s68, v236\n"
"v_mfma_f32_16x16x32_bf16 a[128:131], v[24:27], a[4:7], a[128:131]\n"
"v_exp_f32 v208, v208\n"
"v_mfma_f32_16x16x32_bf16 a[144:147], v[28:31], a[4:7], 0\n"
"buffer_load_dwordx4 v224, s[44:47], s67 offen offset:0 sc1 lds\n"
"v_lshlrev_b32 v237, 1, v236\n"
"v_mfma_f32_16x16x32_bf16 a[144:147], v[12:15], a[0:3], a[144:147]\n"
"s_waitcnt lgkmcnt(1)\n"
"v_cmp_eq_u32_e64 s[72:73], s90, v248\n"
"v_cmp_eq_u32_e64 s[74:75], s91, v248\n"
"v_mfma_f32_16x16x32_bf16 v[200:203], a[8:11], v[0:3], v[228:231]\n"
"ds_read_b128 a[28:31], v224 offset:21504\n"
"v_add_u32 v249, 2048, v237\n"
"v_exp_f32 v212, v212\n"
"v_mfma_f32_16x16x32_bf16 v[200:203], a[12:15], v[16:19], v[200:203]\n"
"v_cndmask_b32_e64 v237, v237, v249, s[74:75]\n"
"v_exp_f32 v209, v209\n"
"v_mfma_f32_16x16x32_bf16 v[200:203], a[16:19], v[32:35], v[200:203]\n"
"s_or_b64 s[72:73], s[72:73], s[74:75]\n"
"ds_read_b128 a[32:35], v227 offset:24576\n"
"ds_read_b128 a[36:39], v227 offset:25088\n"
"v_mov_b32 v249, 1073741824\n"
"v_mfma_f32_16x16x32_bf16 v[200:203], a[20:23], v[48:51], v[200:203]\n"
"buffer_load_dwordx4 v224, s[44:47], s67 offen offset:1024 sc1 lds\n"
"v_cndmask_b32_e64 v237, v249, v237, s[72:73]\n"
"v_mfma_f32_16x16x32_bf16 v[204:207], a[20:23], v[52:55], v[232:235]\n"
"ds_read_b128 a[40:43], v227 offset:28672\n"
"ds_read_b128 a[44:47], v227 offset:29184\n"
"v_mul_u32_u24 v249, 896, v248\n"
"v_exp_f32 v213, v213\n"
"v_add_u32 v236, v249, v236\n"
"v_mfma_f32_16x16x32_bf16 v[204:207], a[16:19], v[36:39], v[204:207]\n"
"v_lshlrev_b32 v248, 3, v248\n"
"v_cvt_pk_bf16_f32 v168, v208, v209\n"
"v_mfma_f32_16x16x32_bf16 v[204:207], a[12:15], v[20:23], v[204:207]\n"
"s_mov_b32 s68, 32768\n"
"s_mov_b32 s80, 65536\n"
"v_cvt_pk_bf16_f32 v172, v212, v213\n"
"v_mfma_f32_16x16x32_bf16 v[204:207], a[8:11], v[4:7], v[204:207]\n"
"s_waitcnt lgkmcnt(4)\n"
"s_mov_b32 s60, s28\n"
"s_and_b32 s61, s29, 0xffff\n"
"v_mfma_f32_16x16x32_bf16 a[132:135], v[8:11], a[24:27], 0\n"
"s_mov_b32 s62, 1835008\n"
"buffer_load_dwordx4 v224, s[44:47], s67 offen offset:2048 sc1 lds\n"
"v_mfma_f32_16x16x32_bf16 a[132:135], v[24:27], a[28:31], a[132:135]\n"
"s_mov_b32 s63, 0x20000\n"
"v_exp_f32 v210, v210\n"
"v_mfma_f32_16x16x32_bf16 a[148:151], v[28:31], a[28:31], 0\n"
"ds_read_b128 a[0:3], v224 offset:18432\n"
"ds_read_b128 a[4:7], v224 offset:22528\n"
"v_mfma_f32_16x16x32_bf16 a[148:151], v[12:15], a[24:27], a[148:151]\n"
"s_waitcnt lgkmcnt(2)\n"
"v_exp_f32 v214, v214\n"
"v_mfma_f32_16x16x32_bf16 v[192:195], a[32:35], v[64:67], v[192:195]\n"
"ds_read_b128 a[8:11], v227 offset:26624\n"
"ds_read_b128 a[12:15], v227 offset:27136\n"
"v_mfma_f32_16x16x32_bf16 v[192:195], a[36:39], v[80:83], v[192:195]\n"
"buffer_load_dwordx4 v224, s[44:47], s67 offen offset:3072 sc1 lds\n"
"buffer_load_dwordx2 a[64:65], v248, s[48:51], s68 offen offset:0 sc1\n"
"v_exp_f32 v211, v211\n"
"v_mfma_f32_16x16x32_bf16 v[192:195], a[40:43], v[96:99], v[192:195]\n"
"ds_read_b128 a[16:19], v227 offset:30720\n"
"ds_read_b128 a[20:23], v227 offset:31232\n"
"v_mfma_f32_16x16x32_bf16 v[192:195], a[44:47], v[112:115], v[192:195]\n"
"v_exp_f32 v215, v215\n"
"v_mfma_f32_16x16x32_bf16 v[196:199], a[44:47], v[116:119], v[196:199]\n"
"v_cvt_pk_bf16_f32 v169, v210, v211\n"
"v_cvt_pk_bf16_f32 v173, v214, v215\n"
"v_mfma_f32_16x16x32_bf16 v[196:199], a[40:43], v[100:103], v[196:199]\n"
"buffer_load_dwordx2 a[56:57], v236, s[32:35], 0 offen offset:0\n"
"v_mfma_f32_16x16x32_bf16 v[196:199], a[36:39], v[84:87], v[196:199]\n"
"v_exp_f32 v216, v216\n"
"v_mfma_f32_16x16x32_bf16 v[196:199], a[32:35], v[68:71], v[196:199]\n"
"s_waitcnt lgkmcnt(4)\n"
"ds_read_b128 a[24:27], v224 offset:19456\n"
"ds_read_b128 a[28:31], v224 offset:23552\n"
"v_mfma_f32_16x16x32_bf16 a[136:139], v[8:11], a[0:3], 0\n"
"buffer_load_dwordx2 a[58:59], v236, s[32:35], 0 offen offset:256\n"
"v_mfma_f32_16x16x32_bf16 a[136:139], v[24:27], a[4:7], a[136:139]\n"
"v_exp_f32 v217, v217\n"
"v_mfma_f32_16x16x32_bf16 a[152:155], v[28:31], a[4:7], 0\n"
"v_exp_f32 v220, v220\n"
"v_mfma_f32_16x16x32_bf16 a[152:155], v[12:15], a[0:3], a[152:155]\n"
"s_waitcnt lgkmcnt(2)\n"
"ds_read_b128 a[32:35], v225 offset:0\n"
"ds_read_b128 a[36:39], v225 offset:512\n"
"v_mfma_f32_16x16x32_bf16 v[200:203], a[8:11], v[64:67], v[200:203]\n"
"ds_read_b128 a[40:43], v225 offset:4096\n"
"v_cvt_pk_bf16_f32 v170, v216, v217\n"
"v_mfma_f32_16x16x32_bf16 v[200:203], a[12:15], v[80:83], v[200:203]\n"
"v_exp_f32 v221, v221\n"
"v_mfma_f32_16x16x32_bf16 v[200:203], a[16:19], v[96:99], v[200:203]\n"
"buffer_load_dwordx2 a[60:61], v236, s[32:35], 0 offen offset:512\n"
"v_mfma_f32_16x16x32_bf16 v[200:203], a[20:23], v[112:115], v[200:203]\n"
"ds_read_b128 a[44:47], v225 offset:4608\n"
"v_cvt_pk_bf16_f32 v174, v220, v221\n"
"v_mfma_f32_16x16x32_bf16 v[204:207], a[20:23], v[116:119], v[204:207]\n"
"v_exp_f32 v218, v218\n"
"v_mfma_f32_16x16x32_bf16 v[204:207], a[16:19], v[100:103], v[204:207]\n"
"v_exp_f32 v222, v222\n"
"v_mfma_f32_16x16x32_bf16 v[204:207], a[12:15], v[84:87], v[204:207]\n"
"v_exp_f32 v219, v219\n"
"v_mfma_f32_16x16x32_bf16 v[204:207], a[8:11], v[68:71], v[204:207]\n"
"s_waitcnt lgkmcnt(4)\n"
"buffer_load_dwordx2 a[62:63], v236, s[32:35], 0 offen offset:768\n"
"v_mfma_f32_16x16x32_bf16 a[140:143], v[8:11], a[24:27], 0\n"
"v_exp_f32 v223, v223\n"
"v_mfma_f32_16x16x32_bf16 a[140:143], v[24:27], a[28:31], a[140:143]\n"
"v_cvt_pk_bf16_f32 v171, v218, v219\n"
"v_cvt_pk_bf16_f32 v175, v222, v223\n"
"v_mfma_f32_16x16x32_bf16 a[156:159], v[28:31], a[28:31], 0\n"
"s_lshl_b32 s97, s5, 4\n"
"ds_read_b128 a[0:3], v224 offset:24576\n"
"ds_read_b128 a[4:7], v224 offset:28672\n"
"v_mfma_f32_16x16x32_bf16 a[156:159], v[12:15], a[24:27], a[156:159]\n"
"s_add_u32 s97, s97, 1048576\n"
"ds_read_b128 a[8:11], v225 offset:2048\n"
"ds_read_b128 a[12:15], v225 offset:2560\n"
"v_mfma_f32_16x16x32_bf16 a[112:115], v[168:171], a[120:123], a[112:115]\n"
"s_lshl_b32 s70, s31, 8\n"
"ds_read_b128 a[16:19], v225 offset:6144\n"
"ds_read_b128 a[20:23], v225 offset:6656\n"
"v_mfma_f32_16x16x32_bf16 a[116:119], v[172:175], a[120:123], a[116:119]\n"
"s_waitcnt lgkmcnt(6)\n"
"v_exp_f32 v192, v192\n"
"v_mfma_f32_16x16x32_bf16 v[208:211], a[32:35], v[0:3], v[228:231]\n"
"s_add_u32 s70, s70, s5\n"
"v_exp_f32 v196, v196\n"
"v_mfma_f32_16x16x32_bf16 v[208:211], a[36:39], v[16:19], v[208:211]\n"
"v_exp_f32 v193, v193\n"
"v_mfma_f32_16x16x32_bf16 v[208:211], a[40:43], v[32:35], v[208:211]\n"
"ds_read_b128 a[24:27], v224 offset:25600\n"
"v_cvt_pk_bf16_f32 v176, v192, v193\n"
"v_mfma_f32_16x16x32_bf16 v[208:211], a[44:47], v[48:51], v[208:211]\n"
"ds_read_b128 a[28:31], v224 offset:29696\n"
"v_mov_b32 v253, 0x5d800000\n"
"v_mfma_f32_16x16x32_bf16 v[212:215], a[44:47], v[52:55], v[232:235]\n"
"s_lshl_b32 s68, s31, 2\n"
"v_exp_f32 v197, v197\n"
"v_mfma_f32_16x16x32_bf16 v[212:215], a[40:43], v[36:39], v[212:215]\n"
"s_add_u32 s68, s68, 32768\n"
"v_cvt_pk_bf16_f32 v180, v196, v197\n"
"v_mov_b32 v250, s68\n"
"v_mfma_f32_16x16x32_bf16 v[212:215], a[36:39], v[20:23], v[212:215]\n"
"v_exp_f32 v194, v194\n"
"v_mfma_f32_16x16x32_bf16 v[212:215], a[32:35], v[4:7], v[212:215]\n"
"s_waitcnt lgkmcnt(6)\n"
"ds_read_b128 a[32:35], v225 offset:8192\n"
"ds_read_b128 a[36:39], v225 offset:8704\n"
"v_mfma_f32_16x16x32_bf16 a[128:131], v[40:43], a[0:3], a[128:131]\n"
"ds_read_b128 a[40:43], v225 offset:12288\n"
"ds_read_b128 a[44:47], v225 offset:12800\n"
"v_mfma_f32_16x16x32_bf16 a[128:131], v[56:59], a[4:7], a[128:131]\n"
"v_exp_f32 v195, v195\n"
"v_mfma_f32_16x16x32_bf16 a[144:147], v[60:63], a[4:7], a[144:147]\n"
"v_exp_f32 v198, v198\n"
"v_mfma_f32_16x16x32_bf16 a[144:147], v[44:47], a[0:3], a[144:147]\n"
"s_waitcnt lgkmcnt(6)\n"
"ds_read_b128 a[0:3], v224 offset:26624\n"
"v_cvt_pk_bf16_f32 v177, v194, v195\n"
"v_mfma_f32_16x16x32_bf16 v[216:219], a[8:11], v[0:3], v[228:231]\n"
"ds_read_b128 a[4:7], v224 offset:30720\n"
"v_mov_b32 v254, 32768\n"
"v_mfma_f32_16x16x32_bf16 v[216:219], a[12:15], v[16:19], v[216:219]\n"
"v_exp_f32 v199, v199\n"
"v_mfma_f32_16x16x32_bf16 v[216:219], a[16:19], v[32:35], v[216:219]\n"
"v_cvt_pk_bf16_f32 v181, v198, v199\n"
"v_mfma_f32_16x16x32_bf16 v[216:219], a[20:23], v[48:51], v[216:219]\n"
"v_exp_f32 v200, v200\n"
"v_mfma_f32_16x16x32_bf16 v[220:223], a[20:23], v[52:55], v[232:235]\n"
"v_exp_f32 v204, v204\n"
"v_mfma_f32_16x16x32_bf16 v[220:223], a[16:19], v[36:39], v[220:223]\n"
"v_exp_f32 v201, v201\n"
"v_mfma_f32_16x16x32_bf16 v[220:223], a[12:15], v[20:23], v[220:223]\n"
"v_cvt_pk_bf16_f32 v178, v200, v201\n"
"v_mfma_f32_16x16x32_bf16 v[220:223], a[8:11], v[4:7], v[220:223]\n"
"s_waitcnt lgkmcnt(6)\n"
"ds_read_b128 a[8:11], v225 offset:10240\n"
"ds_read_b128 a[12:15], v225 offset:10752\n"
"v_mfma_f32_16x16x32_bf16 a[132:135], v[40:43], a[24:27], a[132:135]\n"
"ds_read_b128 a[16:19], v225 offset:14336\n"
"ds_read_b128 a[20:23], v225 offset:14848\n"
"v_mfma_f32_16x16x32_bf16 a[132:135], v[56:59], a[28:31], a[132:135]\n"
"v_exp_f32 v205, v205\n"
"v_mfma_f32_16x16x32_bf16 a[148:151], v[60:63], a[28:31], a[148:151]\n"
"v_exp_f32 v202, v202\n"
"v_mfma_f32_16x16x32_bf16 a[148:151], v[44:47], a[24:27], a[148:151]\n"
"s_waitcnt lgkmcnt(6)\n"
"ds_read_b128 a[24:27], v224 offset:27648\n"
"v_cvt_pk_bf16_f32 v182, v204, v205\n"
"v_mfma_f32_16x16x32_bf16 v[208:211], a[32:35], v[64:67], v[208:211]\n"
"ds_read_b128 a[28:31], v224 offset:31744\n"
"v_mfma_f32_16x16x32_bf16 v[208:211], a[36:39], v[80:83], v[208:211]\n"
"v_exp_f32 v206, v206\n"
"v_mfma_f32_16x16x32_bf16 v[208:211], a[40:43], v[96:99], v[208:211]\n"
"v_exp_f32 v203, v203\n"
"v_mfma_f32_16x16x32_bf16 v[208:211], a[44:47], v[112:115], v[208:211]\n"
"v_exp_f32 v207, v207\n"
"v_mfma_f32_16x16x32_bf16 v[212:215], a[44:47], v[116:119], v[212:215]\n"
"v_cvt_pk_bf16_f32 v179, v202, v203\n"
"v_cvt_pk_bf16_f32 v183, v206, v207\n"
"v_mfma_f32_16x16x32_bf16 v[212:215], a[40:43], v[100:103], v[212:215]\n"
"s_cmp_lt_u32 s70, 224\n"
"s_cselect_b32 s97, s97, 1073741824\n"
"s_cselect_b64 s[6:7], 0, -1\n"
"v_mfma_f32_16x16x32_bf16 v[212:215], a[36:39], v[84:87], v[212:215]\n"
"v_lshlrev_b32 v255, 8, v224\n"
"v_mfma_f32_16x16x32_bf16 v[212:215], a[32:35], v[68:71], v[212:215]\n"
"s_waitcnt lgkmcnt(6)\n"
"v_mfma_f32_16x16x32_bf16 a[136:139], v[40:43], a[0:3], a[136:139]\n"
"s_waitcnt vmcnt(14)\n"
"s_barrier\n"
"ds_read_b128 a[32:35], v224 offset:49152\n"
"v_mfma_f32_16x16x32_bf16 a[136:139], v[56:59], a[4:7], a[136:139]\n"
"s_add_u32 m0, s65, 131072\n"
"s_add_u32 s67, s65, 98304\n"
"ds_read_b128 a[36:39], v224 offset:53248\n"
"v_mfma_f32_16x16x32_bf16 a[152:155], v[60:63], a[4:7], a[152:155]\n"
"buffer_load_dwordx4 v224, s[44:47], s67 offen offset:0 sc1 lds\n"
"v_mfma_f32_16x16x32_bf16 a[152:155], v[44:47], a[0:3], a[152:155]\n"
"s_waitcnt lgkmcnt(4)\n"
"ds_read_b128 a[40:43], v224 offset:50176\n"
"v_mfma_f32_16x16x32_bf16 v[216:219], a[8:11], v[64:67], v[216:219]\n"
"IWB6_%=:\n"
"s_cbranch_execz IWB10_%=\n"
"ds_read_b128 a[44:47], v224 offset:54272\n"
"v_mfma_f32_16x16x32_bf16 v[216:219], a[12:15], v[80:83], v[216:219]\n"
"ds_read_b128 a[0:3], v224 offset:51200\n"
"v_mfma_f32_16x16x32_bf16 v[216:219], a[16:19], v[96:99], v[216:219]\n"
"buffer_load_dwordx4 v224, s[44:47], s67 offen offset:1024 sc1 lds\n"
"v_mfma_f32_16x16x32_bf16 v[216:219], a[20:23], v[112:115], v[216:219]\n"
"ds_read_b128 a[4:7], v224 offset:55296\n"
"v_mfma_f32_16x16x32_bf16 v[220:223], a[20:23], v[116:119], v[220:223]\n"
"v_mfma_f32_16x16x32_bf16 v[220:223], a[16:19], v[100:103], v[220:223]\n"
"v_mfma_f32_16x16x32_bf16 v[220:223], a[12:15], v[84:87], v[220:223]\n"
"buffer_load_dwordx4 v224, s[44:47], s67 offen offset:2048 sc1 lds\n"
"v_mfma_f32_16x16x32_bf16 v[220:223], a[8:11], v[68:71], v[220:223]\n"
"s_waitcnt lgkmcnt(6)\n"
"ds_read_b128 a[8:11], v224 offset:52224\n"
"v_mfma_f32_16x16x32_bf16 a[140:143], v[40:43], a[24:27], a[140:143]\n"
"ds_read_b128 a[12:15], v224 offset:56320\n"
"v_mfma_f32_16x16x32_bf16 a[140:143], v[56:59], a[28:31], a[140:143]\n"
"ds_read_b128 a[16:19], v224 offset:57344\n"
"v_mfma_f32_16x16x32_bf16 a[156:159], v[60:63], a[28:31], a[156:159]\n"
"buffer_load_dwordx4 v224, s[44:47], s67 offen offset:3072 sc1 lds\n"
"v_mfma_f32_16x16x32_bf16 a[156:159], v[44:47], a[24:27], a[156:159]\n"
"s_add_u32 m0, s65, 147456\n"
"s_add_u32 s67, s65, 114688\n"
"ds_read_b128 a[20:23], v224 offset:61440\n"
"v_mfma_f32_16x16x32_bf16 a[112:115], v[176:179], a[120:123], a[112:115]\n"
"v_exp_f32 v208, v208\n"
"v_mfma_f32_16x16x32_bf16 a[116:119], v[180:183], a[120:123], a[116:119]\n"
"s_waitcnt lgkmcnt(8)\n"
"v_exp_f32 v212, v212\n"
"v_mfma_f32_16x16x32_bf16 a[128:131], v[72:75], a[32:35], a[128:131]\n"
"buffer_load_dwordx4 v224, s[44:47], s67 offen offset:0 sc1 lds\n"
"v_mfma_f32_16x16x32_bf16 a[128:131], v[88:91], a[36:39], a[128:131]\n"
"ds_read_b128 a[24:27], v224 offset:58368\n"
"v_mfma_f32_16x16x32_bf16 a[144:147], v[92:95], a[36:39], a[144:147]\n"
"ds_read_b128 a[28:31], v224 offset:62464\n"
"v_mfma_f32_16x16x32_bf16 a[144:147], v[76:79], a[32:35], a[144:147]\n"
"s_waitcnt lgkmcnt(8)\n"
"v_exp_f32 v209, v209\n"
"v_mfma_f32_16x16x32_bf16 a[132:135], v[72:75], a[40:43], a[132:135]\n"
"ds_read_b128 a[32:35], v224 offset:59392\n"
"v_cvt_pk_bf16_f32 v184, v208, v209\n"
"v_mfma_f32_16x16x32_bf16 a[132:135], v[88:91], a[44:47], a[132:135]\n"
"buffer_load_dwordx4 v224, s[44:47], s67 offen offset:1024 sc1 lds\n"
"v_mfma_f32_16x16x32_bf16 a[148:151], v[92:95], a[44:47], a[148:151]\n"
"ds_read_b128 a[36:39], v224 offset:63488\n"
"v_mfma_f32_16x16x32_bf16 a[148:151], v[76:79], a[40:43], a[148:151]\n"
"s_waitcnt lgkmcnt(8)\n"
"v_exp_f32 v213, v213\n"
"v_mfma_f32_16x16x32_bf16 a[136:139], v[72:75], a[0:3], a[136:139]\n"
"ds_read_b128 a[40:43], v224 offset:60416\n"
"v_cvt_pk_bf16_f32 v188, v212, v213\n"
"v_mfma_f32_16x16x32_bf16 a[136:139], v[88:91], a[4:7], a[136:139]\n"
"ds_read_b128 a[44:47], v224 offset:64512\n"
"v_mfma_f32_16x16x32_bf16 a[152:155], v[92:95], a[4:7], a[152:155]\n"
"buffer_load_dwordx4 v224, s[44:47], s67 offen offset:2048 sc1 lds\n"
"v_mfma_f32_16x16x32_bf16 a[152:155], v[76:79], a[0:3], a[152:155]\n"
"s_waitcnt lgkmcnt(8)\n"
"ds_read_b128 a[0:3], v239 offset:0\n"
"v_mfma_f32_16x16x32_bf16 a[140:143], v[72:75], a[8:11], a[140:143]\n"
"ds_read_b128 a[4:7], v239 offset:4096\n"
"v_mfma_f32_16x16x32_bf16 a[140:143], v[88:91], a[12:15], a[140:143]\n"
"v_exp_f32 v210, v210\n"
"v_mfma_f32_16x16x32_bf16 a[156:159], v[92:95], a[12:15], a[156:159]\n"
"buffer_load_dwordx4 v224, s[44:47], s67 offen offset:3072 sc1 lds\n"
"v_mfma_f32_16x16x32_bf16 a[156:159], v[76:79], a[8:11], a[156:159]\n"
"s_waitcnt lgkmcnt(8)\n"
"ds_read_b128 a[8:11], v239 offset:1024\n"
"v_mfma_f32_16x16x32_bf16 a[128:131], v[104:107], a[16:19], a[128:131]\n"
"v_exp_f32 v214, v214\n"
"v_mfma_f32_16x16x32_bf16 a[128:131], v[120:123], a[20:23], a[128:131]\n"
"ds_read_b128 a[12:15], v239 offset:5120\n"
"v_mfma_f32_16x16x32_bf16 a[144:147], v[124:127], a[20:23], a[144:147]\n"
"v_exp_f32 v211, v211\n"
"v_mfma_f32_16x16x32_bf16 a[144:147], v[108:111], a[16:19], a[144:147]\n"
"s_waitcnt lgkmcnt(8)\n"
"ds_read_b128 a[16:19], v239 offset:2048\n"
"v_cvt_pk_bf16_f32 v185, v210, v211\n"
"v_mfma_f32_16x16x32_bf16 a[132:135], v[104:107], a[24:27], a[132:135]\n"
"ds_read_b128 a[20:23], v239 offset:6144\n"
"v_mfma_f32_16x16x32_bf16 a[132:135], v[120:123], a[28:31], a[132:135]\n"
"v_exp_f32 v215, v215\n"
"v_mfma_f32_16x16x32_bf16 a[148:151], v[124:127], a[28:31], a[148:151]\n"
"v_cvt_pk_bf16_f32 v189, v214, v215\n"
"v_mfma_f32_16x16x32_bf16 a[148:151], v[108:111], a[24:27], a[148:151]\n"
"s_waitcnt lgkmcnt(8)\n"
"ds_read_b128 a[24:27], v239 offset:3072\n"
"v_mfma_f32_16x16x32_bf16 a[136:139], v[104:107], a[32:35], a[136:139]\n"
"ds_read_b128 a[28:31], v239 offset:7168\n"
"v_mfma_f32_16x16x32_bf16 a[136:139], v[120:123], a[36:39], a[136:139]\n"
"v_exp_f32 v216, v216\n"
"v_mfma_f32_16x16x32_bf16 a[152:155], v[124:127], a[36:39], a[152:155]\n"
"v_exp_f32 v220, v220\n"
"v_mfma_f32_16x16x32_bf16 a[152:155], v[108:111], a[32:35], a[152:155]\n"
"s_waitcnt lgkmcnt(8)\n"
"s_add_u32 s98, s97, 0\n"
"ds_read_b128 a[32:35], v239 offset:8192\n"
"v_mfma_f32_16x16x32_bf16 a[140:143], v[104:107], a[40:43], a[140:143]\n"
"buffer_load_dwordx4 a[88:91], v255, s[48:51], s98 offen offset:0 sc1\n"
"v_mfma_f32_16x16x32_bf16 a[140:143], v[120:123], a[44:47], a[140:143]\n"
"ds_read_b128 a[36:39], v239 offset:12288\n"
"v_mfma_f32_16x16x32_bf16 a[156:159], v[124:127], a[44:47], a[156:159]\n"
"v_exp_f32 v217, v217\n"
"v_mfma_f32_16x16x32_bf16 a[156:159], v[108:111], a[40:43], a[156:159]\n"
"s_waitcnt lgkmcnt(8)\n"
"ds_read_b128 a[40:43], v239 offset:9216\n"
"v_cvt_pk_bf16_f32 v186, v216, v217\n"
"v_mfma_f32_16x16x32_bf16 a[128:131], v[128:131], a[0:3], a[128:131]\n"
"ds_read_b128 a[44:47], v239 offset:13312\n"
"v_mfma_f32_16x16x32_bf16 a[128:131], v[136:139], a[4:7], a[128:131]\n"
"v_exp_f32 v221, v221\n"
"v_mfma_f32_16x16x32_bf16 a[144:147], v[140:143], a[4:7], a[144:147]\n"
"v_cvt_pk_bf16_f32 v190, v220, v221\n"
"v_mfma_f32_16x16x32_bf16 a[144:147], v[132:135], a[0:3], a[144:147]\n"
"s_waitcnt lgkmcnt(8)\n"
"ds_read_b128 a[0:3], v239 offset:10240\n"
"v_mfma_f32_16x16x32_bf16 a[132:135], v[128:131], a[8:11], a[132:135]\n"
"ds_read_b128 a[4:7], v239 offset:14336\n"
"v_mfma_f32_16x16x32_bf16 a[132:135], v[136:139], a[12:15], a[132:135]\n"
"v_exp_f32 v218, v218\n"
"v_mfma_f32_16x16x32_bf16 a[148:151], v[140:143], a[12:15], a[148:151]\n"
"v_exp_f32 v222, v222\n"
"v_mfma_f32_16x16x32_bf16 a[148:151], v[132:135], a[8:11], a[148:151]\n"
"s_waitcnt lgkmcnt(8)\n"
"ds_read_b128 a[8:11], v239 offset:11264\n"
"v_mfma_f32_16x16x32_bf16 a[136:139], v[128:131], a[16:19], a[136:139]\n"
"ds_read_b128 a[12:15], v239 offset:15360\n"
"v_mfma_f32_16x16x32_bf16 a[136:139], v[136:139], a[20:23], a[136:139]\n"
"v_exp_f32 v219, v219\n"
"v_mfma_f32_16x16x32_bf16 a[152:155], v[140:143], a[20:23], a[152:155]\n"
"v_exp_f32 v223, v223\n"
"v_mfma_f32_16x16x32_bf16 a[152:155], v[132:135], a[16:19], a[152:155]\n"
"s_waitcnt lgkmcnt(8)\n"
"s_add_u32 s98, s97, 262144\n"
"ds_read_b128 a[16:19], v239 offset:16384\n"
"v_mfma_f32_16x16x32_bf16 a[140:143], v[128:131], a[24:27], a[140:143]\n"
"buffer_load_dwordx4 a[92:95], v255, s[48:51], s98 offen offset:0 sc1\n"
"v_mfma_f32_16x16x32_bf16 a[140:143], v[136:139], a[28:31], a[140:143]\n"
"ds_read_b128 a[20:23], v239 offset:20480\n"
"v_cvt_pk_bf16_f32 v187, v218, v219\n"
"v_mfma_f32_16x16x32_bf16 a[156:159], v[140:143], a[28:31], a[156:159]\n"
"v_cvt_pk_bf16_f32 v191, v222, v223\n"
"v_mfma_f32_16x16x32_bf16 a[156:159], v[132:135], a[24:27], a[156:159]\n"
"s_waitcnt lgkmcnt(8)\n"
"ds_read_b128 a[24:27], v239 offset:17408\n"
"v_mfma_f32_16x16x32_bf16 a[128:131], v[144:147], a[32:35], a[128:131]\n"
"ds_read_b128 a[28:31], v239 offset:21504\n"
"v_mfma_f32_16x16x32_bf16 a[128:131], v[152:155], a[36:39], a[128:131]\n"
"v_mfma_f32_16x16x32_bf16 a[144:147], v[156:159], a[36:39], a[144:147]\n"
"v_mfma_f32_16x16x32_bf16 a[144:147], v[148:151], a[32:35], a[144:147]\n"
"s_waitcnt lgkmcnt(8)\n"
"ds_read_b128 a[32:35], v239 offset:18432\n"
"v_mfma_f32_16x16x32_bf16 a[132:135], v[144:147], a[40:43], a[132:135]\n"
"ds_read_b128 a[36:39], v239 offset:22528\n"
"v_mfma_f32_16x16x32_bf16 a[132:135], v[152:155], a[44:47], a[132:135]\n"
"v_mfma_f32_16x16x32_bf16 a[148:151], v[156:159], a[44:47], a[148:151]\n"
"v_mfma_f32_16x16x32_bf16 a[148:151], v[148:151], a[40:43], a[148:151]\n"
"s_waitcnt lgkmcnt(8)\n"
"ds_read_b128 a[40:43], v239 offset:19456\n"
"v_mfma_f32_16x16x32_bf16 a[136:139], v[144:147], a[0:3], a[136:139]\n"
"ds_read_b128 a[44:47], v239 offset:23552\n"
"v_mfma_f32_16x16x32_bf16 a[136:139], v[152:155], a[4:7], a[136:139]\n"
"v_mfma_f32_16x16x32_bf16 a[152:155], v[156:159], a[4:7], a[152:155]\n"
"v_mfma_f32_16x16x32_bf16 a[152:155], v[148:151], a[0:3], a[152:155]\n"
"s_waitcnt lgkmcnt(8)\n"
"s_add_u32 s98, s97, 524288\n"
"ds_read_b128 a[0:3], v239 offset:24576\n"
"v_mfma_f32_16x16x32_bf16 a[140:143], v[144:147], a[8:11], a[140:143]\n"
"buffer_load_dwordx4 a[96:99], v255, s[48:51], s98 offen offset:0 sc1\n"
"v_mfma_f32_16x16x32_bf16 a[140:143], v[152:155], a[12:15], a[140:143]\n"
"ds_read_b128 a[4:7], v239 offset:28672\n"
"v_mfma_f32_16x16x32_bf16 a[156:159], v[156:159], a[12:15], a[156:159]\n"
"v_mfma_f32_16x16x32_bf16 a[156:159], v[148:151], a[8:11], a[156:159]\n"
"ds_read_b128 a[8:11], v239 offset:25600\n"
"v_mfma_f32_16x16x32_bf16 a[112:115], v[184:187], a[120:123], a[112:115]\n"
"ds_read_b128 a[12:15], v239 offset:29696\n"
"v_mfma_f32_16x16x32_bf16 a[116:119], v[188:191], a[120:123], a[116:119]\n"
"s_waitcnt lgkmcnt(10)\n"
"v_mfma_f32_16x16x32_bf16 a[128:131], v[160:163], a[16:19], a[128:131]\n"
"v_mfma_f32_16x16x32_bf16 a[128:131], v[168:171], a[20:23], a[128:131]\n"
"v_mfma_f32_16x16x32_bf16 a[144:147], v[172:175], a[20:23], a[144:147]\n"
"v_mfma_f32_16x16x32_bf16 a[144:147], v[164:167], a[16:19], a[144:147]\n"
"s_waitcnt lgkmcnt(8)\n"
"ds_read_b128 a[16:19], v239 offset:26624\n"
"v_mfma_f32_16x16x32_bf16 a[132:135], v[160:163], a[24:27], a[132:135]\n"
"ds_read_b128 a[20:23], v239 offset:30720\n"
"v_accvgpr_read_b32 v248, a112\n"
"v_mfma_f32_16x16x32_bf16 a[132:135], v[168:171], a[28:31], a[132:135]\n"
"v_accvgpr_read_b32 v249, a116\n"
"v_add_f32 v251, v248, v249\n"
"v_mfma_f32_16x16x32_bf16 a[148:151], v[172:175], a[28:31], a[148:151]\n"
"v_accvgpr_read_b32 v248, a113\n"
"v_accvgpr_read_b32 v249, a117\n"
"v_mfma_f32_16x16x32_bf16 a[148:151], v[164:167], a[24:27], a[148:151]\n"
"s_waitcnt lgkmcnt(8)\n"
"v_add_f32 v251, v251, v248\n"
"v_add_f32 v251, v251, v249\n"
"v_mfma_f32_16x16x32_bf16 a[136:139], v[160:163], a[32:35], a[136:139]\n"
"v_accvgpr_read_b32 v248, a114\n"
"v_accvgpr_read_b32 v249, a118\n"
"v_mfma_f32_16x16x32_bf16 a[136:139], v[168:171], a[36:39], a[136:139]\n"
"s_add_u32 s98, s97, 786432\n"
"v_add_f32 v251, v251, v248\n"
"v_add_f32 v251, v251, v249\n"
"v_mfma_f32_16x16x32_bf16 a[152:155], v[172:175], a[36:39], a[152:155]\n"
"buffer_load_dwordx4 a[100:103], v255, s[48:51], s98 offen offset:0 sc1\n"
"v_mfma_f32_16x16x32_bf16 a[152:155], v[164:167], a[32:35], a[152:155]\n"
"s_waitcnt lgkmcnt(6)\n"
"v_accvgpr_read_b32 v248, a115\n"
"v_accvgpr_read_b32 v249, a119\n"
"v_mfma_f32_16x16x32_bf16 a[140:143], v[160:163], a[40:43], a[140:143]\n"
"v_add_f32 v251, v251, v248\n"
"v_add_f32 v251, v251, v249\n"
"v_mfma_f32_16x16x32_bf16 a[140:143], v[168:171], a[44:47], a[140:143]\n"
"ds_read_b128 a[24:27], v239 offset:27648\n"
"v_cmp_nlt_f32_e64 s[82:83], v251, v253\n"
"v_mfma_f32_16x16x32_bf16 a[156:159], v[172:175], a[44:47], a[156:159]\n"
"s_or_b32 s70, s82, s83\n"
"ds_read_b128 a[28:31], v239 offset:31744\n"
"v_mov_b32 v252, s70\n"
"v_mfma_f32_16x16x32_bf16 a[156:159], v[164:167], a[40:43], a[156:159]\n"
"s_waitcnt lgkmcnt(6)\n"
"ds_write_b32 v250, v252 offset:0\n"
"v_mfma_f32_16x16x32_bf16 a[128:131], v[176:179], a[0:3], a[128:131]\n"
"v_mfma_f32_16x16x32_bf16 a[128:131], v[184:187], a[4:7], a[128:131]\n"
"v_mfma_f32_16x16x32_bf16 a[144:147], v[188:191], a[4:7], a[144:147]\n"
"v_mfma_f32_16x16x32_bf16 a[144:147], v[180:183], a[0:3], a[144:147]\n"
"s_waitcnt lgkmcnt(5)\n"
"v_mfma_f32_16x16x32_bf16 a[132:135], v[176:179], a[8:11], a[132:135]\n"
"s_waitcnt lgkmcnt(0)\n"
"s_waitcnt vmcnt(8)\n"
"s_barrier\n"
"ds_read_b128 v[248:251], v254 offset:0\n"
"v_mfma_f32_16x16x32_bf16 a[132:135], v[184:187], a[12:15], a[132:135]\n"
"s_add_u32 m0, s65, 0\n"
"ds_read_b128 a[32:35], v239 offset:32768\n"
"ds_read_b128 a[36:39], v239 offset:36864\n"
"v_mfma_f32_16x16x32_bf16 a[148:151], v[188:191], a[12:15], a[148:151]\n"
"s_add_u32 s67, s65, 131072\n"
"ds_read_b128 a[40:43], v239 offset:40960\n"
"ds_read_b128 a[44:47], v239 offset:45056\n"
"v_mfma_f32_16x16x32_bf16 a[148:151], v[180:183], a[8:11], a[148:151]\n"
"buffer_load_dwordx4 v224, s[44:47], s67 offen offset:0 sc1 lds\n"
"v_mfma_f32_16x16x32_bf16 a[136:139], v[176:179], a[16:19], a[136:139]\n"
"ds_read_b128 a[0:3], v239 offset:33792\n"
"ds_read_b128 a[4:7], v239 offset:37888\n"
"v_mfma_f32_16x16x32_bf16 a[136:139], v[184:187], a[20:23], a[136:139]\n"
"ds_read_b128 a[8:11], v239 offset:41984\n"
"ds_read_b128 a[12:15], v239 offset:46080\n"
"v_mfma_f32_16x16x32_bf16 a[152:155], v[188:191], a[20:23], a[152:155]\n"
"v_mfma_f32_16x16x32_bf16 a[152:155], v[180:183], a[16:19], a[152:155]\n"
"s_waitcnt lgkmcnt(8)\n"
"v_or3_b32 v248, v248, v249, v250\n"
"v_mfma_f32_16x16x32_bf16 a[140:143], v[176:179], a[24:27], a[140:143]\n"
"ds_read_b128 a[16:19], v239 offset:34816\n"
"v_or_b32 v248, v248, v251\n"
"v_mfma_f32_16x16x32_bf16 a[140:143], v[184:187], a[28:31], a[140:143]\n"
"ds_read_b128 a[20:23], v239 offset:38912\n"
"v_readfirstlane_b32 s70, v248\n"
"v_mfma_f32_16x16x32_bf16 a[156:159], v[188:191], a[28:31], a[156:159]\n"
"buffer_load_dwordx4 v224, s[44:47], s67 offen offset:1024 sc1 lds\n"
"v_mfma_f32_16x16x32_bf16 a[156:159], v[180:183], a[24:27], a[156:159]\n"
"s_waitcnt lgkmcnt(6)\n"
"buffer_load_dwordx4 v224, s[44:47], s67 offen offset:2048 sc1 lds\n"
"s_cmp_lg_u32 s70, 0\n"
"s_cbranch_scc1 K1SLOW_%=\n"
"K1BACK_%=:\n"
"v_mfma_f32_16x16x32_bf16 v[64:67], v[8:11], a[32:35], 0\n"
"v_accvgpr_read_b32 v242, a112\n"
"v_accvgpr_read_b32 v243, a113\n"
"v_accvgpr_read_b32 v246, a114\n"
"v_mfma_f32_16x16x32_bf16 v[64:67], v[24:27], a[36:39], v[64:67]\n"
"v_accvgpr_read_b32 v247, a115\n"
"v_rcp_f32 v242, v242\n"
"v_mfma_f32_16x16x32_bf16 v[64:67], v[40:43], a[40:43], v[64:67]\n"
"s_ashr_i32 s69, s88, 31\n"
"v_rcp_f32 v243, v243\n"
"v_mfma_f32_16x16x32_bf16 v[64:67], v[56:59], a[44:47], v[64:67]\n"
"s_not_b32 s69, s69\n"
"v_rcp_f32 v246, v246\n"
"v_mfma_f32_16x16x32_bf16 v[96:99], v[60:63], a[44:47], 0\n"
"v_rcp_f32 v247, v247\n"
"v_accvgpr_read_b32 v225, a116\n"
"v_mfma_f32_16x16x32_bf16 v[96:99], v[44:47], a[40:43], v[96:99]\n"
"v_accvgpr_read_b32 v238, a117\n"
"v_accvgpr_read_b32 v252, a118\n"
"v_accvgpr_read_b32 v253, a119\n"
"v_mfma_f32_16x16x32_bf16 v[96:99], v[28:31], a[36:39], v[96:99]\n"
"s_or_b32 s69, s69, 0x80000000\n"
"v_rcp_f32 v225, v225\n"
"v_mfma_f32_16x16x32_bf16 v[96:99], v[12:15], a[32:35], v[96:99]\n"
"s_waitcnt lgkmcnt(2)\n"
"ds_read_b128 a[24:27], v239 offset:43008\n"
"v_rcp_f32 v238, v238\n"
"v_mfma_f32_16x16x32_bf16 v[68:71], v[8:11], a[0:3], 0\n"
"ds_read_b128 a[28:31], v239 offset:47104\n"
"v_rcp_f32 v252, v252\n"
"v_mfma_f32_16x16x32_bf16 v[68:71], v[24:27], a[4:7], v[68:71]\n"
"s_xor_b32 s69, s88, s69\n"
"buffer_load_dwordx4 v224, s[44:47], s67 offen offset:3072 sc1 lds\n"
"v_mfma_f32_16x16x32_bf16 v[68:71], v[40:43], a[8:11], v[68:71]\n"
"v_rcp_f32 v253, v253\n"
"v_lshrrev_b32 v248, 8, v224\n"
"v_mfma_f32_16x16x32_bf16 v[68:71], v[56:59], a[12:15], v[68:71]\n"
"s_add_u32 m0, s65, 16384\n"
"s_add_u32 s67, s65, 147456\n"
"v_and_b32 v249, 255, v224\n"
"v_lshl_add_u32 v248, v248, 10, v249\n"
"v_mfma_f32_16x16x32_bf16 v[100:103], v[60:63], a[12:15], 0\n"
"v_lshrrev_b32 v249, 4, v249\n"
"v_lshlrev_b32 v249, 3, v249\n"
"v_mfma_f32_16x16x32_bf16 v[100:103], v[44:47], a[8:11], v[100:103]\n"
"ds_read_b128 a[32:35], v239 offset:35840\n"
"v_mov_b32 v251, s87\n"
"v_mfma_f32_16x16x32_bf16 v[100:103], v[28:31], a[4:7], v[100:103]\n"
"ds_read_b128 a[36:39], v239 offset:39936\n"
"v_mov_b32 v250, s86\n"
"v_mfma_f32_16x16x32_bf16 v[100:103], v[12:15], a[0:3], v[100:103]\n"
"s_waitcnt lgkmcnt(2)\n"
"s_add_u32 s68, s93, 0\n"
"v_accvgpr_read_b32 v0, a128\n"
"v_accvgpr_read_b32 v1, a129\n"
"v_mfma_f32_16x16x32_bf16 v[80:83], v[8:11], a[16:19], 0\n"
"buffer_load_dwordx4 v224, s[44:47], s67 offen offset:0 sc1 lds\n"
"ds_read_b128 a[40:43], v239 offset:44032\n"
"ds_read_b128 a[44:47], v239 offset:48128\n"
"v_mfma_f32_16x16x32_bf16 v[80:83], v[24:27], a[20:23], v[80:83]\n"
"v_accvgpr_read_b32 v2, a130\n"
"v_accvgpr_read_b32 v3, a131\n"
"v_mfma_f32_16x16x32_bf16 v[80:83], v[40:43], a[24:27], v[80:83]\n"
"v_accvgpr_read_b32 v4, a132\n"
"v_accvgpr_read_b32 v5, a133\n"
"v_mfma_f32_16x16x32_bf16 v[80:83], v[56:59], a[28:31], v[80:83]\n"
"buffer_load_dwordx4 v224, s[44:47], s67 offen offset:1024 sc1 lds\n"
"v_accvgpr_read_b32 v6, a134\n"
"v_mfma_f32_16x16x32_bf16 v[112:115], v[60:63], a[28:31], 0\n"
"v_accvgpr_read_b32 v7, a135\n"
"v_mul_f32 v0, v242, v0\n"
"v_mfma_f32_16x16x32_bf16 v[112:115], v[44:47], a[24:27], v[112:115]\n"
"ds_read_b128 a[0:3], v239 offset:49152\n"
"v_mul_f32 v1, v243, v1\n"
"v_mfma_f32_16x16x32_bf16 v[112:115], v[28:31], a[20:23], v[112:115]\n"
"ds_read_b128 a[4:7], v239 offset:53248\n"
"v_mul_f32 v2, v246, v2\n"
"v_mfma_f32_16x16x32_bf16 v[112:115], v[12:15], a[16:19], v[112:115]\n"
"s_waitcnt lgkmcnt(2)\n"
"buffer_load_dwordx4 v224, s[44:47], s67 offen offset:2048 sc1 lds\n"
"v_mul_f32 v3, v247, v3\n"
"v_mfma_f32_16x16x32_bf16 v[84:87], v[8:11], a[32:35], 0\n"
"ds_read_b128 a[8:11], v239 offset:57344\n"
"ds_read_b128 a[12:15], v239 offset:61440\n"
"v_mul_f32 v4, v242, v4\n"
"v_mfma_f32_16x16x32_bf16 v[84:87], v[24:27], a[36:39], v[84:87]\n"
"v_mul_f32 v5, v243, v5\n"
"v_mul_f32 v6, v246, v6\n"
"v_mfma_f32_16x16x32_bf16 v[84:87], v[40:43], a[40:43], v[84:87]\n"
"v_mul_f32 v7, v247, v7\n"
"v_cvt_pk_bf16_f32 v0, v0, v1\n"
"v_mfma_f32_16x16x32_bf16 v[84:87], v[56:59], a[44:47], v[84:87]\n"
"buffer_load_dwordx4 v224, s[44:47], s67 offen offset:3072 sc1 lds\n"
"v_cvt_pk_bf16_f32 v1, v2, v3\n"
"v_mfma_f32_16x16x32_bf16 v[116:119], v[60:63], a[44:47], 0\n"
"s_add_u32 m0, s65, 49152\n"
"v_cvt_pk_bf16_f32 v2, v4, v5\n"
"v_cvt_pk_bf16_f32 v3, v6, v7\n"
"v_mfma_f32_16x16x32_bf16 v[116:119], v[44:47], a[40:43], v[116:119]\n"
"s_add_u32 s67, s65, 163840\n"
"ds_read_b128 a[16:19], v239 offset:50176\n"
"ds_read_b128 a[20:23], v239 offset:54272\n"
"v_mfma_f32_16x16x32_bf16 v[116:119], v[28:31], a[36:39], v[116:119]\n"
"buffer_store_dwordx4 v[0:3], v224, s[52:55], s68 offen offset:0 sc1\n"
"v_mfma_f32_16x16x32_bf16 v[116:119], v[12:15], a[32:35], v[116:119]\n"
"s_waitcnt lgkmcnt(2)\n"
"ds_read_b128 a[24:27], v239 offset:58368\n"
"ds_read_b128 a[28:31], v239 offset:62464\n"
"v_mfma_f32_16x16x32_bf16 v[64:67], v[72:75], a[0:3], v[64:67]\n"
"s_add_u32 s68, s93, 32768\n"
"v_accvgpr_read_b32 v32, a144\n"
"v_accvgpr_read_b32 v33, a145\n"
"v_mfma_f32_16x16x32_bf16 v[64:67], v[88:91], a[4:7], v[64:67]\n"
"v_accvgpr_read_b32 v34, a146\n"
"v_accvgpr_read_b32 v35, a147\n"
"v_mfma_f32_16x16x32_bf16 v[64:67], v[104:107], a[8:11], v[64:67]\n"
"ds_read_b128 a[32:35], v239 offset:51200\n"
"v_accvgpr_read_b32 v36, a148\n"
"v_mfma_f32_16x16x32_bf16 v[64:67], v[120:123], a[12:15], v[64:67]\n"
"ds_read_b128 a[36:39], v239 offset:55296\n"
"v_accvgpr_read_b32 v37, a149\n"
"v_mfma_f32_16x16x32_bf16 v[96:99], v[124:127], a[12:15], v[96:99]\n"
"buffer_load_dwordx4 v224, s[44:47], s67 offen offset:0 sc1 lds\n"
"v_mfma_f32_16x16x32_bf16 v[96:99], v[108:111], a[8:11], v[96:99]\n"
"v_accvgpr_read_b32 v38, a150\n"
"v_accvgpr_read_b32 v39, a151\n"
"v_mfma_f32_16x16x32_bf16 v[96:99], v[92:95], a[4:7], v[96:99]\n"
"ds_read_b128 a[40:43], v239 offset:59392\n"
"v_mul_f32 v32, v225, v32\n"
"v_mfma_f32_16x16x32_bf16 v[96:99], v[76:79], a[0:3], v[96:99]\n"
"s_waitcnt lgkmcnt(3)\n"
"ds_read_b128 a[44:47], v239 offset:63488\n"
"v_mul_f32 v33, v238, v33\n"
"v_mfma_f32_16x16x32_bf16 v[68:71], v[72:75], a[16:19], v[68:71]\n"
"v_mul_f32 v34, v252, v34\n"
"v_mul_f32 v35, v253, v35\n"
"v_mfma_f32_16x16x32_bf16 v[68:71], v[88:91], a[20:23], v[68:71]\n"
"buffer_load_dwordx4 v224, s[44:47], s67 offen offset:1024 sc1 lds\n"
"v_mfma_f32_16x16x32_bf16 v[68:71], v[104:107], a[24:27], v[68:71]\n"
"v_mul_f32 v36, v225, v36\n"
"v_mul_f32 v37, v238, v37\n"
"v_mfma_f32_16x16x32_bf16 v[68:71], v[120:123], a[28:31], v[68:71]\n"
"v_mul_f32 v38, v252, v38\n"
"v_mul_f32 v39, v253, v39\n"
"v_mfma_f32_16x16x32_bf16 v[100:103], v[124:127], a[28:31], v[100:103]\n"
"ds_read_b128 a[0:3], v239 offset:52224\n"
"v_cvt_pk_bf16_f32 v32, v32, v33\n"
"v_mfma_f32_16x16x32_bf16 v[100:103], v[108:111], a[24:27], v[100:103]\n"
"ds_read_b128 a[4:7], v239 offset:56320\n"
"v_cvt_pk_bf16_f32 v33, v34, v35\n"
"v_mfma_f32_16x16x32_bf16 v[100:103], v[92:95], a[20:23], v[100:103]\n"
"buffer_load_dwordx4 v224, s[44:47], s67 offen offset:2048 sc1 lds\n"
"v_mfma_f32_16x16x32_bf16 v[100:103], v[76:79], a[16:19], v[100:103]\n"
"s_waitcnt lgkmcnt(2)\n"
"ds_read_b128 a[8:11], v239 offset:60416\n"
"v_cvt_pk_bf16_f32 v34, v36, v37\n"
"v_mfma_f32_16x16x32_bf16 v[80:83], v[72:75], a[32:35], v[80:83]\n"
"ds_read_b128 a[12:15], v239 offset:64512\n"
"v_cvt_pk_bf16_f32 v35, v38, v39\n"
"v_mfma_f32_16x16x32_bf16 v[80:83], v[88:91], a[36:39], v[80:83]\n"
"ds_read_b128 a[16:19], v240 offset:0\n"
"ds_read_b128 a[20:23], v240 offset:4096\n"
"v_mfma_f32_16x16x32_bf16 v[80:83], v[104:107], a[40:43], v[80:83]\n"
"buffer_load_dwordx4 v224, s[44:47], s67 offen offset:3072 sc1 lds\n"
"v_mfma_f32_16x16x32_bf16 v[80:83], v[120:123], a[44:47], v[80:83]\n"
"s_add_u32 m0, s65, 65536\n"
"s_add_u32 s67, s65, 180224\n"
"ds_read_b128 a[24:27], v240 offset:8192\n"
"v_mfma_f32_16x16x32_bf16 v[112:115], v[124:127], a[44:47], v[112:115]\n"
"ds_read_b128 a[28:31], v240 offset:12288\n"
"v_mov_b32 v232, s69\n"
"v_mfma_f32_16x16x32_bf16 v[112:115], v[108:111], a[40:43], v[112:115]\n"
"s_ashr_i32 s69, s89, 31\n"
"s_not_b32 s69, s69\n"
"s_or_b32 s69, s69, 0x80000000\n"
"v_mfma_f32_16x16x32_bf16 v[112:115], v[92:95], a[36:39], v[112:115]\n"
"s_xor_b32 s69, s89, s69\n"
"buffer_store_dwordx4 v[32:35], v224, s[52:55], s68 offen offset:0 sc1\n"
"v_mfma_f32_16x16x32_bf16 v[112:115], v[76:79], a[32:35], v[112:115]\n"
"s_waitcnt lgkmcnt(4)\n"
"s_add_u32 s68, s93, 16384\n"
"v_accvgpr_read_b32 v16, a136\n"
"v_accvgpr_read_b32 v17, a137\n"
"v_mfma_f32_16x16x32_bf16 v[84:87], v[72:75], a[0:3], v[84:87]\n"
"buffer_load_dwordx4 v224, s[44:47], s67 offen offset:0 sc1 lds\n"
"v_accvgpr_read_b32 v18, a138\n"
"v_mfma_f32_16x16x32_bf16 v[84:87], v[88:91], a[4:7], v[84:87]\n"
"v_accvgpr_read_b32 v19, a139\n"
"v_accvgpr_read_b32 v20, a140\n"
"v_accvgpr_read_b32 v21, a141\n"
"v_mfma_f32_16x16x32_bf16 v[84:87], v[104:107], a[8:11], v[84:87]\n"
"v_accvgpr_read_b32 v22, a142\n"
"v_accvgpr_read_b32 v23, a143\n"
"v_mfma_f32_16x16x32_bf16 v[84:87], v[120:123], a[12:15], v[84:87]\n"
"v_mul_f32 v16, v242, v16\n"
"v_mul_f32 v17, v243, v17\n"
"v_mfma_f32_16x16x32_bf16 v[116:119], v[124:127], a[12:15], v[116:119]\n"
"ds_read_b128 a[32:35], v240 offset:1024\n"
"v_mul_f32 v18, v246, v18\n"
"v_mfma_f32_16x16x32_bf16 v[116:119], v[108:111], a[8:11], v[116:119]\n"
"buffer_load_dwordx4 v224, s[44:47], s67 offen offset:1024 sc1 lds\n"
"v_mfma_f32_16x16x32_bf16 v[116:119], v[92:95], a[4:7], v[116:119]\n"
"ds_read_b128 a[36:39], v240 offset:5120\n"
"v_mul_f32 v19, v247, v19\n"
"v_mul_f32 v20, v242, v20\n"
"v_mfma_f32_16x16x32_bf16 v[116:119], v[76:79], a[0:3], v[116:119]\n"
"s_waitcnt lgkmcnt(2)\n"
"ds_read_b128 a[40:43], v240 offset:9216\n"
"v_mul_f32 v21, v243, v21\n"
"v_mfma_f32_16x16x32_bf16 v[64:67], v[128:131], a[16:19], v[64:67]\n"
"ds_read_b128 a[44:47], v240 offset:13312\n"
"v_mul_f32 v22, v246, v22\n"
"v_mfma_f32_16x16x32_bf16 v[64:67], v[136:139], a[20:23], v[64:67]\n"
"v_mul_f32 v23, v247, v23\n"
"v_cvt_pk_bf16_f32 v16, v16, v17\n"
"v_mfma_f32_16x16x32_bf16 v[64:67], v[144:147], a[24:27], v[64:67]\n"
"buffer_load_dwordx4 v224, s[44:47], s67 offen offset:2048 sc1 lds\n"
"v_cvt_pk_bf16_f32 v17, v18, v19\n"
"v_mfma_f32_16x16x32_bf16 v[64:67], v[152:155], a[28:31], v[64:67]\n"
"v_cvt_pk_bf16_f32 v18, v20, v21\n"
"v_cvt_pk_bf16_f32 v19, v22, v23\n"
"v_mfma_f32_16x16x32_bf16 v[96:99], v[156:159], a[28:31], v[96:99]\n"
"ds_read_b128 a[0:3], v240 offset:2048\n"
"ds_read_b128 a[4:7], v240 offset:6144\n"
"v_mfma_f32_16x16x32_bf16 v[96:99], v[148:151], a[24:27], v[96:99]\n"
"ds_read_b128 a[8:11], v240 offset:10240\n"
"ds_read_b128 a[12:15], v240 offset:14336\n"
"v_mfma_f32_16x16x32_bf16 v[96:99], v[140:143], a[20:23], v[96:99]\n"
"v_mov_b32 v233, s69\n"
"v_mfma_f32_16x16x32_bf16 v[96:99], v[132:135], a[16:19], v[96:99]\n"
"s_waitcnt lgkmcnt(4)\n"
"buffer_store_dwordx4 v[16:19], v224, s[52:55], s68 offen offset:0 sc1\n"
"buffer_load_dwordx4 v224, s[44:47], s67 offen offset:3072 sc1 lds\n"
"v_mfma_f32_16x16x32_bf16 v[68:71], v[128:131], a[32:35], v[68:71]\n"
"s_add_u32 s68, s93, 49152\n"
"v_accvgpr_read_b32 v48, a152\n"
"v_accvgpr_read_b32 v49, a153\n"
"v_mfma_f32_16x16x32_bf16 v[68:71], v[136:139], a[36:39], v[68:71]\n"
"s_mov_b32 s82, 0xffff0000\n"
"v_accvgpr_read_b32 v50, a154\n"
"v_accvgpr_read_b32 v51, a155\n"
"v_mfma_f32_16x16x32_bf16 v[68:71], v[144:147], a[40:43], v[68:71]\n"
"s_mov_b32 s83, 0\n"
"v_accvgpr_read_b32 v52, a156\n"
"v_accvgpr_read_b32 v53, a157\n"
"v_mfma_f32_16x16x32_bf16 v[68:71], v[152:155], a[44:47], v[68:71]\n"
"ds_read_b128 a[16:19], v240 offset:3072\n"
"v_accvgpr_read_b32 v54, a158\n"
"v_mfma_f32_16x16x32_bf16 v[100:103], v[156:159], a[44:47], v[100:103]\n"
"ds_read_b128 a[20:23], v240 offset:7168\n"
"v_accvgpr_read_b32 v55, a159\n"
"v_mfma_f32_16x16x32_bf16 v[100:103], v[148:151], a[40:43], v[100:103]\n"
"ds_read_b128 a[24:27], v240 offset:11264\n"
"v_mul_f32 v48, v225, v48\n"
"v_mfma_f32_16x16x32_bf16 v[100:103], v[140:143], a[36:39], v[100:103]\n"
"v_mul_f32 v49, v238, v49\n"
"v_mul_f32 v50, v252, v50\n"
"v_mfma_f32_16x16x32_bf16 v[100:103], v[132:135], a[32:35], v[100:103]\n"
"s_waitcnt lgkmcnt(3)\n"
"ds_read_b128 a[28:31], v240 offset:15360\n"
"v_cndmask_b32_e64 v232, v232, v233, s[82:83]\n"
"v_mfma_f32_16x16x32_bf16 v[80:83], v[128:131], a[0:3], v[80:83]\n"
"s_waitcnt vmcnt(11)\n"
"s_barrier\n"
"v_mul_f32 v51, v253, v51\n"
"v_mfma_f32_16x16x32_bf16 v[80:83], v[136:139], a[4:7], v[80:83]\n"
"s_add_u32 m0, s65, 32768\n"
"v_cvt_pk_bf16_f32 v232, v232, v232\n"
"v_mul_f32 v52, v225, v52\n"
"v_mfma_f32_16x16x32_bf16 v[80:83], v[144:147], a[8:11], v[80:83]\n"
"s_add_u32 s67, s65, 196608\n"
"v_mul_f32 v53, v238, v53\n"
"v_lshlrev_b32 v232, 16, v232\n"
"v_mfma_f32_16x16x32_bf16 v[80:83], v[152:155], a[12:15], v[80:83]\n"
"v_mul_f32 v54, v252, v54\n"
"v_mul_f32 v232, 0xbfb8aa3b, v232\n"
"v_mfma_f32_16x16x32_bf16 v[112:115], v[156:159], a[12:15], v[112:115]\n"
"v_mul_f32 v55, v253, v55\n"
"v_exp_f32 v232, v232\n"
"v_mfma_f32_16x16x32_bf16 v[112:115], v[148:151], a[8:11], v[112:115]\n"
"v_cvt_pk_bf16_f32 v48, v48, v49\n"
"v_cvt_pk_bf16_f32 v49, v50, v51\n"
"v_add_f32 v232, 1.0, v232\n"
"v_mfma_f32_16x16x32_bf16 v[112:115], v[140:143], a[4:7], v[112:115]\n"
"ds_read_b128 a[32:35], v240 offset:16384\n"
"ds_read_b128 a[36:39], v240 offset:20480\n"
"v_cvt_pk_bf16_f32 v50, v52, v53\n"
"v_mfma_f32_16x16x32_bf16 v[112:115], v[132:135], a[0:3], v[112:115]\n"
"s_waitcnt lgkmcnt(2)\n"
"v_cvt_pk_bf16_f32 v51, v54, v55\n"
"v_rcp_f32 v232, v232\n"
"v_mfma_f32_16x16x32_bf16 v[84:87], v[128:131], a[16:19], v[84:87]\n"
"ds_read_b128 a[40:43], v240 offset:24576\n"
"ds_read_b128 a[44:47], v240 offset:28672\n"
"buffer_store_dwordx4 v[48:51], v224, s[52:55], s68 offen offset:0 sc1\n"
"v_mfma_f32_16x16x32_bf16 v[84:87], v[136:139], a[20:23], v[84:87]\n"
"v_and_b32 v233, 752, v224\n"
"v_min_u32 v233, 1, v233\n"
"v_lshlrev_b32 v233, 30, v233\n"
"v_lshl_add_u32 v233, v224, 3, v233\n"
"v_mfma_f32_16x16x32_bf16 v[84:87], v[144:147], a[24:27], v[84:87]\n"
"ds_read_b128 a[0:3], v240 offset:17408\n"
"v_accvgpr_read_b32 v192, a89\n"
"v_mfma_f32_16x16x32_bf16 v[84:87], v[152:155], a[28:31], v[84:87]\n"
"ds_read_b128 a[4:7], v240 offset:21504\n"
"ds_read_b128 a[8:11], v240 offset:25600\n"
"v_mfma_f32_16x16x32_bf16 v[116:119], v[156:159], a[28:31], v[116:119]\n"
"ds_read_b128 a[12:15], v240 offset:29696\n"
"v_accvgpr_read_b32 v193, a91\n"
"v_mfma_f32_16x16x32_bf16 v[116:119], v[148:151], a[24:27], v[116:119]\n"
"v_accvgpr_read_b32 v194, a93\n"
"v_accvgpr_read_b32 v195, a95\n"
"v_mfma_f32_16x16x32_bf16 v[116:119], v[140:143], a[20:23], v[116:119]\n"
"IWB7_%=:\n"
"s_cbranch_execz IRB_%=\n"
"v_accvgpr_read_b32 v196, a97\n"
"v_accvgpr_read_b32 v197, a99\n"
"v_mfma_f32_16x16x32_bf16 v[116:119], v[132:135], a[16:19], v[116:119]\n"
"s_waitcnt lgkmcnt(4)\n"
"ds_read_b128 a[16:19], v240 offset:18432\n"
"ds_read_b128 a[20:23], v240 offset:22528\n"
"v_mfma_f32_16x16x32_bf16 v[64:67], v[160:163], a[32:35], v[64:67]\n"
"ds_read_b128 a[24:27], v240 offset:26624\n"
"v_accvgpr_read_b32 v198, a101\n"
"v_mfma_f32_16x16x32_bf16 v[64:67], v[168:171], a[36:39], v[64:67]\n"
"ds_read_b128 a[28:31], v240 offset:30720\n"
"v_accvgpr_read_b32 v199, a103\n"
"v_mfma_f32_16x16x32_bf16 v[64:67], v[176:179], a[40:43], v[64:67]\n"
"v_cmp_eq_u32_e64 s[84:85], s86, v192\n"
"v_cmp_eq_u32_e64 s[82:83], s87, v193\n"
"v_mfma_f32_16x16x32_bf16 v[64:67], v[184:187], a[44:47], v[64:67]\n"
"s_and_b64 s[84:85], s[84:85], s[82:83]\n"
"v_cmp_eq_u32_e64 s[82:83], s86, v194\n"
"v_mfma_f32_16x16x32_bf16 v[96:99], v[188:191], a[44:47], v[96:99]\n"
"s_and_b64 s[84:85], s[84:85], s[82:83]\n"
"v_cmp_eq_u32_e64 s[82:83], s87, v195\n"
"v_mfma_f32_16x16x32_bf16 v[96:99], v[180:183], a[40:43], v[96:99]\n"
"s_and_b64 s[84:85], s[84:85], s[82:83]\n"
"v_cmp_eq_u32_e64 s[82:83], s86, v196\n"
"v_mfma_f32_16x16x32_bf16 v[96:99], v[172:175], a[36:39], v[96:99]\n"
"s_and_b64 s[84:85], s[84:85], s[82:83]\n"
"v_cmp_eq_u32_e64 s[82:83], s87, v197\n"
"v_mfma_f32_16x16x32_bf16 v[96:99], v[164:167], a[32:35], v[96:99]\n"
"s_waitcnt lgkmcnt(4)\n"
"ds_read_b128 a[32:35], v240 offset:19456\n"
"ds_read_b128 a[36:39], v240 offset:23552\n"
"v_mfma_f32_16x16x32_bf16 v[68:71], v[160:163], a[0:3], v[68:71]\n"
"s_and_b64 s[84:85], s[84:85], s[82:83]\n"
"ds_read_b128 a[40:43], v240 offset:27648\n"
"ds_read_b128 a[44:47], v240 offset:31744\n"
"v_mfma_f32_16x16x32_bf16 v[68:71], v[168:171], a[4:7], v[68:71]\n"
"v_cmp_eq_u32_e64 s[82:83], s86, v198\n"
"v_mfma_f32_16x16x32_bf16 v[68:71], v[176:179], a[8:11], v[68:71]\n"
"s_and_b64 s[84:85], s[84:85], s[82:83]\n"
"v_cmp_eq_u32_e64 s[82:83], s87, v199\n"
"v_mfma_f32_16x16x32_bf16 v[68:71], v[184:187], a[12:15], v[68:71]\n"
"s_and_b64 s[84:85], s[84:85], s[82:83]\n"
"s_or_b64 s[84:85], s[84:85], s[6:7]\n"
"v_mfma_f32_16x16x32_bf16 v[100:103], v[188:191], a[12:15], v[100:103]\n"
"s_cmp_eq_u64 s[84:85], -1\n"
"s_cbranch_scc0 SPB_%=\n"
"SBB_%=:\n"
"v_mfma_f32_16x16x32_bf16 v[100:103], v[180:183], a[8:11], v[100:103]\n"
"buffer_load_dwordx4 v224, s[44:47], s67 offen offset:0 sc1 lds\n"
"v_mfma_f32_16x16x32_bf16 v[100:103], v[172:175], a[4:7], v[100:103]\n"
"v_mfma_f32_16x16x32_bf16 v[100:103], v[164:167], a[0:3], v[100:103]\n"
"s_waitcnt lgkmcnt(4)\n"
"ds_read_b128 a[0:3], v224 offset:0\n"
"ds_read_b128 a[4:7], v224 offset:4096\n"
"v_mfma_f32_16x16x32_bf16 v[80:83], v[160:163], a[16:19], v[80:83]\n"
"ds_read_b128 a[8:11], v224 offset:8192\n"
"ds_read_b128 a[12:15], v224 offset:12288\n"
"v_mfma_f32_16x16x32_bf16 v[80:83], v[168:171], a[20:23], v[80:83]\n"
"buffer_load_dwordx4 v224, s[44:47], s67 offen offset:1024 sc1 lds\n"
"v_mfma_f32_16x16x32_bf16 v[80:83], v[176:179], a[24:27], v[80:83]\n"
"v_mfma_f32_16x16x32_bf16 v[80:83], v[184:187], a[28:31], v[80:83]\n"
"buffer_load_dwordx4 v224, s[44:47], s67 offen offset:2048 sc1 lds\n"
"v_mfma_f32_16x16x32_bf16 v[112:115], v[188:191], a[28:31], v[112:115]\n"
"v_mfma_f32_16x16x32_bf16 v[112:115], v[180:183], a[24:27], v[112:115]\n"
"v_mfma_f32_16x16x32_bf16 v[112:115], v[172:175], a[20:23], v[112:115]\n"
"v_mfma_f32_16x16x32_bf16 v[112:115], v[164:167], a[16:19], v[112:115]\n"
"s_waitcnt lgkmcnt(4)\n"
"ds_read_b128 a[16:19], v224 offset:1024\n"
"ds_read_b128 a[20:23], v224 offset:5120\n"
"v_mfma_f32_16x16x32_bf16 v[84:87], v[160:163], a[32:35], v[84:87]\n"
"ds_read_b128 a[24:27], v224 offset:9216\n"
"v_mfma_f32_16x16x32_bf16 v[84:87], v[168:171], a[36:39], v[84:87]\n"
"ds_read_b128 a[28:31], v224 offset:13312\n"
"v_mfma_f32_16x16x32_bf16 v[84:87], v[176:179], a[40:43], v[84:87]\n"
"buffer_load_dwordx4 v224, s[44:47], s67 offen offset:3072 sc1 lds\n"
"v_mfma_f32_16x16x32_bf16 v[84:87], v[184:187], a[44:47], v[84:87]\n"
"s_add_u32 m0, s65, 81920\n"
"s_add_u32 s67, s65, 212992\n"
"v_mfma_f32_16x16x32_bf16 v[116:119], v[188:191], a[44:47], v[116:119]\n"
"buffer_load_dwordx4 v224, s[44:47], s67 offen offset:0 sc1 lds\n"
"v_mfma_f32_16x16x32_bf16 v[116:119], v[180:183], a[40:43], v[116:119]\n"
"v_mfma_f32_16x16x32_bf16 v[116:119], v[172:175], a[36:39], v[116:119]\n"
"v_mfma_f32_16x16x32_bf16 v[116:119], v[164:167], a[32:35], v[116:119]\n"
"s_waitcnt lgkmcnt(4)\n"
"s_add_u32 s68, s93, 65536\n"
"v_mul_f32 v64, v242, v64\n"
"v_mfma_f32_16x16x32_bf16 v[0:3], v[8:11], a[0:3], 0\n"
"v_mul_f32 v65, v243, v65\n"
"v_mul_f32 v66, v246, v66\n"
"v_mfma_f32_16x16x32_bf16 v[0:3], v[24:27], a[4:7], v[0:3]\n"
"v_mul_f32 v67, v247, v67\n"
"v_mul_f32 v68, v242, v68\n"
"v_mfma_f32_16x16x32_bf16 v[0:3], v[40:43], a[8:11], v[0:3]\n"
"v_mul_f32 v69, v243, v69\n"
"v_mul_f32 v70, v246, v70\n"
"v_mfma_f32_16x16x32_bf16 v[0:3], v[56:59], a[12:15], v[0:3]\n"
"ds_read_b128 a[32:35], v224 offset:2048\n"
"v_mul_f32 v71, v247, v71\n"
"v_mfma_f32_16x16x32_bf16 v[32:35], v[60:63], a[12:15], 0\n"
"ds_read_b128 a[36:39], v224 offset:6144\n"
"v_cvt_pk_bf16_f32 v64, v64, v65\n"
"v_mfma_f32_16x16x32_bf16 v[32:35], v[44:47], a[8:11], v[32:35]\n"
"v_cvt_pk_bf16_f32 v65, v66, v67\n"
"v_cvt_pk_bf16_f32 v66, v68, v69\n"
"v_mfma_f32_16x16x32_bf16 v[32:35], v[28:31], a[4:7], v[32:35]\n"
"ds_read_b128 a[40:43], v224 offset:10240\n"
"v_cvt_pk_bf16_f32 v67, v70, v71\n"
"v_mfma_f32_16x16x32_bf16 v[32:35], v[12:15], a[0:3], v[32:35]\n"
"ds_read_b128 a[44:47], v224 offset:14336\n"
"v_mfma_f32_16x16x32_bf16 v[228:231], a[48:51], a[88:91], 0\n"
"s_waitcnt lgkmcnt(4)\n"
"buffer_store_dwordx4 v[64:67], v224, s[52:55], s68 offen offset:0 sc1\n"
"v_mfma_f32_16x16x32_bf16 v[4:7], v[8:11], a[16:19], 0\n"
"s_add_u32 s68, s93, 98304\n"
"ds_read_b128 a[0:3], v224 offset:3072\n"
"v_mul_f32 v96, v225, v96\n"
"v_mfma_f32_16x16x32_bf16 v[4:7], v[24:27], a[20:23], v[4:7]\n"
"ds_read_b128 a[4:7], v224 offset:7168\n"
"v_mul_f32 v97, v238, v97\n"
"v_mfma_f32_16x16x32_bf16 v[4:7], v[40:43], a[24:27], v[4:7]\n"
"v_mul_f32 v98, v252, v98\n"
"v_mul_f32 v99, v253, v99\n"
"v_mfma_f32_16x16x32_bf16 v[4:7], v[56:59], a[28:31], v[4:7]\n"
"s_add_u32 s71, s0, 0\n"
"s_waitcnt vmcnt(6)\n"
"ds_read_b128 a[8:11], v224 offset:11264\n"
"v_mfma_f32_16x16x32_bf16 v[36:39], v[60:63], a[28:31], 0\n"
"ds_read_b128 a[12:15], v224 offset:15360\n"
"v_mul_f32 v100, v225, v100\n"
"v_mfma_f32_16x16x32_bf16 v[36:39], v[44:47], a[24:27], v[36:39]\n"
"buffer_store_dwordx2 v[250:251], v241, s[48:51], s71 offen offset:0 sc1\n"
"v_mfma_f32_16x16x32_bf16 v[36:39], v[28:31], a[20:23], v[36:39]\n"
"v_mul_f32 v101, v238, v101\n"
"v_mul_f32 v102, v252, v102\n"
"v_mfma_f32_16x16x32_bf16 v[36:39], v[12:15], a[16:19], v[36:39]\n"
"v_mul_f32 v103, v253, v103\n"
"v_cvt_pk_bf16_f32 v96, v96, v97\n"
"v_mfma_f32_16x16x32_bf16 v[228:231], a[48:51], a[92:95], v[228:231]\n"
"v_cvt_pk_bf16_f32 v97, v98, v99\n"
"v_cvt_pk_bf16_f32 v98, v100, v101\n"
"v_mfma_f32_16x16x32_bf16 v[228:231], a[48:51], a[96:99], v[228:231]\n"
"ds_read_b128 a[16:19], v224 offset:16384\n"
"v_cvt_pk_bf16_f32 v99, v102, v103\n"
"v_mfma_f32_16x16x32_bf16 v[228:231], a[48:51], a[100:103], v[228:231]\n"
"s_waitcnt lgkmcnt(5)\n"
"buffer_store_dwordx4 v[96:99], v224, s[52:55], s68 offen offset:0 sc1\n"
"v_mfma_f32_16x16x32_bf16 v[16:19], v[8:11], a[32:35], 0\n"
"s_add_u32 s68, s93, 81920\n"
"ds_read_b128 a[20:23], v224 offset:20480\n"
"ds_read_b128 a[24:27], v224 offset:24576\n"
"v_mfma_f32_16x16x32_bf16 v[16:19], v[24:27], a[36:39], v[16:19]\n"
"ds_read_b128 a[28:31], v224 offset:28672\n"
"v_mul_f32 v80, v242, v80\n"
"v_mfma_f32_16x16x32_bf16 v[16:19], v[40:43], a[40:43], v[16:19]\n"
"v_add_f32_dpp v228, v228, v228 quad_perm:[1,0,3,2] row_mask:0xf bank_mask:0xf\n"
"v_mul_f32 v81, v243, v81\n"
"v_mfma_f32_16x16x32_bf16 v[16:19], v[56:59], a[44:47], v[16:19]\n"
"v_add_f32_dpp v229, v229, v229 quad_perm:[1,0,3,2] row_mask:0xf bank_mask:0xf\n"
"v_mul_f32 v82, v246, v82\n"
"v_mfma_f32_16x16x32_bf16 v[48:51], v[60:63], a[44:47], 0\n"
"v_mul_f32 v83, v247, v83\n"
"v_add_f32_dpp v228, v228, v228 quad_perm:[2,3,0,1] row_mask:0xf bank_mask:0xf\n"
"v_mfma_f32_16x16x32_bf16 v[48:51], v[44:47], a[40:43], v[48:51]\n"
"v_mul_f32 v84, v242, v84\n"
"v_mul_f32 v85, v243, v85\n"
"v_mfma_f32_16x16x32_bf16 v[48:51], v[28:31], a[36:39], v[48:51]\n"
"v_mul_f32 v86, v246, v86\n"
"v_add_f32_dpp v229, v229, v229 quad_perm:[2,3,0,1] row_mask:0xf bank_mask:0xf\n"
"v_mfma_f32_16x16x32_bf16 v[48:51], v[12:15], a[32:35], v[48:51]\n"
"s_waitcnt lgkmcnt(4)\n"
"ds_read_b128 a[32:35], v224 offset:17408\n"
"ds_read_b128 a[36:39], v224 offset:21504\n"
"v_mfma_f32_16x16x32_bf16 v[20:23], v[8:11], a[0:3], 0\n"
"ds_read_b128 a[40:43], v224 offset:25600\n"
"v_mul_f32 v87, v247, v87\n"
"v_mfma_f32_16x16x32_bf16 v[20:23], v[24:27], a[4:7], v[20:23]\n"
"ds_read_b128 a[44:47], v224 offset:29696\n"
"v_cvt_pk_bf16_f32 v80, v80, v81\n"
"v_mfma_f32_16x16x32_bf16 v[20:23], v[40:43], a[8:11], v[20:23]\n"
"v_cvt_pk_bf16_f32 v81, v82, v83\n"
"v_add_f32_dpp v228, v228, v228 row_half_mirror row_mask:0xf bank_mask:0xf\n"
"v_mfma_f32_16x16x32_bf16 v[20:23], v[56:59], a[12:15], v[20:23]\n"
"v_cvt_pk_bf16_f32 v82, v84, v85\n"
"v_add_f32_dpp v229, v229, v229 row_half_mirror row_mask:0xf bank_mask:0xf\n"
"v_mfma_f32_16x16x32_bf16 v[52:55], v[60:63], a[12:15], 0\n"
"v_cvt_pk_bf16_f32 v83, v86, v87\n"
"v_add_f32_dpp v228, v228, v228 row_mirror row_mask:0xf bank_mask:0xf\n"
"v_mfma_f32_16x16x32_bf16 v[52:55], v[44:47], a[8:11], v[52:55]\n"
"buffer_store_dwordx4 v[80:83], v224, s[52:55], s68 offen offset:0 sc1\n"
"v_mfma_f32_16x16x32_bf16 v[52:55], v[28:31], a[4:7], v[52:55]\n"
"s_add_u32 s68, s93, 114688\n"
"v_add_f32_dpp v229, v229, v229 row_mirror row_mask:0xf bank_mask:0xf\n"
"v_mul_f32 v112, v225, v112\n"
"v_mfma_f32_16x16x32_bf16 v[52:55], v[12:15], a[0:3], v[52:55]\n"
"s_waitcnt lgkmcnt(4)\n"
"ds_read_b128 a[0:3], v224 offset:18432\n"
"ds_read_b128 a[4:7], v224 offset:22528\n"
"v_mfma_f32_16x16x32_bf16 v[0:3], v[72:75], a[16:19], v[0:3]\n"
"ds_read_b128 a[8:11], v224 offset:26624\n"
"ds_read_b128 a[12:15], v224 offset:30720\n"
"v_mfma_f32_16x16x32_bf16 v[0:3], v[88:91], a[20:23], v[0:3]\n"
"v_mul_f32 v228, v228, v232\n"
"v_mul_f32 v113, v238, v113\n"
"v_mfma_f32_16x16x32_bf16 v[0:3], v[104:107], a[24:27], v[0:3]\n"
"v_mul_f32 v114, v252, v114\n"
"v_mul_f32 v229, v229, v232\n"
"v_mfma_f32_16x16x32_bf16 v[0:3], v[120:123], a[28:31], v[0:3]\n"
"buffer_load_dwordx4 v224, s[44:47], s67 offen offset:1024 sc1 lds\n"
"v_mfma_f32_16x16x32_bf16 v[32:35], v[124:127], a[28:31], v[32:35]\n"
"v_mul_f32 v115, v253, v115\n"
"v_mul_f32 v116, v225, v116\n"
"v_mfma_f32_16x16x32_bf16 v[32:35], v[108:111], a[24:27], v[32:35]\n"
"v_cvt_pk_bf16_f32 v228, v228, v229\n"
"v_mul_f32 v117, v238, v117\n"
"v_mfma_f32_16x16x32_bf16 v[32:35], v[92:95], a[20:23], v[32:35]\n"
"v_mov_b32 v229, s86\n"
"v_mul_f32 v118, v252, v118\n"
"v_mfma_f32_16x16x32_bf16 v[32:35], v[76:79], a[16:19], v[32:35]\n"
"s_waitcnt lgkmcnt(4)\n"
"buffer_load_dwordx4 v224, s[44:47], s67 offen offset:2048 sc1 lds\n"
"v_mfma_f32_16x16x32_bf16 v[4:7], v[72:75], a[32:35], v[4:7]\n"
"s_lshl_b32 s69, s5, 3\n"
"ds_read_b128 a[16:19], v224 offset:19456\n"
"ds_read_b128 a[20:23], v224 offset:23552\n"
"v_mfma_f32_16x16x32_bf16 v[4:7], v[88:91], a[36:39], v[4:7]\n"
"s_add_u32 s69, s69, 65536\n"
"ds_read_b128 a[24:27], v224 offset:27648\n"
"v_mul_f32 v119, v253, v119\n"
"v_mfma_f32_16x16x32_bf16 v[4:7], v[104:107], a[40:43], v[4:7]\n"
"s_and_b32 s70, s6, 1073741824\n"
"ds_read_b128 a[28:31], v224 offset:31744\n"
"v_cvt_pk_bf16_f32 v112, v112, v113\n"
"v_mfma_f32_16x16x32_bf16 v[4:7], v[120:123], a[44:47], v[4:7]\n"
"s_add_u32 s69, s69, s70\n"
"buffer_load_dwordx4 v224, s[44:47], s67 offen offset:3072 sc1 lds\n"
"v_mfma_f32_16x16x32_bf16 v[36:39], v[124:127], a[44:47], v[36:39]\n"
"s_add_u32 m0, s65, 98304\n"
"s_add_u32 s67, s65, 229376\n"
"v_cvt_pk_bf16_f32 v113, v114, v115\n"
"v_mfma_f32_16x16x32_bf16 v[36:39], v[108:111], a[40:43], v[36:39]\n"
"v_cvt_pk_bf16_f32 v114, v116, v117\n"
"v_cvt_pk_bf16_f32 v115, v118, v119\n"
"v_mfma_f32_16x16x32_bf16 v[36:39], v[92:95], a[36:39], v[36:39]\n"
"v_mfma_f32_16x16x32_bf16 v[36:39], v[76:79], a[32:35], v[36:39]\n"
"s_waitcnt lgkmcnt(4)\n"
"buffer_store_dwordx4 v[112:115], v224, s[52:55], s68 offen offset:0 sc1\n"
"v_mfma_f32_16x16x32_bf16 v[16:19], v[72:75], a[0:3], v[16:19]\n"
"s_waitcnt vmcnt(9)\n"
"s_barrier\n"
"ds_read_b128 a[32:35], v224 offset:49152\n"
"v_mfma_f32_16x16x32_bf16 v[16:19], v[88:91], a[4:7], v[16:19]\n"
"ds_read_b128 a[36:39], v224 offset:53248\n"
"ds_read_b128 a[40:43], v224 offset:57344\n"
"v_mfma_f32_16x16x32_bf16 v[16:19], v[104:107], a[8:11], v[16:19]\n"
"ds_read_b128 a[44:47], v224 offset:61440\n"
"v_mfma_f32_16x16x32_bf16 v[16:19], v[120:123], a[12:15], v[16:19]\n"
"v_mfma_f32_16x16x32_bf16 v[48:51], v[124:127], a[12:15], v[48:51]\n"
"v_mfma_f32_16x16x32_bf16 v[48:51], v[108:111], a[8:11], v[48:51]\n"
"v_mfma_f32_16x16x32_bf16 v[48:51], v[92:95], a[4:7], v[48:51]\n"
"buffer_load_dwordx4 v224, s[44:47], s67 offen offset:0 sc1 lds\n"
"v_mfma_f32_16x16x32_bf16 v[48:51], v[76:79], a[0:3], v[48:51]\n"
"s_waitcnt lgkmcnt(4)\n"
"buffer_store_dwordx2 v[228:229], v233, s[48:51], s69 offen offset:0 sc1\n"
"v_mfma_f32_16x16x32_bf16 v[20:23], v[72:75], a[16:19], v[20:23]\n"
"ds_read_b128 a[0:3], v224 offset:50176\n"
"ds_read_b128 a[4:7], v224 offset:54272\n"
"v_mfma_f32_16x16x32_bf16 v[20:23], v[88:91], a[20:23], v[20:23]\n"
"ds_read_b128 a[8:11], v224 offset:58368\n"
"ds_read_b128 a[12:15], v224 offset:62464\n"
"v_mfma_f32_16x16x32_bf16 v[20:23], v[104:107], a[24:27], v[20:23]\n"
"v_mfma_f32_16x16x32_bf16 v[20:23], v[120:123], a[28:31], v[20:23]\n"
"buffer_load_dwordx4 v224, s[44:47], s67 offen offset:1024 sc1 lds\n"
"v_mfma_f32_16x16x32_bf16 v[52:55], v[124:127], a[28:31], v[52:55]\n"
"v_mfma_f32_16x16x32_bf16 v[52:55], v[108:111], a[24:27], v[52:55]\n"
"v_mfma_f32_16x16x32_bf16 v[52:55], v[92:95], a[20:23], v[52:55]\n"
"v_mfma_f32_16x16x32_bf16 v[52:55], v[76:79], a[16:19], v[52:55]\n"
"s_waitcnt lgkmcnt(4)\n"
"ds_read_b128 a[16:19], v224 offset:51200\n"
"ds_read_b128 a[20:23], v224 offset:55296\n"
"v_mfma_f32_16x16x32_bf16 v[0:3], v[128:131], a[32:35], v[0:3]\n"
"buffer_load_dwordx4 v224, s[44:47], s67 offen offset:2048 sc1 lds\n"
"v_mfma_f32_16x16x32_bf16 v[0:3], v[136:139], a[36:39], v[0:3]\n"
"ds_read_b128 a[24:27], v224 offset:59392\n"
"ds_read_b128 a[28:31], v224 offset:63488\n"
"v_mfma_f32_16x16x32_bf16 v[0:3], v[144:147], a[40:43], v[0:3]\n"
"v_mfma_f32_16x16x32_bf16 v[0:3], v[152:155], a[44:47], v[0:3]\n"
"v_mfma_f32_16x16x32_bf16 v[32:35], v[156:159], a[44:47], v[32:35]\n"
"v_mfma_f32_16x16x32_bf16 v[32:35], v[148:151], a[40:43], v[32:35]\n"
"buffer_load_dwordx4 v224, s[44:47], s67 offen offset:3072 sc1 lds\n"
"s_add_u32 m0, s65, 114688\n"
"s_add_u32 s67, s65, 245760\n"
"v_mfma_f32_16x16x32_bf16 v[32:35], v[140:143], a[36:39], v[32:35]\n"
"v_mfma_f32_16x16x32_bf16 v[32:35], v[132:135], a[32:35], v[32:35]\n"
"s_waitcnt lgkmcnt(4)\n"
"ds_read_b128 a[32:35], v224 offset:52224\n"
"ds_read_b128 a[36:39], v224 offset:56320\n"
"v_mfma_f32_16x16x32_bf16 v[4:7], v[128:131], a[0:3], v[4:7]\n"
"ds_read_b128 a[40:43], v224 offset:60416\n"
"ds_read_b128 a[44:47], v224 offset:64512\n"
"v_mfma_f32_16x16x32_bf16 v[4:7], v[136:139], a[4:7], v[4:7]\n"
"buffer_load_dwordx4 v224, s[44:47], s67 offen offset:0 sc1 lds\n"
"v_mfma_f32_16x16x32_bf16 v[4:7], v[144:147], a[8:11], v[4:7]\n"
"v_mfma_f32_16x16x32_bf16 v[4:7], v[152:155], a[12:15], v[4:7]\n"
"v_mfma_f32_16x16x32_bf16 v[36:39], v[156:159], a[12:15], v[36:39]\n"
"v_mfma_f32_16x16x32_bf16 v[36:39], v[148:151], a[8:11], v[36:39]\n"
"buffer_load_dwordx4 v224, s[44:47], s67 offen offset:1024 sc1 lds\n"
"v_mfma_f32_16x16x32_bf16 v[36:39], v[140:143], a[4:7], v[36:39]\n"
"v_mfma_f32_16x16x32_bf16 v[36:39], v[132:135], a[0:3], v[36:39]\n"
"s_waitcnt lgkmcnt(4)\n"
"ds_read_b128 a[0:3], v239 offset:0\n"
"ds_read_b128 a[4:7], v239 offset:4096\n"
"v_mfma_f32_16x16x32_bf16 v[16:19], v[128:131], a[16:19], v[16:19]\n"
"ds_read_b128 a[8:11], v239 offset:8192\n"
"ds_read_b128 a[12:15], v239 offset:12288\n"
"v_mfma_f32_16x16x32_bf16 v[16:19], v[136:139], a[20:23], v[16:19]\n"
"buffer_load_dwordx4 v224, s[44:47], s67 offen offset:2048 sc1 lds\n"
"v_mfma_f32_16x16x32_bf16 v[16:19], v[144:147], a[24:27], v[16:19]\n"
"v_mfma_f32_16x16x32_bf16 v[16:19], v[152:155], a[28:31], v[16:19]\n"
"v_mfma_f32_16x16x32_bf16 v[48:51], v[156:159], a[28:31], v[48:51]\n"
"v_mfma_f32_16x16x32_bf16 v[48:51], v[148:151], a[24:27], v[48:51]\n"
"buffer_load_dwordx4 v224, s[44:47], s67 offen offset:3072 sc1 lds\n"
"v_mfma_f32_16x16x32_bf16 v[48:51], v[140:143], a[20:23], v[48:51]\n"
"s_add_u32 m0, s65, 131072\n"
"s_add_u32 s67, s65, 262144\n"
"v_mfma_f32_16x16x32_bf16 v[48:51], v[132:135], a[16:19], v[48:51]\n"
"s_waitcnt lgkmcnt(4)\n"
"ds_read_b128 a[16:19], v239 offset:1024\n"
"ds_read_b128 a[20:23], v239 offset:5120\n"
"v_mfma_f32_16x16x32_bf16 v[20:23], v[128:131], a[32:35], v[20:23]\n"
"ds_read_b128 a[24:27], v239 offset:9216\n"
"ds_read_b128 a[28:31], v239 offset:13312\n"
"v_mfma_f32_16x16x32_bf16 v[20:23], v[136:139], a[36:39], v[20:23]\n"
"buffer_load_dwordx4 v224, s[44:47], s67 offen offset:0 sc1 lds\n"
"v_mfma_f32_16x16x32_bf16 v[20:23], v[144:147], a[40:43], v[20:23]\n"
"v_mfma_f32_16x16x32_bf16 v[20:23], v[152:155], a[44:47], v[20:23]\n"
"v_mfma_f32_16x16x32_bf16 v[52:55], v[156:159], a[44:47], v[52:55]\n"
"v_mfma_f32_16x16x32_bf16 v[52:55], v[148:151], a[40:43], v[52:55]\n"
"buffer_load_dwordx4 v224, s[44:47], s67 offen offset:1024 sc1 lds\n"
"v_mfma_f32_16x16x32_bf16 v[52:55], v[140:143], a[36:39], v[52:55]\n"
"v_mfma_f32_16x16x32_bf16 v[52:55], v[132:135], a[32:35], v[52:55]\n"
"s_waitcnt lgkmcnt(4)\n"
"ds_read_b128 a[32:35], v239 offset:2048\n"
"ds_read_b128 a[36:39], v239 offset:6144\n"
"v_mfma_f32_16x16x32_bf16 v[0:3], v[160:163], a[0:3], v[0:3]\n"
"ds_read_b128 a[40:43], v239 offset:10240\n"
"ds_read_b128 a[44:47], v239 offset:14336\n"
"v_mfma_f32_16x16x32_bf16 v[0:3], v[168:171], a[4:7], v[0:3]\n"
"buffer_load_dwordx4 v224, s[44:47], s67 offen offset:2048 sc1 lds\n"
"v_mfma_f32_16x16x32_bf16 v[0:3], v[176:179], a[8:11], v[0:3]\n"
"s_add_u32 s13, s1, 0\n"
"v_mfma_f32_16x16x32_bf16 v[0:3], v[184:187], a[12:15], v[0:3]\n"
"v_mfma_f32_16x16x32_bf16 v[32:35], v[188:191], a[12:15], v[32:35]\n"
"v_mfma_f32_16x16x32_bf16 v[32:35], v[180:183], a[8:11], v[32:35]\n"
"buffer_load_dwordx2 v[244:245], v249, s[48:51], s13 offen offset:0 sc1\n"
"v_mfma_f32_16x16x32_bf16 v[32:35], v[172:175], a[4:7], v[32:35]\n"
"v_mfma_f32_16x16x32_bf16 v[32:35], v[164:167], a[0:3], v[32:35]\n"
"s_waitcnt lgkmcnt(4)\n"
"ds_read_b128 a[0:3], v239 offset:3072\n"
"ds_read_b128 a[4:7], v239 offset:7168\n"
"v_mfma_f32_16x16x32_bf16 v[4:7], v[160:163], a[16:19], v[4:7]\n"
"ds_read_b128 a[8:11], v239 offset:11264\n"
"ds_read_b128 a[12:15], v239 offset:15360\n"
"v_mfma_f32_16x16x32_bf16 v[4:7], v[168:171], a[20:23], v[4:7]\n"
"buffer_load_dwordx4 v224, s[44:47], s67 offen offset:3072 sc1 lds\n"
"v_mfma_f32_16x16x32_bf16 v[4:7], v[176:179], a[24:27], v[4:7]\n"
"s_add_u32 m0, s65, 147456\n"
"s_add_u32 s67, s65, 278528\n"
"v_mfma_f32_16x16x32_bf16 v[4:7], v[184:187], a[28:31], v[4:7]\n"
"buffer_load_dwordx4 v224, s[44:47], s67 offen offset:0 sc1 lds\n"
"v_mfma_f32_16x16x32_bf16 v[36:39], v[188:191], a[28:31], v[36:39]\n"
"v_mfma_f32_16x16x32_bf16 v[36:39], v[180:183], a[24:27], v[36:39]\n"
"v_mfma_f32_16x16x32_bf16 v[36:39], v[172:175], a[20:23], v[36:39]\n"
"v_mfma_f32_16x16x32_bf16 v[36:39], v[164:167], a[16:19], v[36:39]\n"
"s_waitcnt lgkmcnt(4)\n"
"s_add_u32 s71, s0, 16384\n"
"s_waitcnt vmcnt(15)\n"
"v_mfma_f32_16x16x32_bf16 v[16:19], v[160:163], a[32:35], v[16:19]\n"
"buffer_store_dwordx2 v[250:251], v241, s[48:51], s71 offen offset:0 sc1\n"
"v_mfma_f32_16x16x32_bf16 v[16:19], v[168:171], a[36:39], v[16:19]\n"
"ds_read_b128 a[16:19], v224 offset:32768\n"
"ds_read_b128 a[20:23], v224 offset:36864\n"
"v_mfma_f32_16x16x32_bf16 v[16:19], v[176:179], a[40:43], v[16:19]\n"
"ds_read_b128 a[24:27], v224 offset:40960\n"
"ds_read_b128 a[28:31], v224 offset:45056\n"
"v_mfma_f32_16x16x32_bf16 v[16:19], v[184:187], a[44:47], v[16:19]\n"
"buffer_load_dwordx4 v224, s[44:47], s67 offen offset:1024 sc1 lds\n"
"v_mfma_f32_16x16x32_bf16 v[48:51], v[188:191], a[44:47], v[48:51]\n"
"v_mfma_f32_16x16x32_bf16 v[48:51], v[180:183], a[40:43], v[48:51]\n"
"buffer_load_dwordx4 a[160:163], v237, s[48:51], s80 offen offset:0 sc1\n"
"v_mfma_f32_16x16x32_bf16 v[48:51], v[172:175], a[36:39], v[48:51]\n"
"v_mfma_f32_16x16x32_bf16 v[48:51], v[164:167], a[32:35], v[48:51]\n"
"s_waitcnt lgkmcnt(4)\n"
"ds_read_b128 a[32:35], v224 offset:33792\n"
"ds_read_b128 a[36:39], v224 offset:37888\n"
"v_mfma_f32_16x16x32_bf16 v[20:23], v[160:163], a[0:3], v[20:23]\n"
"ds_read_b128 a[40:43], v224 offset:41984\n"
"ds_read_b128 a[44:47], v224 offset:46080\n"
"v_mfma_f32_16x16x32_bf16 v[20:23], v[168:171], a[4:7], v[20:23]\n"
"buffer_load_dwordx4 v224, s[44:47], s67 offen offset:2048 sc1 lds\n"
"v_mfma_f32_16x16x32_bf16 v[20:23], v[176:179], a[8:11], v[20:23]\n"
"v_mfma_f32_16x16x32_bf16 v[20:23], v[184:187], a[12:15], v[20:23]\n"
"v_mfma_f32_16x16x32_bf16 v[52:55], v[188:191], a[12:15], v[52:55]\n"
"v_mfma_f32_16x16x32_bf16 v[52:55], v[180:183], a[8:11], v[52:55]\n"
"buffer_load_dwordx4 a[164:167], v237, s[48:51], s80 offen offset:512 sc1\n"
"v_mfma_f32_16x16x32_bf16 v[52:55], v[172:175], a[4:7], v[52:55]\n"
"v_mfma_f32_16x16x32_bf16 v[52:55], v[164:167], a[0:3], v[52:55]\n"
"s_waitcnt lgkmcnt(4)\n"
"s_add_u32 s68, s93, 131072\n"
"v_mul_f32 v0, v242, v0\n"
"v_mfma_f32_16x16x32_bf16 v[64:67], v[8:11], a[16:19], 0\n"
"v_mul_f32 v1, v243, v1\n"
"v_mul_f32 v2, v246, v2\n"
"v_mfma_f32_16x16x32_bf16 v[64:67], v[24:27], a[20:23], v[64:67]\n"
"ds_read_b128 a[0:3], v224 offset:34816\n"
"v_mul_f32 v3, v247, v3\n"
"v_mfma_f32_16x16x32_bf16 v[64:67], v[40:43], a[24:27], v[64:67]\n"
"buffer_load_dwordx4 a[168:171], v237, s[48:51], s80 offen offset:1024 sc1\n"
"v_mfma_f32_16x16x32_bf16 v[64:67], v[56:59], a[28:31], v[64:67]\n"
"ds_read_b128 a[4:7], v224 offset:38912\n"
"v_mul_f32 v4, v242, v4\n"
"v_mfma_f32_16x16x32_bf16 v[96:99], v[60:63], a[28:31], 0\n"
"ds_read_b128 a[8:11], v224 offset:43008\n"
"v_mul_f32 v5, v243, v5\n"
"v_mfma_f32_16x16x32_bf16 v[96:99], v[44:47], a[24:27], v[96:99]\n"
"ds_read_b128 a[12:15], v224 offset:47104\n"
"v_mul_f32 v6, v246, v6\n"
"v_mfma_f32_16x16x32_bf16 v[96:99], v[28:31], a[20:23], v[96:99]\n"
"v_mul_f32 v7, v247, v7\n"
"v_cvt_pk_bf16_f32 v0, v0, v1\n"
"v_mfma_f32_16x16x32_bf16 v[96:99], v[12:15], a[16:19], v[96:99]\n"
"s_waitcnt lgkmcnt(4)\n"
"v_cvt_pk_bf16_f32 v1, v2, v3\n"
"v_cvt_pk_bf16_f32 v2, v4, v5\n"
"v_mfma_f32_16x16x32_bf16 v[68:71], v[8:11], a[32:35], 0\n"
"buffer_load_dwordx4 a[172:175], v237, s[48:51], s80 offen offset:1536 sc1\n"
"v_mfma_f32_16x16x32_bf16 v[68:71], v[24:27], a[36:39], v[68:71]\n"
"ds_read_b128 a[16:19], v224 offset:35840\n"
"v_cvt_pk_bf16_f32 v3, v6, v7\n"
"v_mfma_f32_16x16x32_bf16 v[68:71], v[40:43], a[40:43], v[68:71]\n"
"buffer_store_dwordx4 v[0:3], v224, s[52:55], s68 offen offset:0 sc1\n"
"v_mfma_f32_16x16x32_bf16 v[68:71], v[56:59], a[44:47], v[68:71]\n"
"s_add_u32 s68, s93, 163840\n"
"ds_read_b128 a[20:23], v224 offset:39936\n"
"ds_read_b128 a[24:27], v224 offset:44032\n"
"v_mfma_f32_16x16x32_bf16 v[100:103], v[60:63], a[44:47], 0\n"
"ds_read_b128 a[28:31], v224 offset:48128\n"
"v_mul_f32 v32, v225, v32\n"
"v_mfma_f32_16x16x32_bf16 v[100:103], v[44:47], a[40:43], v[100:103]\n"
"v_mul_f32 v33, v238, v33\n"
"v_mul_f32 v34, v252, v34\n"
"v_mfma_f32_16x16x32_bf16 v[100:103], v[28:31], a[36:39], v[100:103]\n"
"v_mul_f32 v35, v253, v35\n"
"v_mul_f32 v36, v225, v36\n"
"v_mfma_f32_16x16x32_bf16 v[100:103], v[12:15], a[32:35], v[100:103]\n"
"s_waitcnt lgkmcnt(4)\n"
"v_mul_f32 v37, v238, v37\n"
"v_mul_f32 v38, v252, v38\n"
"v_mfma_f32_16x16x32_bf16 v[80:83], v[8:11], a[0:3], 0\n"
"s_waitcnt vmcnt(14)\n"
"s_barrier\n"
"ds_read_b128 a[32:35], v239 offset:16384\n"
"v_mfma_f32_16x16x32_bf16 v[80:83], v[24:27], a[4:7], v[80:83]\n"
"ds_read_b128 a[36:39], v239 offset:20480\n"
"v_mul_f32 v39, v253, v39\n"
"v_mfma_f32_16x16x32_bf16 v[80:83], v[40:43], a[8:11], v[80:83]\n"
"buffer_load_dwordx4 v224, s[44:47], s67 offen offset:3072 sc1 lds\n"
"v_mfma_f32_16x16x32_bf16 v[80:83], v[56:59], a[12:15], v[80:83]\n"
"s_add_u32 m0, s65, 0\n"
"s_add_u32 s67, s65, 294912\n"
"v_cvt_pk_bf16_f32 v32, v32, v33\n"
"v_mfma_f32_16x16x32_bf16 v[112:115], v[60:63], a[12:15], 0\n"
"s_waitcnt vmcnt(11)\n"
"ds_read_b128 a[40:43], v239 offset:24576\n"
"v_cvt_pk_bf16_f32 v33, v34, v35\n"
"v_mfma_f32_16x16x32_bf16 v[112:115], v[44:47], a[8:11], v[112:115]\n"
"ds_read_b128 a[44:47], v239 offset:28672\n"
"v_cmp_eq_u32_e64 s[14:15], s86, v244\n"
"v_mfma_f32_16x16x32_bf16 v[112:115], v[28:31], a[4:7], v[112:115]\n"
"v_cvt_pk_bf16_f32 v34, v36, v37\n"
"v_cvt_pk_bf16_f32 v35, v38, v39\n"
"v_mfma_f32_16x16x32_bf16 v[112:115], v[12:15], a[0:3], v[112:115]\n"
"s_waitcnt lgkmcnt(4)\n"
"ds_read_b128 a[0:3], v239 offset:17408\n"
"v_cmp_eq_u32_e64 s[16:17], s87, v245\n"
"v_mfma_f32_16x16x32_bf16 v[84:87], v[8:11], a[16:19], 0\n"
"s_and_b64 s[14:15], s[14:15], s[16:17]\n"
"buffer_store_dwordx4 v[32:35], v224, s[52:55], s68 offen offset:0 sc1\n"
"v_mfma_f32_16x16x32_bf16 v[84:87], v[24:27], a[20:23], v[84:87]\n"
"s_cmp_eq_u64 s[14:15], -1\n"
"s_cbranch_scc0 SPC0_%=\n"
"SBC0_%=:\n"
"s_add_u32 s68, s93, 147456\n"
"v_mfma_f32_16x16x32_bf16 v[84:87], v[40:43], a[24:27], v[84:87]\n"
"s_add_u32 s13, s94, 0\n"
"buffer_load_dwordx4 v224, s[44:47], s67 offen offset:0 sc1 lds\n"
"v_mfma_f32_16x16x32_bf16 v[84:87], v[56:59], a[28:31], v[84:87]\n"
"ds_read_b128 a[4:7], v239 offset:21504\n"
"v_mul_f32 v16, v242, v16\n"
"v_mfma_f32_16x16x32_bf16 v[116:119], v[60:63], a[28:31], 0\n"
"ds_read_b128 a[8:11], v239 offset:25600\n"
"v_mul_f32 v17, v243, v17\n"
"v_mfma_f32_16x16x32_bf16 v[116:119], v[44:47], a[24:27], v[116:119]\n"
"ds_read_b128 a[12:15], v239 offset:29696\n"
"v_mul_f32 v18, v246, v18\n"
"v_mfma_f32_16x16x32_bf16 v[116:119], v[28:31], a[20:23], v[116:119]\n"
"buffer_load_dwordx4 a[192:195], v248, s[52:55], s13 offen offset:0 sc1\n"
"v_mfma_f32_16x16x32_bf16 v[116:119], v[12:15], a[16:19], v[116:119]\n"
"s_waitcnt lgkmcnt(4)\n"
"buffer_load_dwordx4 v224, s[44:47], s67 offen offset:1024 sc1 lds\n"
"v_mfma_f32_16x16x32_bf16 v[64:67], v[72:75], a[32:35], v[64:67]\n"
"s_add_u32 s13, s94, 4096\n"
"v_mul_f32 v19, v247, v19\n"
"v_mul_f32 v20, v242, v20\n"
"v_mfma_f32_16x16x32_bf16 v[64:67], v[88:91], a[36:39], v[64:67]\n"
"buffer_load_dwordx4 a[196:199], v248, s[52:55], s13 offen offset:0 sc1\n"
"v_mfma_f32_16x16x32_bf16 v[64:67], v[104:107], a[40:43], v[64:67]\n"
"s_add_u32 s13, s94, 8192\n"
"v_mul_f32 v21, v243, v21\n"
"v_mul_f32 v22, v246, v22\n"
"v_mfma_f32_16x16x32_bf16 v[64:67], v[120:123], a[44:47], v[64:67]\n"
"v_mul_f32 v23, v247, v23\n"
"v_cvt_pk_bf16_f32 v16, v16, v17\n"
"v_mfma_f32_16x16x32_bf16 v[96:99], v[124:127], a[44:47], v[96:99]\n"
"buffer_load_dwordx4 v224, s[44:47], s67 offen offset:2048 sc1 lds\n"
"v_mfma_f32_16x16x32_bf16 v[96:99], v[108:111], a[40:43], v[96:99]\n"
"buffer_load_dwordx4 a[200:203], v248, s[52:55], s13 offen offset:0 sc1\n"
"v_cvt_pk_bf16_f32 v17, v18, v19\n"
"v_mfma_f32_16x16x32_bf16 v[96:99], v[92:95], a[36:39], v[96:99]\n"
"ds_read_b128 a[16:19], v239 offset:18432\n"
"ds_read_b128 a[20:23], v239 offset:22528\n"
"v_cvt_pk_bf16_f32 v18, v20, v21\n"
"v_mfma_f32_16x16x32_bf16 v[96:99], v[76:79], a[32:35], v[96:99]\n"
"s_waitcnt lgkmcnt(2)\n"
"ds_read_b128 a[24:27], v239 offset:26624\n"
"v_cvt_pk_bf16_f32 v19, v22, v23\n"
"v_mfma_f32_16x16x32_bf16 v[68:71], v[72:75], a[0:3], v[68:71]\n"
"s_add_u32 s13, s94, 12288\n"
"ds_read_b128 a[28:31], v239 offset:30720\n"
"ds_read_b128 a[32:35], v239 offset:19456\n"
"v_mfma_f32_16x16x32_bf16 v[68:71], v[88:91], a[4:7], v[68:71]\n"
"buffer_load_dwordx4 v224, s[44:47], s67 offen offset:3072 sc1 lds\n"
"buffer_load_dwordx4 a[204:207], v248, s[52:55], s13 offen offset:0 sc1\n"
"v_mfma_f32_16x16x32_bf16 v[68:71], v[104:107], a[8:11], v[68:71]\n"
"s_add_u32 m0, s65, 16384\n"
"buffer_store_dwordx4 v[16:19], v224, s[52:55], s68 offen offset:0 sc1\n"
"v_mfma_f32_16x16x32_bf16 v[68:71], v[120:123], a[12:15], v[68:71]\n"
"s_add_u32 s68, s93, 180224\n"
"v_mul_f32 v48, v225, v48\n"
"v_mul_f32 v49, v238, v49\n"
"v_mfma_f32_16x16x32_bf16 v[100:103], v[124:127], a[12:15], v[100:103]\n"
"s_add_u32 s67, s65, 311296\n"
"ds_read_b128 a[36:39], v239 offset:23552\n"
"v_mul_f32 v50, v252, v50\n"
"v_mfma_f32_16x16x32_bf16 v[100:103], v[108:111], a[8:11], v[100:103]\n"
"ds_read_b128 a[40:43], v239 offset:27648\n"
"v_mul_f32 v51, v253, v51\n"
"v_mfma_f32_16x16x32_bf16 v[100:103], v[92:95], a[4:7], v[100:103]\n"
"ds_read_b128 a[44:47], v239 offset:31744\n"
"v_mul_f32 v52, v225, v52\n"
"v_mfma_f32_16x16x32_bf16 v[100:103], v[76:79], a[0:3], v[100:103]\n"
"s_waitcnt lgkmcnt(4)\n"
"v_mul_f32 v53, v238, v53\n"
"v_mul_f32 v54, v252, v54\n"
"v_mfma_f32_16x16x32_bf16 v[80:83], v[72:75], a[16:19], v[80:83]\n"
"v_mul_f32 v55, v253, v55\n"
"v_cvt_pk_bf16_f32 v48, v48, v49\n"
"v_mfma_f32_16x16x32_bf16 v[80:83], v[88:91], a[20:23], v[80:83]\n"
"ds_read_b128 a[0:3], v239 offset:32768\n"
"v_cvt_pk_bf16_f32 v49, v50, v51\n"
"v_mfma_f32_16x16x32_bf16 v[80:83], v[104:107], a[24:27], v[80:83]\n"
"ds_read_b128 a[4:7], v239 offset:36864\n"
"v_cvt_pk_bf16_f32 v50, v52, v53\n"
"v_mfma_f32_16x16x32_bf16 v[80:83], v[120:123], a[28:31], v[80:83]\n"
"ds_read_b128 a[8:11], v239 offset:40960\n"
"v_cvt_pk_bf16_f32 v51, v54, v55\n"
"v_mfma_f32_16x16x32_bf16 v[112:115], v[124:127], a[28:31], v[112:115]\n"
"IWB8_%=:\n"
"s_cbranch_execz IRB_%=\n"
"ds_read_b128 a[12:15], v239 offset:45056\n"
"v_mfma_f32_16x16x32_bf16 v[112:115], v[108:111], a[24:27], v[112:115]\n"
"v_mfma_f32_16x16x32_bf16 v[112:115], v[92:95], a[20:23], v[112:115]\n"
"v_mfma_f32_16x16x32_bf16 v[112:115], v[76:79], a[16:19], v[112:115]\n"
"s_waitcnt lgkmcnt(4)\n"
"buffer_store_dwordx4 v[48:51], v224, s[52:55], s68 offen offset:0 sc1\n"
"v_mfma_f32_16x16x32_bf16 v[84:87], v[72:75], a[32:35], v[84:87]\n"
"ds_read_b128 a[16:19], v239 offset:33792\n"
"ds_read_b128 a[20:23], v239 offset:37888\n"
"v_mfma_f32_16x16x32_bf16 v[84:87], v[88:91], a[36:39], v[84:87]\n"
"ds_read_b128 a[24:27], v239 offset:41984\n"
"ds_read_b128 a[28:31], v239 offset:46080\n"
"v_mfma_f32_16x16x32_bf16 v[84:87], v[104:107], a[40:43], v[84:87]\n"
"v_mfma_f32_16x16x32_bf16 v[84:87], v[120:123], a[44:47], v[84:87]\n"
"v_mfma_f32_16x16x32_bf16 v[116:119], v[124:127], a[44:47], v[116:119]\n"
"v_mfma_f32_16x16x32_bf16 v[116:119], v[108:111], a[40:43], v[116:119]\n"
"v_mfma_f32_16x16x32_bf16 v[116:119], v[92:95], a[36:39], v[116:119]\n"
"v_mfma_f32_16x16x32_bf16 v[116:119], v[76:79], a[32:35], v[116:119]\n"
"s_waitcnt lgkmcnt(4)\n"
"ds_read_b128 a[32:35], v239 offset:34816\n"
"ds_read_b128 a[36:39], v239 offset:38912\n"
"v_mfma_f32_16x16x32_bf16 v[64:67], v[128:131], a[0:3], v[64:67]\n"
"ds_read_b128 a[40:43], v239 offset:43008\n"
"ds_read_b128 a[44:47], v239 offset:47104\n"
"v_mfma_f32_16x16x32_bf16 v[64:67], v[136:139], a[4:7], v[64:67]\n"
"v_mfma_f32_16x16x32_bf16 v[64:67], v[144:147], a[8:11], v[64:67]\n"
"v_mfma_f32_16x16x32_bf16 v[64:67], v[152:155], a[12:15], v[64:67]\n"
"v_mfma_f32_16x16x32_bf16 v[96:99], v[156:159], a[12:15], v[96:99]\n"
"v_mfma_f32_16x16x32_bf16 v[96:99], v[148:151], a[8:11], v[96:99]\n"
"v_accvgpr_read_b32 v208, a56\n"
"v_accvgpr_read_b32 v209, a57\n"
"v_mfma_f32_16x16x32_bf16 v[96:99], v[140:143], a[4:7], v[96:99]\n"
"v_accvgpr_read_b32 v210, a58\n"
"v_accvgpr_read_b32 v211, a59\n"
"v_mfma_f32_16x16x32_bf16 v[96:99], v[132:135], a[0:3], v[96:99]\n"
"s_waitcnt lgkmcnt(4)\n"
"ds_read_b128 a[0:3], v239 offset:35840\n"
"ds_read_b128 a[4:7], v239 offset:39936\n"
"v_mfma_f32_16x16x32_bf16 v[68:71], v[128:131], a[16:19], v[68:71]\n"
"ds_read_b128 a[8:11], v239 offset:44032\n"
"ds_read_b128 a[12:15], v239 offset:48128\n"
"v_mfma_f32_16x16x32_bf16 v[68:71], v[136:139], a[20:23], v[68:71]\n"
"s_add_u32 s13, s1, 16384\n"
"v_accvgpr_read_b32 v212, a60\n"
"v_accvgpr_read_b32 v213, a61\n"
"v_mfma_f32_16x16x32_bf16 v[68:71], v[144:147], a[24:27], v[68:71]\n"
"v_accvgpr_read_b32 v214, a62\n"
"v_accvgpr_read_b32 v215, a63\n"
"v_mfma_f32_16x16x32_bf16 v[68:71], v[152:155], a[28:31], v[68:71]\n"
"v_accvgpr_read_b32 v226, a64\n"
"v_accvgpr_read_b32 v227, a65\n"
"v_mfma_f32_16x16x32_bf16 v[100:103], v[156:159], a[28:31], v[100:103]\n"
"s_waitcnt vmcnt(17)\n"
"v_accvgpr_read_b32 v192, a160\n"
"v_accvgpr_read_b32 v193, a161\n"
"v_mfma_f32_16x16x32_bf16 v[100:103], v[148:151], a[24:27], v[100:103]\n"
"buffer_load_dwordx2 v[244:245], v249, s[48:51], s13 offen offset:0 sc1\n"
"v_mfma_f32_16x16x32_bf16 v[100:103], v[140:143], a[20:23], v[100:103]\n"
"v_accvgpr_read_b32 v194, a162\n"
"v_accvgpr_read_b32 v195, a163\n"
"v_mfma_f32_16x16x32_bf16 v[100:103], v[132:135], a[16:19], v[100:103]\n"
"s_waitcnt lgkmcnt(4)\n"
"ds_read_b128 a[16:19], v239 offset:49152\n"
"ds_read_b128 a[20:23], v239 offset:53248\n"
"v_mfma_f32_16x16x32_bf16 v[80:83], v[128:131], a[32:35], v[80:83]\n"
"buffer_load_dwordx4 v224, s[44:47], s67 offen offset:0 sc1 lds\n"
"v_mfma_f32_16x16x32_bf16 v[80:83], v[136:139], a[36:39], v[80:83]\n"
"ds_read_b128 a[24:27], v239 offset:57344\n"
"ds_read_b128 a[28:31], v239 offset:61440\n"
"v_mfma_f32_16x16x32_bf16 v[80:83], v[144:147], a[40:43], v[80:83]\n"
"s_waitcnt vmcnt(17)\n"
"v_accvgpr_read_b32 v196, a164\n"
"v_accvgpr_read_b32 v197, a165\n"
"v_mfma_f32_16x16x32_bf16 v[80:83], v[152:155], a[44:47], v[80:83]\n"
"v_accvgpr_read_b32 v198, a166\n"
"v_accvgpr_read_b32 v199, a167\n"
"v_mfma_f32_16x16x32_bf16 v[112:115], v[156:159], a[44:47], v[112:115]\n"
"buffer_load_dwordx4 v224, s[44:47], s67 offen offset:1024 sc1 lds\n"
"v_mfma_f32_16x16x32_bf16 v[112:115], v[148:151], a[40:43], v[112:115]\n"
"s_waitcnt vmcnt(17)\n"
"v_accvgpr_read_b32 v200, a168\n"
"v_accvgpr_read_b32 v201, a169\n"
"v_mfma_f32_16x16x32_bf16 v[112:115], v[140:143], a[36:39], v[112:115]\n"
"v_accvgpr_read_b32 v202, a170\n"
"v_accvgpr_read_b32 v203, a171\n"
"v_mfma_f32_16x16x32_bf16 v[112:115], v[132:135], a[32:35], v[112:115]\n"
"s_waitcnt lgkmcnt(4)\n"
"ds_read_b128 a[32:35], v239 offset:50176\n"
"ds_read_b128 a[36:39], v239 offset:54272\n"
"v_mfma_f32_16x16x32_bf16 v[84:87], v[128:131], a[0:3], v[84:87]\n"
"buffer_load_dwordx4 v224, s[44:47], s67 offen offset:2048 sc1 lds\n"
"v_mfma_f32_16x16x32_bf16 v[84:87], v[136:139], a[4:7], v[84:87]\n"
"ds_read_b128 a[40:43], v239 offset:58368\n"
"ds_read_b128 a[44:47], v239 offset:62464\n"
"v_mfma_f32_16x16x32_bf16 v[84:87], v[144:147], a[8:11], v[84:87]\n"
"s_waitcnt vmcnt(17)\n"
"v_accvgpr_read_b32 v204, a172\n"
"v_accvgpr_read_b32 v205, a173\n"
"v_mfma_f32_16x16x32_bf16 v[84:87], v[152:155], a[12:15], v[84:87]\n"
"v_accvgpr_read_b32 v206, a174\n"
"v_accvgpr_read_b32 v207, a175\n"
"v_mfma_f32_16x16x32_bf16 v[116:119], v[156:159], a[12:15], v[116:119]\n"
"buffer_load_dwordx4 v224, s[44:47], s67 offen offset:3072 sc1 lds\n"
"v_mfma_f32_16x16x32_bf16 v[116:119], v[148:151], a[8:11], v[116:119]\n"
"s_add_u32 m0, s65, 49152\n"
"s_add_u32 s67, s65, 327680\n"
"v_cmp_eq_u32_e64 s[84:85], s86, v193\n"
"v_mfma_f32_16x16x32_bf16 v[116:119], v[140:143], a[4:7], v[116:119]\n"
"v_cmp_eq_u32_e64 s[82:83], s86, v195\n"
"v_mfma_f32_16x16x32_bf16 v[116:119], v[132:135], a[0:3], v[116:119]\n"
"s_waitcnt lgkmcnt(4)\n"
"s_and_b64 s[84:85], s[84:85], s[82:83]\n"
"ds_read_b128 a[0:3], v239 offset:51200\n"
"v_mfma_f32_16x16x32_bf16 v[64:67], v[160:163], a[16:19], v[64:67]\n"
"buffer_load_dwordx4 v224, s[44:47], s67 offen offset:0 sc1 lds\n"
"v_mfma_f32_16x16x32_bf16 v[64:67], v[168:171], a[20:23], v[64:67]\n"
"ds_read_b128 a[4:7], v239 offset:55296\n"
"v_cmp_eq_u32_e64 s[82:83], s86, v197\n"
"v_mfma_f32_16x16x32_bf16 v[64:67], v[176:179], a[24:27], v[64:67]\n"
"s_and_b64 s[84:85], s[84:85], s[82:83]\n"
"ds_read_b128 a[8:11], v239 offset:59392\n"
"v_cmp_eq_u32_e64 s[82:83], s86, v199\n"
"v_mfma_f32_16x16x32_bf16 v[64:67], v[184:187], a[28:31], v[64:67]\n"
"s_and_b64 s[84:85], s[84:85], s[82:83]\n"
"ds_read_b128 a[12:15], v239 offset:63488\n"
"v_cmp_eq_u32_e64 s[82:83], s86, v201\n"
"v_mfma_f32_16x16x32_bf16 v[96:99], v[188:191], a[28:31], v[96:99]\n"
"s_and_b64 s[84:85], s[84:85], s[82:83]\n"
"buffer_load_dwordx4 v224, s[44:47], s67 offen offset:1024 sc1 lds\n"
"v_mfma_f32_16x16x32_bf16 v[96:99], v[180:183], a[24:27], v[96:99]\n"
"v_cmp_eq_u32_e64 s[82:83], s86, v203\n"
"v_mfma_f32_16x16x32_bf16 v[96:99], v[172:175], a[20:23], v[96:99]\n"
"s_and_b64 s[84:85], s[84:85], s[82:83]\n"
"v_cmp_eq_u32_e64 s[82:83], s86, v205\n"
"v_mfma_f32_16x16x32_bf16 v[96:99], v[164:167], a[16:19], v[96:99]\n"
"s_waitcnt lgkmcnt(4)\n"
"s_or_b32 s82, s82, 0xff00ff00\n"
"ds_read_b128 a[16:19], v239 offset:52224\n"
"v_mfma_f32_16x16x32_bf16 v[68:71], v[160:163], a[32:35], v[68:71]\n"
"s_or_b32 s83, s83, 0xff00ff00\n"
"buffer_load_dwordx4 v224, s[44:47], s67 offen offset:2048 sc1 lds\n"
"v_mfma_f32_16x16x32_bf16 v[68:71], v[168:171], a[36:39], v[68:71]\n"
"s_and_b64 s[84:85], s[84:85], s[82:83]\n"
"ds_read_b128 a[20:23], v239 offset:56320\n"
"v_cmp_eq_u32_e64 s[82:83], s86, v207\n"
"v_mfma_f32_16x16x32_bf16 v[68:71], v[176:179], a[40:43], v[68:71]\n"
"s_or_b32 s82, s82, 0xff00ff00\n"
"s_or_b32 s83, s83, 0xff00ff00\n"
"ds_read_b128 a[24:27], v239 offset:60416\n"
"v_mfma_f32_16x16x32_bf16 v[68:71], v[184:187], a[44:47], v[68:71]\n"
"s_and_b64 s[84:85], s[84:85], s[82:83]\n"
"s_orn2_b64 s[84:85], s[84:85], s[72:73]\n"
"ds_read_b128 a[28:31], v239 offset:64512\n"
"v_mfma_f32_16x16x32_bf16 v[100:103], v[188:191], a[44:47], v[100:103]\n"
"buffer_load_dwordx4 v224, s[44:47], s67 offen offset:3072 sc1 lds\n"
"v_mfma_f32_16x16x32_bf16 v[100:103], v[180:183], a[40:43], v[100:103]\n"
"s_add_u32 m0, s65, 65536\n"
"s_add_u32 s67, s65, 344064\n"
"v_mfma_f32_16x16x32_bf16 v[100:103], v[172:175], a[36:39], v[100:103]\n"
"s_cmp_eq_u64 s[84:85], -1\n"
"s_cbranch_scc0 SPZ_%=\n"
"SBZ_%=:\n"
"v_mul_f32_e64 v226, -v226, v227\n"
"v_mfma_f32_16x16x32_bf16 v[100:103], v[164:167], a[32:35], v[100:103]\n"
"s_waitcnt vmcnt(11)\n"
"v_and_b32 v193, 0xffff0000, v192\n"
"v_lshlrev_b32 v192, 16, v192\n"
"v_mfma_f32_16x16x32_bf16 v[216:219], a[192:195], a[48:51], 0\n"
"buffer_load_dwordx4 v224, s[44:47], s67 offen offset:0 sc1 lds\n"
"v_mfma_f32_16x16x32_bf16 v[216:219], a[196:199], a[48:51], v[216:219]\n"
"v_and_b32 v195, 0xffff0000, v194\n"
"v_lshlrev_b32 v194, 16, v194\n"
"v_mfma_f32_16x16x32_bf16 v[216:219], a[200:203], a[48:51], v[216:219]\n"
"v_add_f32 v192, v226, v192\n"
"v_add_f32 v193, v226, v193\n"
"v_mfma_f32_16x16x32_bf16 v[216:219], a[204:207], a[48:51], v[216:219]\n"
"s_waitcnt lgkmcnt(4)\n"
"s_add_u32 s71, s0, 32768\n"
"s_waitcnt vmcnt(10)\n"
"v_mfma_f32_16x16x32_bf16 v[80:83], v[160:163], a[0:3], v[80:83]\n"
"s_barrier\n"
"ds_read_b128 a[32:35], v240 offset:0\n"
"v_mfma_f32_16x16x32_bf16 v[80:83], v[168:171], a[4:7], v[80:83]\n"
"buffer_store_dwordx2 v[250:251], v241, s[48:51], s71 offen offset:0 sc1\n"
"v_mfma_f32_16x16x32_bf16 v[80:83], v[176:179], a[8:11], v[80:83]\n"
"ds_read_b128 a[36:39], v240 offset:4096\n"
"ds_read_b128 a[40:43], v240 offset:8192\n"
"v_mfma_f32_16x16x32_bf16 v[80:83], v[184:187], a[12:15], v[80:83]\n"
"ds_read_b128 a[44:47], v240 offset:12288\n"
"v_add_f32 v194, v226, v194\n"
"v_mfma_f32_16x16x32_bf16 v[112:115], v[188:191], a[12:15], v[112:115]\n"
"buffer_load_dwordx4 v224, s[44:47], s67 offen offset:1024 sc1 lds\n"
"v_mfma_f32_16x16x32_bf16 v[112:115], v[180:183], a[8:11], v[112:115]\n"
"v_add_f32 v195, v226, v195\n"
"v_lshlrev_b32 v232, 16, v208\n"
"v_mfma_f32_16x16x32_bf16 v[112:115], v[172:175], a[4:7], v[112:115]\n"
"buffer_load_dwordx4 v224, s[44:47], s67 offen offset:2048 sc1 lds\n"
"v_mfma_f32_16x16x32_bf16 v[112:115], v[164:167], a[0:3], v[112:115]\n"
"s_waitcnt lgkmcnt(4)\n"
"s_waitcnt vmcnt(12)\n"
"v_cmp_eq_u32_e64 s[14:15], s86, v244\n"
"v_mfma_f32_16x16x32_bf16 v[84:87], v[160:163], a[16:19], v[84:87]\n"
"ds_read_b128 a[0:3], v240 offset:1024\n"
"v_cmp_eq_u32_e64 s[16:17], s87, v245\n"
"v_mfma_f32_16x16x32_bf16 v[84:87], v[168:171], a[20:23], v[84:87]\n"
"s_and_b64 s[14:15], s[14:15], s[16:17]\n"
"ds_read_b128 a[4:7], v240 offset:5120\n"
"ds_read_b128 a[8:11], v240 offset:9216\n"
"v_mfma_f32_16x16x32_bf16 v[84:87], v[176:179], a[24:27], v[84:87]\n"
"s_cmp_eq_u64 s[14:15], -1\n"
"s_cbranch_scc0 SPC1_%=\n"
"SBC1_%=:\n"
"ds_read_b128 a[12:15], v240 offset:13312\n"
"v_mfma_f32_16x16x32_bf16 v[84:87], v[184:187], a[28:31], v[84:87]\n"
"s_add_u32 s13, s94, 65536\n"
"buffer_load_dwordx4 a[208:211], v248, s[52:55], s13 offen offset:0 sc1\n"
"v_mfma_f32_16x16x32_bf16 v[116:119], v[188:191], a[28:31], v[116:119]\n"
"s_add_u32 s13, s94, 69632\n"
"v_and_b32 v233, 0xffff0000, v208\n"
"v_lshlrev_b32 v234, 16, v209\n"
"v_mfma_f32_16x16x32_bf16 v[116:119], v[180:183], a[24:27], v[116:119]\n"
"v_and_b32 v235, 0xffff0000, v209\n"
"v_fmac_f32 v192, v232, v227\n"
"v_mfma_f32_16x16x32_bf16 v[116:119], v[172:175], a[20:23], v[116:119]\n"
"buffer_load_dwordx4 a[212:215], v248, s[52:55], s13 offen offset:0 sc1\n"
"v_mfma_f32_16x16x32_bf16 v[116:119], v[164:167], a[16:19], v[116:119]\n"
"s_waitcnt lgkmcnt(4)\n"
"s_add_u32 s68, s93, 196608\n"
"v_mul_f32 v64, v242, v64\n"
"v_mfma_f32_16x16x32_bf16 v[0:3], v[8:11], a[32:35], 0\n"
"s_add_u32 s13, s94, 73728\n"
"v_mul_f32 v65, v243, v65\n"
"v_mul_f32 v66, v246, v66\n"
"v_mfma_f32_16x16x32_bf16 v[0:3], v[24:27], a[36:39], v[0:3]\n"
"ds_read_b128 a[16:19], v240 offset:2048\n"
"v_mul_f32 v67, v247, v67\n"
"v_mfma_f32_16x16x32_bf16 v[0:3], v[40:43], a[40:43], v[0:3]\n"
"buffer_load_dwordx4 v224, s[44:47], s67 offen offset:3072 sc1 lds\n"
"v_mfma_f32_16x16x32_bf16 v[0:3], v[56:59], a[44:47], v[0:3]\n"
"s_add_u32 m0, s65, 32768\n"
"ds_read_b128 a[20:23], v240 offset:6144\n"
"v_mul_f32 v68, v242, v68\n"
"v_mfma_f32_16x16x32_bf16 v[32:35], v[60:63], a[44:47], 0\n"
"s_add_u32 s67, s65, 360448\n"
"ds_read_b128 a[24:27], v240 offset:10240\n"
"v_mul_f32 v69, v243, v69\n"
"v_mfma_f32_16x16x32_bf16 v[32:35], v[44:47], a[40:43], v[32:35]\n"
"ds_read_b128 a[28:31], v240 offset:14336\n"
"v_mul_f32 v70, v246, v70\n"
"v_mfma_f32_16x16x32_bf16 v[32:35], v[28:31], a[36:39], v[32:35]\n"
"buffer_load_dwordx4 a[216:219], v248, s[52:55], s13 offen offset:0 sc1\n"
"v_mfma_f32_16x16x32_bf16 v[32:35], v[12:15], a[32:35], v[32:35]\n"
"s_waitcnt lgkmcnt(4)\n"
"v_mul_f32 v71, v247, v71\n"
"v_cvt_pk_bf16_f32 v64, v64, v65\n"
"v_mfma_f32_16x16x32_bf16 v[4:7], v[8:11], a[0:3], 0\n"
"s_add_u32 s13, s94, 77824\n"
"v_cvt_pk_bf16_f32 v65, v66, v67\n"
"v_cvt_pk_bf16_f32 v66, v68, v69\n"
"v_mfma_f32_16x16x32_bf16 v[4:7], v[24:27], a[4:7], v[4:7]\n"
"ds_read_b128 a[32:35], v240 offset:3072\n"
"v_cvt_pk_bf16_f32 v67, v70, v71\n"
"v_mfma_f32_16x16x32_bf16 v[4:7], v[40:43], a[8:11], v[4:7]\n"
"buffer_load_dwordx4 a[220:223], v248, s[52:55], s13 offen offset:0 sc1\n"
"v_mfma_f32_16x16x32_bf16 v[4:7], v[56:59], a[12:15], v[4:7]\n"
"ds_read_b128 a[36:39], v240 offset:7168\n"
"ds_read_b128 a[40:43], v240 offset:11264\n"
"v_mfma_f32_16x16x32_bf16 v[36:39], v[60:63], a[12:15], 0\n"
"ds_read_b128 a[44:47], v240 offset:15360\n"
"v_fmac_f32 v193, v233, v227\n"
"v_mfma_f32_16x16x32_bf16 v[36:39], v[44:47], a[8:11], v[36:39]\n"
"v_fmac_f32 v194, v234, v227\n"
"v_fmac_f32 v195, v235, v227\n"
"v_mfma_f32_16x16x32_bf16 v[36:39], v[28:31], a[4:7], v[36:39]\n"
"buffer_store_dwordx4 v[64:67], v224, s[52:55], s68 offen offset:0 sc1\n"
"v_mfma_f32_16x16x32_bf16 v[36:39], v[12:15], a[0:3], v[36:39]\n"
"s_waitcnt lgkmcnt(4)\n"
"s_add_u32 s68, s93, 229376\n"
"v_mul_f32 v96, v225, v96\n"
"v_mfma_f32_16x16x32_bf16 v[16:19], v[8:11], a[16:19], 0\n"
"v_mul_f32 v97, v238, v97\n"
"v_mul_f32 v98, v252, v98\n"
"v_mfma_f32_16x16x32_bf16 v[16:19], v[24:27], a[20:23], v[16:19]\n"
"v_mul_f32 v99, v253, v99\n"
"v_mul_f32 v100, v225, v100\n"
"v_mfma_f32_16x16x32_bf16 v[16:19], v[40:43], a[24:27], v[16:19]\n"
"ds_read_b128 a[0:3], v240 offset:16384\n"
"v_mul_f32 v101, v238, v101\n"
"v_mfma_f32_16x16x32_bf16 v[16:19], v[56:59], a[28:31], v[16:19]\n"
"v_mul_f32 v102, v252, v102\n"
"v_mul_f32 v103, v253, v103\n"
"v_mfma_f32_16x16x32_bf16 v[48:51], v[60:63], a[28:31], 0\n"
"ds_read_b128 a[4:7], v240 offset:20480\n"
"v_cvt_pk_bf16_f32 v96, v96, v97\n"
"v_mfma_f32_16x16x32_bf16 v[48:51], v[44:47], a[24:27], v[48:51]\n"
"ds_read_b128 a[8:11], v240 offset:24576\n"
"v_cvt_pk_bf16_f32 v97, v98, v99\n"
"v_mfma_f32_16x16x32_bf16 v[48:51], v[28:31], a[20:23], v[48:51]\n"
"ds_read_b128 a[12:15], v240 offset:28672\n"
"v_cvt_pk_bf16_f32 v98, v100, v101\n"
"v_mfma_f32_16x16x32_bf16 v[48:51], v[12:15], a[16:19], v[48:51]\n"
"s_waitcnt lgkmcnt(4)\n"
"ds_read_b128 a[16:19], v240 offset:17408\n"
"v_cvt_pk_bf16_f32 v99, v102, v103\n"
"v_mfma_f32_16x16x32_bf16 v[20:23], v[8:11], a[32:35], 0\n"
"buffer_store_dwordx4 v[96:99], v224, s[52:55], s68 offen offset:0 sc1\n"
"v_mfma_f32_16x16x32_bf16 v[20:23], v[24:27], a[36:39], v[20:23]\n"
"s_add_u32 s68, s93, 212992\n"
"ds_read_b128 a[20:23], v240 offset:21504\n"
"v_mul_f32 v80, v242, v80\n"
"v_mfma_f32_16x16x32_bf16 v[20:23], v[40:43], a[40:43], v[20:23]\n"
"ds_read_b128 a[24:27], v240 offset:25600\n"
"v_mul_f32 v81, v243, v81\n"
"v_mfma_f32_16x16x32_bf16 v[20:23], v[56:59], a[44:47], v[20:23]\n"
"ds_read_b128 a[28:31], v240 offset:29696\n"
"v_mul_f32 v82, v246, v82\n"
"v_mfma_f32_16x16x32_bf16 v[52:55], v[60:63], a[44:47], 0\n"
"v_mul_f32 v83, v247, v83\n"
"v_mul_f32 v84, v242, v84\n"
"v_mfma_f32_16x16x32_bf16 v[52:55], v[44:47], a[40:43], v[52:55]\n"
"v_mul_f32 v85, v243, v85\n"
"v_mul_f32 v86, v246, v86\n"
"v_mfma_f32_16x16x32_bf16 v[52:55], v[28:31], a[36:39], v[52:55]\n"
"v_mul_f32 v87, v247, v87\n"
"v_cvt_pk_bf16_f32 v80, v80, v81\n"
"v_mfma_f32_16x16x32_bf16 v[52:55], v[12:15], a[32:35], v[52:55]\n"
"s_waitcnt lgkmcnt(4)\n"
"ds_read_b128 a[32:35], v240 offset:18432\n"
"v_cvt_pk_bf16_f32 v81, v82, v83\n"
"v_mfma_f32_16x16x32_bf16 v[0:3], v[72:75], a[0:3], v[0:3]\n"
"ds_read_b128 a[36:39], v240 offset:22528\n"
"v_cvt_pk_bf16_f32 v82, v84, v85\n"
"v_mfma_f32_16x16x32_bf16 v[0:3], v[88:91], a[4:7], v[0:3]\n"
"ds_read_b128 a[40:43], v240 offset:26624\n"
"v_cvt_pk_bf16_f32 v83, v86, v87\n"
"v_mfma_f32_16x16x32_bf16 v[0:3], v[104:107], a[8:11], v[0:3]\n"
"buffer_store_dwordx4 v[80:83], v224, s[52:55], s68 offen offset:0 sc1\n"
"v_mfma_f32_16x16x32_bf16 v[0:3], v[120:123], a[12:15], v[0:3]\n"
"s_add_u32 s68, s93, 245760\n"
"ds_read_b128 a[44:47], v240 offset:30720\n"
"v_mul_f32 v112, v225, v112\n"
"v_mfma_f32_16x16x32_bf16 v[32:35], v[124:127], a[12:15], v[32:35]\n"
"v_mul_f32 v113, v238, v113\n"
"v_mul_f32 v114, v252, v114\n"
"v_mfma_f32_16x16x32_bf16 v[32:35], v[108:111], a[8:11], v[32:35]\n"
"v_mul_f32 v115, v253, v115\n"
"v_mul_f32 v116, v225, v116\n"
"v_mfma_f32_16x16x32_bf16 v[32:35], v[92:95], a[4:7], v[32:35]\n"
"v_mul_f32 v117, v238, v117\n"
"v_mul_f32 v118, v252, v118\n"
"v_mfma_f32_16x16x32_bf16 v[32:35], v[76:79], a[0:3], v[32:35]\n"
"s_waitcnt lgkmcnt(4)\n"
"ds_read_b128 a[0:3], v240 offset:19456\n"
"ds_read_b128 a[4:7], v240 offset:23552\n"
"v_mfma_f32_16x16x32_bf16 v[4:7], v[72:75], a[16:19], v[4:7]\n"
"ds_read_b128 a[8:11], v240 offset:27648\n"
"ds_read_b128 a[12:15], v240 offset:31744\n"
"v_mfma_f32_16x16x32_bf16 v[4:7], v[88:91], a[20:23], v[4:7]\n"
"buffer_load_dwordx4 v224, s[44:47], s67 offen offset:0 sc1 lds\n"
"v_mfma_f32_16x16x32_bf16 v[4:7], v[104:107], a[24:27], v[4:7]\n"
"v_mul_f32 v119, v253, v119\n"
"v_cvt_pk_bf16_f32 v112, v112, v113\n"
"v_mfma_f32_16x16x32_bf16 v[4:7], v[120:123], a[28:31], v[4:7]\n"
"v_cvt_pk_bf16_f32 v113, v114, v115\n"
"v_cvt_pk_bf16_f32 v114, v116, v117\n"
"v_mfma_f32_16x16x32_bf16 v[36:39], v[124:127], a[28:31], v[36:39]\n"
"buffer_load_dwordx4 v224, s[44:47], s67 offen offset:1024 sc1 lds\n"
"v_mfma_f32_16x16x32_bf16 v[36:39], v[108:111], a[24:27], v[36:39]\n"
"v_cvt_pk_bf16_f32 v115, v118, v119\n"
"v_mul_f32 v232, 0xbfb8aa3b, v192\n"
"v_mfma_f32_16x16x32_bf16 v[36:39], v[92:95], a[20:23], v[36:39]\n"
"v_mul_f32 v233, 0xbfb8aa3b, v193\n"
"v_mul_f32 v234, 0xbfb8aa3b, v194\n"
"v_mfma_f32_16x16x32_bf16 v[36:39], v[76:79], a[16:19], v[36:39]\n"
"s_waitcnt lgkmcnt(4)\n"
"ds_read_b128 a[16:19], v224 offset:0\n"
"ds_read_b128 a[20:23], v224 offset:4096\n"
"v_mfma_f32_16x16x32_bf16 v[16:19], v[72:75], a[32:35], v[16:19]\n"
"ds_read_b128 a[24:27], v224 offset:8192\n"
"ds_read_b128 a[28:31], v224 offset:12288\n"
"v_mfma_f32_16x16x32_bf16 v[16:19], v[88:91], a[36:39], v[16:19]\n"
"buffer_store_dwordx4 v[112:115], v224, s[52:55], s68 offen offset:0 sc1\n"
"v_mfma_f32_16x16x32_bf16 v[16:19], v[104:107], a[40:43], v[16:19]\n"
"v_mul_f32 v235, 0xbfb8aa3b, v195\n"
"v_mfma_f32_16x16x32_bf16 v[16:19], v[120:123], a[44:47], v[16:19]\n"
"v_exp_f32 v232, v232\n"
"v_mfma_f32_16x16x32_bf16 v[48:51], v[124:127], a[44:47], v[48:51]\n"
"v_exp_f32 v233, v233\n"
"v_mfma_f32_16x16x32_bf16 v[48:51], v[108:111], a[40:43], v[48:51]\n"
"v_exp_f32 v234, v234\n"
"v_mfma_f32_16x16x32_bf16 v[48:51], v[92:95], a[36:39], v[48:51]\n"
"v_exp_f32 v235, v235\n"
"v_mfma_f32_16x16x32_bf16 v[48:51], v[76:79], a[32:35], v[48:51]\n"
"s_waitcnt lgkmcnt(4)\n"
"ds_read_b128 a[32:35], v224 offset:1024\n"
"ds_read_b128 a[36:39], v224 offset:5120\n"
"v_mfma_f32_16x16x32_bf16 v[20:23], v[72:75], a[0:3], v[20:23]\n"
"buffer_load_dwordx4 v224, s[44:47], s67 offen offset:2048 sc1 lds\n"
"v_mfma_f32_16x16x32_bf16 v[20:23], v[88:91], a[4:7], v[20:23]\n"
"ds_read_b128 a[40:43], v224 offset:9216\n"
"ds_read_b128 a[44:47], v224 offset:13312\n"
"v_mfma_f32_16x16x32_bf16 v[20:23], v[104:107], a[8:11], v[20:23]\n"
"v_add_f32 v232, 1.0, v232\n"
"v_add_f32 v233, 1.0, v233\n"
"v_mfma_f32_16x16x32_bf16 v[20:23], v[120:123], a[12:15], v[20:23]\n"
"v_add_f32 v234, 1.0, v234\n"
"v_add_f32 v235, 1.0, v235\n"
"v_mfma_f32_16x16x32_bf16 v[52:55], v[124:127], a[12:15], v[52:55]\n"
"buffer_load_dwordx4 v224, s[44:47], s67 offen offset:3072 sc1 lds\n"
"v_mfma_f32_16x16x32_bf16 v[52:55], v[108:111], a[8:11], v[52:55]\n"
"s_add_u32 m0, s65, 81920\n"
"s_add_u32 s67, s65, 376832\n"
"v_mfma_f32_16x16x32_bf16 v[52:55], v[92:95], a[4:7], v[52:55]\n"
"v_rcp_f32 v232, v232\n"
"v_mfma_f32_16x16x32_bf16 v[52:55], v[76:79], a[0:3], v[52:55]\n"
"s_waitcnt lgkmcnt(4)\n"
"ds_read_b128 a[0:3], v224 offset:2048\n"
"ds_read_b128 a[4:7], v224 offset:6144\n"
"v_mfma_f32_16x16x32_bf16 v[0:3], v[128:131], a[16:19], v[0:3]\n"
"buffer_load_dwordx4 v224, s[44:47], s67 offen offset:0 sc1 lds\n"
"v_mfma_f32_16x16x32_bf16 v[0:3], v[136:139], a[20:23], v[0:3]\n"
"ds_read_b128 a[8:11], v224 offset:10240\n"
"ds_read_b128 a[12:15], v224 offset:14336\n"
"v_mfma_f32_16x16x32_bf16 v[0:3], v[144:147], a[24:27], v[0:3]\n"
"v_rcp_f32 v233, v233\n"
"v_mfma_f32_16x16x32_bf16 v[0:3], v[152:155], a[28:31], v[0:3]\n"
"v_rcp_f32 v234, v234\n"
"v_mfma_f32_16x16x32_bf16 v[32:35], v[156:159], a[28:31], v[32:35]\n"
"buffer_load_dwordx4 v224, s[44:47], s67 offen offset:1024 sc1 lds\n"
"v_mfma_f32_16x16x32_bf16 v[32:35], v[148:151], a[24:27], v[32:35]\n"
"v_rcp_f32 v235, v235\n"
"v_mfma_f32_16x16x32_bf16 v[32:35], v[140:143], a[20:23], v[32:35]\n"
"v_mul_f32 v192, v192, v232\n"
"v_mul_f32 v193, v193, v233\n"
"v_mfma_f32_16x16x32_bf16 v[32:35], v[132:135], a[16:19], v[32:35]\n"
"s_waitcnt lgkmcnt(4)\n"
"ds_read_b128 a[16:19], v224 offset:3072\n"
"v_mul_f32 v194, v194, v234\n"
"v_mfma_f32_16x16x32_bf16 v[4:7], v[128:131], a[32:35], v[4:7]\n"
"buffer_load_dwordx4 v224, s[44:47], s67 offen offset:2048 sc1 lds\n"
"v_mfma_f32_16x16x32_bf16 v[4:7], v[136:139], a[36:39], v[4:7]\n"
"s_add_u32 s13, s1, 32768\n"
"ds_read_b128 a[20:23], v224 offset:7168\n"
"v_mul_f32 v195, v195, v235\n"
"v_mfma_f32_16x16x32_bf16 v[4:7], v[144:147], a[40:43], v[4:7]\n"
"ds_read_b128 a[24:27], v224 offset:11264\n"
"v_and_b32 v197, 0xffff0000, v196\n"
"v_mfma_f32_16x16x32_bf16 v[4:7], v[152:155], a[44:47], v[4:7]\n"
"ds_read_b128 a[28:31], v224 offset:15360\n"
"v_lshlrev_b32 v196, 16, v196\n"
"v_mfma_f32_16x16x32_bf16 v[36:39], v[156:159], a[44:47], v[36:39]\n"
"buffer_load_dwordx2 v[244:245], v249, s[48:51], s13 offen offset:0 sc1\n"
"v_mfma_f32_16x16x32_bf16 v[36:39], v[148:151], a[40:43], v[36:39]\n"
"v_and_b32 v199, 0xffff0000, v198\n"
"v_lshlrev_b32 v198, 16, v198\n"
"v_mfma_f32_16x16x32_bf16 v[36:39], v[140:143], a[36:39], v[36:39]\n"
"buffer_load_dwordx4 v224, s[44:47], s67 offen offset:3072 sc1 lds\n"
"v_mfma_f32_16x16x32_bf16 v[36:39], v[132:135], a[32:35], v[36:39]\n"
"s_waitcnt lgkmcnt(4)\n"
"s_add_u32 m0, s65, 98304\n"
"s_add_u32 s67, s65, 393216\n"
"v_mfma_f32_16x16x32_bf16 v[16:19], v[128:131], a[0:3], v[16:19]\n"
"s_waitcnt vmcnt(15)\n"
"s_barrier\n"
"ds_read_b128 a[32:35], v224 offset:16384\n"
"v_mfma_f32_16x16x32_bf16 v[16:19], v[136:139], a[4:7], v[16:19]\n"
"ds_read_b128 a[36:39], v224 offset:20480\n"
"v_add_f32 v196, v226, v196\n"
"v_mfma_f32_16x16x32_bf16 v[16:19], v[144:147], a[8:11], v[16:19]\n"
"buffer_load_dwordx4 v224, s[44:47], s67 offen offset:0 sc1 lds\n"
"v_mfma_f32_16x16x32_bf16 v[16:19], v[152:155], a[12:15], v[16:19]\n"
"ds_read_b128 a[40:43], v224 offset:24576\n"
"v_add_f32 v197, v226, v197\n"
"v_mfma_f32_16x16x32_bf16 v[48:51], v[156:159], a[12:15], v[48:51]\n"
"ds_read_b128 a[44:47], v224 offset:28672\n"
"v_add_f32 v198, v226, v198\n"
"v_mfma_f32_16x16x32_bf16 v[48:51], v[148:151], a[8:11], v[48:51]\n"
"v_add_f32 v199, v226, v199\n"
"v_lshlrev_b32 v232, 16, v210\n"
"v_mfma_f32_16x16x32_bf16 v[48:51], v[140:143], a[4:7], v[48:51]\n"
"buffer_load_dwordx4 v224, s[44:47], s67 offen offset:1024 sc1 lds\n"
"v_mfma_f32_16x16x32_bf16 v[48:51], v[132:135], a[0:3], v[48:51]\n"
"s_waitcnt vmcnt(15)\n"
"ds_read_b128 a[0:3], v224 offset:17408\n"
"v_and_b32 v233, 0xffff0000, v210\n"
"v_mfma_f32_16x16x32_bf16 v[216:219], a[208:211], a[52:55], v[216:219]\n"
"ds_read_b128 a[4:7], v224 offset:21504\n"
"v_lshlrev_b32 v234, 16, v211\n"
"v_mfma_f32_16x16x32_bf16 v[216:219], a[212:215], a[52:55], v[216:219]\n"
"ds_read_b128 a[8:11], v224 offset:25600\n"
"v_and_b32 v235, 0xffff0000, v211\n"
"v_mfma_f32_16x16x32_bf16 v[216:219], a[216:219], a[52:55], v[216:219]\n"
"ds_read_b128 a[12:15], v224 offset:29696\n"
"v_fmac_f32 v196, v232, v227\n"
"v_mfma_f32_16x16x32_bf16 v[216:219], a[220:223], a[52:55], v[216:219]\n"
"s_waitcnt lgkmcnt(8)\n"
"buffer_load_dwordx4 v224, s[44:47], s67 offen offset:2048 sc1 lds\n"
"v_mfma_f32_16x16x32_bf16 v[20:23], v[128:131], a[16:19], v[20:23]\n"
"v_fmac_f32 v197, v233, v227\n"
"v_fmac_f32 v198, v234, v227\n"
"v_mfma_f32_16x16x32_bf16 v[20:23], v[136:139], a[20:23], v[20:23]\n"
"v_fmac_f32 v199, v235, v227\n"
"v_mul_f32 v232, 0xbfb8aa3b, v196\n"
"v_mfma_f32_16x16x32_bf16 v[20:23], v[144:147], a[24:27], v[20:23]\n"
"buffer_load_dwordx4 v224, s[44:47], s67 offen offset:3072 sc1 lds\n"
"v_mfma_f32_16x16x32_bf16 v[20:23], v[152:155], a[28:31], v[20:23]\n"
"s_add_u32 m0, s65, 114688\n"
"v_mul_f32 v233, 0xbfb8aa3b, v197\n"
"v_mul_f32 v234, 0xbfb8aa3b, v198\n"
"v_mfma_f32_16x16x32_bf16 v[52:55], v[156:159], a[28:31], v[52:55]\n"
"s_add_u32 s67, s65, 409600\n"
"v_mul_f32 v235, 0xbfb8aa3b, v199\n"
"v_mfma_f32_16x16x32_bf16 v[52:55], v[148:151], a[24:27], v[52:55]\n"
"v_exp_f32 v232, v232\n"
"v_mfma_f32_16x16x32_bf16 v[52:55], v[140:143], a[20:23], v[52:55]\n"
"v_exp_f32 v233, v233\n"
"v_mfma_f32_16x16x32_bf16 v[52:55], v[132:135], a[16:19], v[52:55]\n"
"s_waitcnt lgkmcnt(4)\n"
"ds_read_b128 a[16:19], v224 offset:18432\n"
"ds_read_b128 a[20:23], v224 offset:22528\n"
"v_mfma_f32_16x16x32_bf16 v[0:3], v[160:163], a[32:35], v[0:3]\n"
"ds_read_b128 a[24:27], v224 offset:26624\n"
"ds_read_b128 a[28:31], v224 offset:30720\n"
"v_mfma_f32_16x16x32_bf16 v[0:3], v[168:171], a[36:39], v[0:3]\n"
"v_exp_f32 v234, v234\n"
"v_mfma_f32_16x16x32_bf16 v[0:3], v[176:179], a[40:43], v[0:3]\n"
"v_exp_f32 v235, v235\n"
"v_mfma_f32_16x16x32_bf16 v[0:3], v[184:187], a[44:47], v[0:3]\n"
"v_add_f32 v232, 1.0, v232\n"
"v_add_f32 v233, 1.0, v233\n"
"v_mfma_f32_16x16x32_bf16 v[32:35], v[188:191], a[44:47], v[32:35]\n"
"v_add_f32 v234, 1.0, v234\n"
"v_add_f32 v235, 1.0, v235\n"
"v_mfma_f32_16x16x32_bf16 v[32:35], v[180:183], a[40:43], v[32:35]\n"
"v_rcp_f32 v232, v232\n"
"v_mfma_f32_16x16x32_bf16 v[32:35], v[172:175], a[36:39], v[32:35]\n"
"v_rcp_f32 v233, v233\n"
"v_mfma_f32_16x16x32_bf16 v[32:35], v[164:167], a[32:35], v[32:35]\n"
"s_waitcnt lgkmcnt(4)\n"
"ds_read_b128 a[32:35], v224 offset:19456\n"
"ds_read_b128 a[36:39], v224 offset:23552\n"
"v_mfma_f32_16x16x32_bf16 v[4:7], v[160:163], a[0:3], v[4:7]\n"
"ds_read_b128 a[40:43], v224 offset:27648\n"
"ds_read_b128 a[44:47], v224 offset:31744\n"
"v_mfma_f32_16x16x32_bf16 v[4:7], v[168:171], a[4:7], v[4:7]\n"
"v_rcp_f32 v234, v234\n"
"v_mfma_f32_16x16x32_bf16 v[4:7], v[176:179], a[8:11], v[4:7]\n"
"v_rcp_f32 v235, v235\n"
"v_mfma_f32_16x16x32_bf16 v[4:7], v[184:187], a[12:15], v[4:7]\n"
"v_mul_f32 v196, v196, v232\n"
"v_mul_f32 v197, v197, v233\n"
"v_mfma_f32_16x16x32_bf16 v[36:39], v[188:191], a[12:15], v[36:39]\n"
"v_mul_f32 v198, v198, v234\n"
"v_mul_f32 v199, v199, v235\n"
"v_mfma_f32_16x16x32_bf16 v[36:39], v[180:183], a[8:11], v[36:39]\n"
"v_and_b32 v201, 0xffff0000, v200\n"
"v_lshlrev_b32 v200, 16, v200\n"
"v_mfma_f32_16x16x32_bf16 v[36:39], v[172:175], a[4:7], v[36:39]\n"
"buffer_load_dwordx4 v224, s[44:47], s67 offen offset:0 sc1 lds\n"
"v_mfma_f32_16x16x32_bf16 v[36:39], v[164:167], a[0:3], v[36:39]\n"
"s_waitcnt lgkmcnt(4)\n"
"s_add_u32 s71, s0, 49152\n"
"s_waitcnt vmcnt(12)\n"
"v_mfma_f32_16x16x32_bf16 v[16:19], v[160:163], a[16:19], v[16:19]\n"
"buffer_store_dwordx2 v[250:251], v241, s[48:51], s71 offen offset:0 sc1\n"
"v_mfma_f32_16x16x32_bf16 v[16:19], v[168:171], a[20:23], v[16:19]\n"
"ds_read_b128 a[0:3], v224 offset:49152\n"
"ds_read_b128 a[4:7], v224 offset:53248\n"
"v_mfma_f32_16x16x32_bf16 v[16:19], v[176:179], a[24:27], v[16:19]\n"
"ds_read_b128 a[8:11], v224 offset:57344\n"
"ds_read_b128 a[12:15], v224 offset:61440\n"
"v_mfma_f32_16x16x32_bf16 v[16:19], v[184:187], a[28:31], v[16:19]\n"
"v_and_b32 v203, 0xffff0000, v202\n"
"v_lshlrev_b32 v202, 16, v202\n"
"v_mfma_f32_16x16x32_bf16 v[48:51], v[188:191], a[28:31], v[48:51]\n"
"buffer_load_dwordx4 v224, s[44:47], s67 offen offset:1024 sc1 lds\n"
"v_mfma_f32_16x16x32_bf16 v[48:51], v[180:183], a[24:27], v[48:51]\n"
"v_add_f32 v200, v226, v200\n"
"v_add_f32 v201, v226, v201\n"
"v_mfma_f32_16x16x32_bf16 v[48:51], v[172:175], a[20:23], v[48:51]\n"
"v_add_f32 v202, v226, v202\n"
"v_add_f32 v203, v226, v203\n"
"v_mfma_f32_16x16x32_bf16 v[48:51], v[164:167], a[16:19], v[48:51]\n"
"IWB9_%=:\n"
"s_cbranch_execz IRB_%=\n"
"s_waitcnt lgkmcnt(4)\n"
"s_waitcnt vmcnt(8)\n"
"v_cmp_eq_u32_e64 s[14:15], s86, v244\n"
"v_mfma_f32_16x16x32_bf16 v[20:23], v[160:163], a[32:35], v[20:23]\n"
"ds_read_b128 a[16:19], v224 offset:50176\n"
"v_cmp_eq_u32_e64 s[16:17], s87, v245\n"
"v_mfma_f32_16x16x32_bf16 v[20:23], v[168:171], a[36:39], v[20:23]\n"
"s_and_b64 s[14:15], s[14:15], s[16:17]\n"
"ds_read_b128 a[20:23], v224 offset:54272\n"
"ds_read_b128 a[24:27], v224 offset:58368\n"
"v_mfma_f32_16x16x32_bf16 v[20:23], v[176:179], a[40:43], v[20:23]\n"
"s_cmp_eq_u64 s[14:15], -1\n"
"s_cbranch_scc0 SPC2_%=\n"
"SBC2_%=:\n"
"ds_read_b128 a[28:31], v224 offset:62464\n"
"v_mfma_f32_16x16x32_bf16 v[20:23], v[184:187], a[44:47], v[20:23]\n"
"s_add_u32 s13, s94, 131072\n"
"buffer_load_dwordx4 a[224:227], v248, s[52:55], s13 offen offset:0 sc1\n"
"v_mfma_f32_16x16x32_bf16 v[52:55], v[188:191], a[44:47], v[52:55]\n"
"s_add_u32 s13, s94, 135168\n"
"v_lshlrev_b32 v232, 16, v212\n"
"v_and_b32 v233, 0xffff0000, v212\n"
"v_mfma_f32_16x16x32_bf16 v[52:55], v[180:183], a[40:43], v[52:55]\n"
"buffer_load_dwordx4 a[228:231], v248, s[52:55], s13 offen offset:0 sc1\n"
"v_mfma_f32_16x16x32_bf16 v[52:55], v[172:175], a[36:39], v[52:55]\n"
"s_add_u32 s13, s94, 139264\n"
"v_lshlrev_b32 v234, 16, v213\n"
"v_and_b32 v235, 0xffff0000, v213\n"
"v_mfma_f32_16x16x32_bf16 v[52:55], v[164:167], a[32:35], v[52:55]\n"
"s_waitcnt lgkmcnt(4)\n"
"s_add_u32 s68, s93, 262144\n"
"v_mul_f32 v0, v242, v0\n"
"v_mfma_f32_16x16x32_bf16 v[64:67], v[8:11], a[0:3], 0\n"
"v_mul_f32 v1, v243, v1\n"
"v_mul_f32 v2, v246, v2\n"
"v_mfma_f32_16x16x32_bf16 v[64:67], v[24:27], a[4:7], v[64:67]\n"
"ds_read_b128 a[32:35], v224 offset:51200\n"
"v_mul_f32 v3, v247, v3\n"
"v_mfma_f32_16x16x32_bf16 v[64:67], v[40:43], a[8:11], v[64:67]\n"
"ds_read_b128 a[36:39], v224 offset:55296\n"
"v_mul_f32 v4, v242, v4\n"
"v_mfma_f32_16x16x32_bf16 v[64:67], v[56:59], a[12:15], v[64:67]\n"
"ds_read_b128 a[40:43], v224 offset:59392\n"
"v_mul_f32 v5, v243, v5\n"
"v_mfma_f32_16x16x32_bf16 v[96:99], v[60:63], a[12:15], 0\n"
"ds_read_b128 a[44:47], v224 offset:63488\n"
"v_mul_f32 v6, v246, v6\n"
"v_mfma_f32_16x16x32_bf16 v[96:99], v[44:47], a[8:11], v[96:99]\n"
"buffer_load_dwordx4 a[232:235], v248, s[52:55], s13 offen offset:0 sc1\n"
"v_mfma_f32_16x16x32_bf16 v[96:99], v[28:31], a[4:7], v[96:99]\n"
"s_add_u32 s13, s94, 143360\n"
"v_mul_f32 v7, v247, v7\n"
"v_cvt_pk_bf16_f32 v0, v0, v1\n"
"v_mfma_f32_16x16x32_bf16 v[96:99], v[12:15], a[0:3], v[96:99]\n"
"s_waitcnt lgkmcnt(4)\n"
"v_cvt_pk_bf16_f32 v1, v2, v3\n"
"v_cvt_pk_bf16_f32 v2, v4, v5\n"
"v_mfma_f32_16x16x32_bf16 v[68:71], v[8:11], a[16:19], 0\n"
"ds_read_b128 a[0:3], v224 offset:52224\n"
"v_cvt_pk_bf16_f32 v3, v6, v7\n"
"v_mfma_f32_16x16x32_bf16 v[68:71], v[24:27], a[20:23], v[68:71]\n"
"buffer_load_dwordx4 a[236:239], v248, s[52:55], s13 offen offset:0 sc1\n"
"v_mfma_f32_16x16x32_bf16 v[68:71], v[40:43], a[24:27], v[68:71]\n"
"ds_read_b128 a[4:7], v224 offset:56320\n"
"ds_read_b128 a[8:11], v224 offset:60416\n"
"v_mfma_f32_16x16x32_bf16 v[68:71], v[56:59], a[28:31], v[68:71]\n"
"buffer_store_dwordx4 v[0:3], v224, s[52:55], s68 offen offset:0 sc1\n"
"v_mfma_f32_16x16x32_bf16 v[100:103], v[60:63], a[28:31], 0\n"
"s_add_u32 s68, s93, 294912\n"
"ds_read_b128 a[12:15], v224 offset:64512\n"
"v_mul_f32 v32, v225, v32\n"
"v_mfma_f32_16x16x32_bf16 v[100:103], v[44:47], a[24:27], v[100:103]\n"
"v_mul_f32 v33, v238, v33\n"
"v_mul_f32 v34, v252, v34\n"
"v_mfma_f32_16x16x32_bf16 v[100:103], v[28:31], a[20:23], v[100:103]\n"
"v_mul_f32 v35, v253, v35\n"
"v_mul_f32 v36, v225, v36\n"
"v_mfma_f32_16x16x32_bf16 v[100:103], v[12:15], a[16:19], v[100:103]\n"
"s_waitcnt lgkmcnt(4)\n"
"ds_read_b128 a[16:19], v239 offset:0\n"
"v_mul_f32 v37, v238, v37\n"
"v_mfma_f32_16x16x32_bf16 v[80:83], v[8:11], a[32:35], 0\n"
"v_mul_f32 v38, v252, v38\n"
"v_mul_f32 v39, v253, v39\n"
"v_mfma_f32_16x16x32_bf16 v[80:83], v[24:27], a[36:39], v[80:83]\n"
"ds_read_b128 a[20:23], v239 offset:4096\n"
"v_cvt_pk_bf16_f32 v32, v32, v33\n"
"v_mfma_f32_16x16x32_bf16 v[80:83], v[40:43], a[40:43], v[80:83]\n"
"ds_read_b128 a[24:27], v239 offset:8192\n"
"v_cvt_pk_bf16_f32 v33, v34, v35\n"
"v_mfma_f32_16x16x32_bf16 v[80:83], v[56:59], a[44:47], v[80:83]\n"
"buffer_load_dwordx4 v224, s[44:47], s67 offen offset:2048 sc1 lds\n"
"v_mfma_f32_16x16x32_bf16 v[112:115], v[60:63], a[44:47], 0\n"
"ds_read_b128 a[28:31], v239 offset:12288\n"
"v_cvt_pk_bf16_f32 v34, v36, v37\n"
"v_mfma_f32_16x16x32_bf16 v[112:115], v[44:47], a[40:43], v[112:115]\n"
"v_cvt_pk_bf16_f32 v35, v38, v39\n"
"v_fmac_f32 v200, v232, v227\n"
"v_mfma_f32_16x16x32_bf16 v[112:115], v[28:31], a[36:39], v[112:115]\n"
"v_fmac_f32 v201, v233, v227\n"
"v_fmac_f32 v202, v234, v227\n"
"v_mfma_f32_16x16x32_bf16 v[112:115], v[12:15], a[32:35], v[112:115]\n"
"s_waitcnt lgkmcnt(4)\n"
"buffer_load_dwordx4 v224, s[44:47], s67 offen offset:3072 sc1 lds\n"
"v_mfma_f32_16x16x32_bf16 v[84:87], v[8:11], a[0:3], 0\n"
"s_add_u32 m0, s65, 131072\n"
"s_add_u32 s67, s65, 425984\n"
"ds_read_b128 a[32:35], v239 offset:1024\n"
"v_mfma_f32_16x16x32_bf16 v[84:87], v[24:27], a[4:7], v[84:87]\n"
"ds_read_b128 a[36:39], v239 offset:5120\n"
"ds_read_b128 a[40:43], v239 offset:9216\n"
"v_mfma_f32_16x16x32_bf16 v[84:87], v[40:43], a[8:11], v[84:87]\n"
"ds_read_b128 a[44:47], v239 offset:13312\n"
"v_fmac_f32 v203, v235, v227\n"
"v_mfma_f32_16x16x32_bf16 v[84:87], v[56:59], a[12:15], v[84:87]\n"
"buffer_store_dwordx4 v[32:35], v224, s[52:55], s68 offen offset:0 sc1\n"
"v_mfma_f32_16x16x32_bf16 v[116:119], v[60:63], a[12:15], 0\n"
"s_add_u32 s68, s93, 278528\n"
"v_mul_f32 v16, v242, v16\n"
"v_mul_f32 v17, v243, v17\n"
"v_mfma_f32_16x16x32_bf16 v[116:119], v[44:47], a[8:11], v[116:119]\n"
"v_mul_f32 v18, v246, v18\n"
"v_mul_f32 v19, v247, v19\n"
"v_mfma_f32_16x16x32_bf16 v[116:119], v[28:31], a[4:7], v[116:119]\n"
"v_mul_f32 v20, v242, v20\n"
"v_mul_f32 v21, v243, v21\n"
"v_mfma_f32_16x16x32_bf16 v[116:119], v[12:15], a[0:3], v[116:119]\n"
"s_waitcnt lgkmcnt(4)\n"
"v_mul_f32 v232, 0xbfb8aa3b, v200\n"
"v_mul_f32 v22, v246, v22\n"
"v_mfma_f32_16x16x32_bf16 v[64:67], v[72:75], a[16:19], v[64:67]\n"
"v_mul_f32 v23, v247, v23\n"
"v_mul_f32 v233, 0xbfb8aa3b, v201\n"
"v_mfma_f32_16x16x32_bf16 v[64:67], v[88:91], a[20:23], v[64:67]\n"
"v_cvt_pk_bf16_f32 v16, v16, v17\n"
"v_cvt_pk_bf16_f32 v17, v18, v19\n"
"v_mfma_f32_16x16x32_bf16 v[64:67], v[104:107], a[24:27], v[64:67]\n"
"buffer_load_dwordx4 v224, s[44:47], s67 offen offset:0 sc1 lds\n"
"v_mfma_f32_16x16x32_bf16 v[64:67], v[120:123], a[28:31], v[64:67]\n"
"ds_read_b128 a[0:3], v239 offset:2048\n"
"v_mul_f32 v234, 0xbfb8aa3b, v202\n"
"v_mfma_f32_16x16x32_bf16 v[96:99], v[124:127], a[28:31], v[96:99]\n"
"ds_read_b128 a[4:7], v239 offset:6144\n"
"v_cvt_pk_bf16_f32 v18, v20, v21\n"
"v_mfma_f32_16x16x32_bf16 v[96:99], v[108:111], a[24:27], v[96:99]\n"
"ds_read_b128 a[8:11], v239 offset:10240\n"
"v_cvt_pk_bf16_f32 v19, v22, v23\n"
"v_mfma_f32_16x16x32_bf16 v[96:99], v[92:95], a[20:23], v[96:99]\n"
"ds_read_b128 a[12:15], v239 offset:14336\n"
"v_mul_f32 v235, 0xbfb8aa3b, v203\n"
"v_mfma_f32_16x16x32_bf16 v[96:99], v[76:79], a[16:19], v[96:99]\n"
"s_waitcnt lgkmcnt(4)\n"
"buffer_load_dwordx4 v224, s[44:47], s67 offen offset:1024 sc1 lds\n"
"v_mfma_f32_16x16x32_bf16 v[68:71], v[72:75], a[32:35], v[68:71]\n"
"v_exp_f32 v232, v232\n"
"v_mfma_f32_16x16x32_bf16 v[68:71], v[88:91], a[36:39], v[68:71]\n"
"v_exp_f32 v233, v233\n"
"v_mfma_f32_16x16x32_bf16 v[68:71], v[104:107], a[40:43], v[68:71]\n"
"buffer_store_dwordx4 v[16:19], v224, s[52:55], s68 offen offset:0 sc1\n"
"v_mfma_f32_16x16x32_bf16 v[68:71], v[120:123], a[44:47], v[68:71]\n"
"s_add_u32 s68, s93, 311296\n"
"v_exp_f32 v234, v234\n"
"v_mfma_f32_16x16x32_bf16 v[100:103], v[124:127], a[44:47], v[100:103]\n"
"buffer_load_dwordx4 v224, s[44:47], s67 offen offset:2048 sc1 lds\n"
"v_mul_f32 v48, v225, v48\n"
"v_mfma_f32_16x16x32_bf16 v[100:103], v[108:111], a[40:43], v[100:103]\n"
"v_exp_f32 v235, v235\n"
"v_mul_f32 v49, v238, v49\n"
"v_mfma_f32_16x16x32_bf16 v[100:103], v[92:95], a[36:39], v[100:103]\n"
"v_mul_f32 v50, v252, v50\n"
"v_add_f32 v232, 1.0, v232\n"
"v_mul_f32 v51, v253, v51\n"
"v_mfma_f32_16x16x32_bf16 v[100:103], v[76:79], a[32:35], v[100:103]\n"
"s_waitcnt lgkmcnt(0)\n"
"ds_read_b128 a[16:19], v239 offset:3072\n"
"v_add_f32 v233, 1.0, v233\n"
"v_mul_f32 v52, v225, v52\n"
"v_mfma_f32_16x16x32_bf16 v[80:83], v[72:75], a[0:3], v[80:83]\n"
"s_waitcnt vmcnt(19)\n"
"s_barrier\n"
"ds_read_b128 a[20:23], v239 offset:7168\n"
"ds_read_b128 a[24:27], v239 offset:11264\n"
"ds_read_b128 a[28:31], v239 offset:15360\n"
"v_mul_f32 v53, v238, v53\n"
"v_mfma_f32_16x16x32_bf16 v[80:83], v[88:91], a[4:7], v[80:83]\n"
"buffer_load_dwordx4 v224, s[44:47], s67 offen offset:3072 sc1 lds\n"
"s_add_u32 m0, s65, 147456\n"
"s_add_u32 s67, s65, 442368\n"
"v_add_f32 v234, 1.0, v234\n"
"v_mul_f32 v54, v252, v54\n"
"v_mfma_f32_16x16x32_bf16 v[80:83], v[104:107], a[8:11], v[80:83]\n"
"v_add_f32 v235, 1.0, v235\n"
"v_mul_f32 v55, v253, v55\n"
"v_mfma_f32_16x16x32_bf16 v[80:83], v[120:123], a[12:15], v[80:83]\n"
"v_cvt_pk_bf16_f32 v48, v48, v49\n"
"v_cvt_pk_bf16_f32 v49, v50, v51\n"
"v_mfma_f32_16x16x32_bf16 v[112:115], v[124:127], a[12:15], v[112:115]\n"
"v_rcp_f32 v232, v232\n"
"v_mfma_f32_16x16x32_bf16 v[112:115], v[108:111], a[8:11], v[112:115]\n"
"v_rcp_f32 v233, v233\n"
"v_cvt_pk_bf16_f32 v50, v52, v53\n"
"v_mfma_f32_16x16x32_bf16 v[112:115], v[92:95], a[4:7], v[112:115]\n"
"buffer_load_dwordx4 v224, s[44:47], s67 offen offset:0 sc1 lds\n"
"v_cvt_pk_bf16_f32 v51, v54, v55\n"
"v_mfma_f32_16x16x32_bf16 v[112:115], v[76:79], a[0:3], v[112:115]\n"
"s_waitcnt lgkmcnt(0)\n"
"buffer_store_dwordx4 v[48:51], v224, s[52:55], s68 offen offset:0 sc1\n"
"v_rcp_f32 v234, v234\n"
"v_mfma_f32_16x16x32_bf16 v[84:87], v[72:75], a[16:19], v[84:87]\n"
"ds_read_b128 a[32:35], v224 offset:32768\n"
"ds_read_b128 a[36:39], v224 offset:36864\n"
"ds_read_b128 a[40:43], v224 offset:40960\n"
"ds_read_b128 a[44:47], v224 offset:45056\n"
"v_rcp_f32 v235, v235\n"
"v_mfma_f32_16x16x32_bf16 v[84:87], v[88:91], a[20:23], v[84:87]\n"
"v_mul_f32 v200, v200, v232\n"
"v_mul_f32 v201, v201, v233\n"
"v_mfma_f32_16x16x32_bf16 v[84:87], v[104:107], a[24:27], v[84:87]\n"
"ds_read_b128 a[0:3], v224 offset:33792\n"
"v_mul_f32 v202, v202, v234\n"
"v_mfma_f32_16x16x32_bf16 v[84:87], v[120:123], a[28:31], v[84:87]\n"
"buffer_load_dwordx4 v224, s[44:47], s67 offen offset:1024 sc1 lds\n"
"v_mfma_f32_16x16x32_bf16 v[116:119], v[124:127], a[28:31], v[116:119]\n"
"ds_read_b128 a[4:7], v224 offset:37888\n"
"v_mul_f32 v203, v203, v235\n"
"v_mfma_f32_16x16x32_bf16 v[116:119], v[108:111], a[24:27], v[116:119]\n"
"ds_read_b128 a[8:11], v224 offset:41984\n"
"v_and_b32 v205, 0xffff0000, v204\n"
"v_mfma_f32_16x16x32_bf16 v[116:119], v[92:95], a[20:23], v[116:119]\n"
"ds_read_b128 a[12:15], v224 offset:46080\n"
"v_lshlrev_b32 v204, 16, v204\n"
"v_mfma_f32_16x16x32_bf16 v[116:119], v[76:79], a[16:19], v[116:119]\n"
"s_waitcnt lgkmcnt(4)\n"
"v_and_b32 v207, 0xffff0000, v206\n"
"v_lshlrev_b32 v206, 16, v206\n"
"v_mfma_f32_16x16x32_bf16 v[64:67], v[128:131], a[32:35], v[64:67]\n"
"buffer_load_dwordx4 v224, s[44:47], s67 offen offset:2048 sc1 lds\n"
"v_mfma_f32_16x16x32_bf16 v[64:67], v[136:139], a[36:39], v[64:67]\n"
"ds_read_b128 a[16:19], v224 offset:34816\n"
"v_add_f32 v204, v226, v204\n"
"v_mfma_f32_16x16x32_bf16 v[64:67], v[144:147], a[40:43], v[64:67]\n"
"ds_read_b128 a[20:23], v224 offset:38912\n"
"ds_read_b128 a[24:27], v224 offset:43008\n"
"v_mfma_f32_16x16x32_bf16 v[64:67], v[152:155], a[44:47], v[64:67]\n"
"ds_read_b128 a[28:31], v224 offset:47104\n"
"v_add_f32 v205, v226, v205\n"
"v_mfma_f32_16x16x32_bf16 v[96:99], v[156:159], a[44:47], v[96:99]\n"
"v_add_f32 v206, v226, v206\n"
"v_add_f32 v207, v226, v207\n"
"v_mfma_f32_16x16x32_bf16 v[96:99], v[148:151], a[40:43], v[96:99]\n"
"buffer_load_dwordx4 v224, s[44:47], s67 offen offset:3072 sc1 lds\n"
"v_mfma_f32_16x16x32_bf16 v[96:99], v[140:143], a[36:39], v[96:99]\n"
"v_lshlrev_b32 v232, 16, v214\n"
"v_and_b32 v233, 0xffff0000, v214\n"
"v_mfma_f32_16x16x32_bf16 v[96:99], v[132:135], a[32:35], v[96:99]\n"
"s_waitcnt lgkmcnt(4)\n"
"v_lshlrev_b32 v234, 16, v215\n"
"v_and_b32 v235, 0xffff0000, v215\n"
"v_mfma_f32_16x16x32_bf16 v[68:71], v[128:131], a[0:3], v[68:71]\n"
"ds_read_b128 a[32:35], v224 offset:35840\n"
"v_fmac_f32 v204, v232, v227\n"
"v_mfma_f32_16x16x32_bf16 v[68:71], v[136:139], a[4:7], v[68:71]\n"
"s_add_u32 s13, s1, 49152\n"
"ds_read_b128 a[36:39], v224 offset:39936\n"
"v_fmac_f32 v205, v233, v227\n"
"v_mfma_f32_16x16x32_bf16 v[68:71], v[144:147], a[8:11], v[68:71]\n"
"ds_read_b128 a[40:43], v224 offset:44032\n"
"v_fmac_f32 v206, v234, v227\n"
"v_mfma_f32_16x16x32_bf16 v[68:71], v[152:155], a[12:15], v[68:71]\n"
"ds_read_b128 a[44:47], v224 offset:48128\n"
"v_fmac_f32 v207, v235, v227\n"
"v_mfma_f32_16x16x32_bf16 v[100:103], v[156:159], a[12:15], v[100:103]\n"
"v_mul_f32 v232, 0xbfb8aa3b, v204\n"
"v_mul_f32 v233, 0xbfb8aa3b, v205\n"
"v_mfma_f32_16x16x32_bf16 v[100:103], v[148:151], a[8:11], v[100:103]\n"
"buffer_load_dwordx2 v[244:245], v249, s[48:51], s13 offen offset:0 sc1\n"
"v_mfma_f32_16x16x32_bf16 v[100:103], v[140:143], a[4:7], v[100:103]\n"
"v_mul_f32 v234, 0xbfb8aa3b, v206\n"
"v_mul_f32 v235, 0xbfb8aa3b, v207\n"
"v_mfma_f32_16x16x32_bf16 v[100:103], v[132:135], a[0:3], v[100:103]\n"
"s_waitcnt lgkmcnt(4)\n"
"ds_read_b128 a[0:3], v239 offset:16384\n"
"v_mfma_f32_16x16x32_bf16 v[80:83], v[128:131], a[16:19], v[80:83]\n"
"ds_read_b128 a[4:7], v239 offset:20480\n"
"v_mfma_f32_16x16x32_bf16 v[80:83], v[136:139], a[20:23], v[80:83]\n"
"ds_read_b128 a[8:11], v239 offset:24576\n"
"v_mfma_f32_16x16x32_bf16 v[80:83], v[144:147], a[24:27], v[80:83]\n"
"ds_read_b128 a[12:15], v239 offset:28672\n"
"v_mfma_f32_16x16x32_bf16 v[80:83], v[152:155], a[28:31], v[80:83]\n"
"v_exp_f32 v232, v232\n"
"v_mfma_f32_16x16x32_bf16 v[112:115], v[156:159], a[28:31], v[112:115]\n"
"v_exp_f32 v233, v233\n"
"v_mfma_f32_16x16x32_bf16 v[112:115], v[148:151], a[24:27], v[112:115]\n"
"v_exp_f32 v234, v234\n"
"v_mfma_f32_16x16x32_bf16 v[112:115], v[140:143], a[20:23], v[112:115]\n"
"v_exp_f32 v235, v235\n"
"v_mfma_f32_16x16x32_bf16 v[112:115], v[132:135], a[16:19], v[112:115]\n"
"s_waitcnt vmcnt(15)\n"
"ds_read_b128 a[16:19], v239 offset:17408\n"
"v_add_f32 v232, 1.0, v232\n"
"v_mfma_f32_16x16x32_bf16 v[220:223], a[224:227], a[48:51], 0\n"
"ds_read_b128 a[20:23], v239 offset:21504\n"
"v_add_f32 v233, 1.0, v233\n"
"v_mfma_f32_16x16x32_bf16 v[220:223], a[228:231], a[48:51], v[220:223]\n"
"ds_read_b128 a[24:27], v239 offset:25600\n"
"v_add_f32 v234, 1.0, v234\n"
"v_mfma_f32_16x16x32_bf16 v[220:223], a[232:235], a[48:51], v[220:223]\n"
"ds_read_b128 a[28:31], v239 offset:29696\n"
"v_add_f32 v235, 1.0, v235\n"
"v_mfma_f32_16x16x32_bf16 v[220:223], a[236:239], a[48:51], v[220:223]\n"
"s_waitcnt lgkmcnt(8)\n"
"v_rcp_f32 v232, v232\n"
"v_mfma_f32_16x16x32_bf16 v[84:87], v[128:131], a[32:35], v[84:87]\n"
"v_rcp_f32 v233, v233\n"
"v_mfma_f32_16x16x32_bf16 v[84:87], v[136:139], a[36:39], v[84:87]\n"
"v_rcp_f32 v234, v234\n"
"v_mfma_f32_16x16x32_bf16 v[84:87], v[144:147], a[40:43], v[84:87]\n"
"v_rcp_f32 v235, v235\n"
"v_mfma_f32_16x16x32_bf16 v[84:87], v[152:155], a[44:47], v[84:87]\n"
"v_mul_f32 v204, v204, v232\n"
"v_mul_f32 v205, v205, v233\n"
"v_mfma_f32_16x16x32_bf16 v[116:119], v[156:159], a[44:47], v[116:119]\n"
"v_mul_f32 v206, v206, v234\n"
"v_mul_f32 v207, v207, v235\n"
"v_mfma_f32_16x16x32_bf16 v[116:119], v[148:151], a[40:43], v[116:119]\n"
"v_mfma_f32_16x16x32_bf16 v[116:119], v[140:143], a[36:39], v[116:119]\n"
"v_mfma_f32_16x16x32_bf16 v[116:119], v[132:135], a[32:35], v[116:119]\n"
"s_waitcnt lgkmcnt(4)\n"
"ds_read_b128 a[32:35], v239 offset:18432\n"
"ds_read_b128 a[36:39], v239 offset:22528\n"
"v_mfma_f32_16x16x32_bf16 v[64:67], v[160:163], a[0:3], v[64:67]\n"
"ds_read_b128 a[40:43], v239 offset:26624\n"
"ds_read_b128 a[44:47], v239 offset:30720\n"
"v_mfma_f32_16x16x32_bf16 v[64:67], v[168:171], a[4:7], v[64:67]\n"
"v_mfma_f32_16x16x32_bf16 v[64:67], v[176:179], a[8:11], v[64:67]\n"
"v_mfma_f32_16x16x32_bf16 v[64:67], v[184:187], a[12:15], v[64:67]\n"
"v_mfma_f32_16x16x32_bf16 v[96:99], v[188:191], a[12:15], v[96:99]\n"
"v_mfma_f32_16x16x32_bf16 v[96:99], v[180:183], a[8:11], v[96:99]\n"
"v_mfma_f32_16x16x32_bf16 v[96:99], v[172:175], a[4:7], v[96:99]\n"
"v_mfma_f32_16x16x32_bf16 v[96:99], v[164:167], a[0:3], v[96:99]\n"
"s_waitcnt lgkmcnt(4)\n"
"ds_read_b128 a[0:3], v239 offset:19456\n"
"ds_read_b128 a[4:7], v239 offset:23552\n"
"v_mfma_f32_16x16x32_bf16 v[68:71], v[160:163], a[16:19], v[68:71]\n"
"ds_read_b128 a[8:11], v239 offset:27648\n"
"ds_read_b128 a[12:15], v239 offset:31744\n"
"v_mfma_f32_16x16x32_bf16 v[68:71], v[168:171], a[20:23], v[68:71]\n"
"v_mfma_f32_16x16x32_bf16 v[68:71], v[176:179], a[24:27], v[68:71]\n"
"v_mfma_f32_16x16x32_bf16 v[68:71], v[184:187], a[28:31], v[68:71]\n"
"v_mfma_f32_16x16x32_bf16 v[100:103], v[188:191], a[28:31], v[100:103]\n"
"v_mfma_f32_16x16x32_bf16 v[100:103], v[180:183], a[24:27], v[100:103]\n"
"v_mfma_f32_16x16x32_bf16 v[100:103], v[172:175], a[20:23], v[100:103]\n"
"v_mfma_f32_16x16x32_bf16 v[100:103], v[164:167], a[16:19], v[100:103]\n"
"s_waitcnt lgkmcnt(4)\n"
"s_add_u32 s71, s0, 65536\n"
"s_waitcnt vmcnt(4)\n"
"v_mfma_f32_16x16x32_bf16 v[80:83], v[160:163], a[32:35], v[80:83]\n"
"s_barrier\n"
"ds_read_b128 a[16:19], v239 offset:32768\n"
"v_mfma_f32_16x16x32_bf16 v[80:83], v[168:171], a[36:39], v[80:83]\n"
"buffer_store_dwordx2 v[250:251], v241, s[48:51], s71 offen offset:0 sc1\n"
"v_mfma_f32_16x16x32_bf16 v[80:83], v[176:179], a[40:43], v[80:83]\n"
"ds_read_b128 a[20:23], v239 offset:36864\n"
"ds_read_b128 a[24:27], v239 offset:40960\n"
"v_mfma_f32_16x16x32_bf16 v[80:83], v[184:187], a[44:47], v[80:83]\n"
"ds_read_b128 a[28:31], v239 offset:45056\n"
"v_lshlrev_b32 v232, 16, v208\n"
"v_mfma_f32_16x16x32_bf16 v[112:115], v[188:191], a[44:47], v[112:115]\n"
"v_and_b32 v233, 0xffff0000, v208\n"
"v_lshlrev_b32 v234, 16, v209\n"
"v_mfma_f32_16x16x32_bf16 v[112:115], v[180:183], a[40:43], v[112:115]\n"
"v_and_b32 v235, 0xffff0000, v209\n"
"v_fmac_f32 v232, v216, v192\n"
"v_mfma_f32_16x16x32_bf16 v[112:115], v[172:175], a[36:39], v[112:115]\n"
"v_fmac_f32 v233, v217, v193\n"
"v_fmac_f32 v234, v218, v194\n"
"v_mfma_f32_16x16x32_bf16 v[112:115], v[164:167], a[32:35], v[112:115]\n"
"s_waitcnt lgkmcnt(4)\n"
"s_waitcnt vmcnt(1)\n"
"v_cmp_eq_u32_e64 s[14:15], s86, v244\n"
"v_mfma_f32_16x16x32_bf16 v[84:87], v[160:163], a[0:3], v[84:87]\n"
"ds_read_b128 a[32:35], v239 offset:33792\n"
"v_cmp_eq_u32_e64 s[16:17], s87, v245\n"
"v_mfma_f32_16x16x32_bf16 v[84:87], v[168:171], a[4:7], v[84:87]\n"
"s_and_b64 s[14:15], s[14:15], s[16:17]\n"
"ds_read_b128 a[36:39], v239 offset:37888\n"
"ds_read_b128 a[40:43], v239 offset:41984\n"
"v_mfma_f32_16x16x32_bf16 v[84:87], v[176:179], a[8:11], v[84:87]\n"
"s_cmp_eq_u64 s[14:15], -1\n"
"s_cbranch_scc0 SPC3_%=\n"
"SBC3_%=:\n"
"ds_read_b128 a[44:47], v239 offset:46080\n"
"v_mfma_f32_16x16x32_bf16 v[84:87], v[184:187], a[12:15], v[84:87]\n"
"s_add_u32 s13, s94, 196608\n"
"buffer_load_dwordx4 a[240:243], v248, s[52:55], s13 offen offset:0 sc1\n"
"v_mfma_f32_16x16x32_bf16 v[116:119], v[188:191], a[12:15], v[116:119]\n"
"s_add_u32 s13, s94, 200704\n"
"v_fmac_f32 v235, v219, v195\n"
"v_cvt_pk_bf16_f32 v230, v232, v233\n"
"v_mfma_f32_16x16x32_bf16 v[116:119], v[180:183], a[8:11], v[116:119]\n"
"buffer_load_dwordx4 a[244:247], v248, s[52:55], s13 offen offset:0 sc1\n"
"v_mfma_f32_16x16x32_bf16 v[116:119], v[172:175], a[4:7], v[116:119]\n"
"s_add_u32 s13, s94, 204800\n"
"v_cvt_pk_bf16_f32 v231, v234, v235\n"
"v_mfma_f32_16x16x32_bf16 v[116:119], v[164:167], a[0:3], v[116:119]\n"
"s_waitcnt lgkmcnt(4)\n"
"s_add_u32 s68, s93, 327680\n"
"v_mul_f32 v64, v242, v64\n"
"v_mfma_f32_16x16x32_bf16 v[0:3], v[8:11], a[16:19], 0\n"
"v_mul_f32 v65, v243, v65\n"
"v_mul_f32 v66, v246, v66\n"
"v_mfma_f32_16x16x32_bf16 v[0:3], v[24:27], a[20:23], v[0:3]\n"
"ds_read_b128 a[0:3], v239 offset:49152\n"
"v_mul_f32 v67, v247, v67\n"
"v_mfma_f32_16x16x32_bf16 v[0:3], v[40:43], a[24:27], v[0:3]\n"
"ds_read_b128 a[4:7], v239 offset:53248\n"
"v_mul_f32 v68, v242, v68\n"
"v_mfma_f32_16x16x32_bf16 v[0:3], v[56:59], a[28:31], v[0:3]\n"
"ds_read_b128 a[8:11], v239 offset:57344\n"
"v_mul_f32 v69, v243, v69\n"
"v_mfma_f32_16x16x32_bf16 v[32:35], v[60:63], a[28:31], 0\n"
"ds_read_b128 a[12:15], v239 offset:61440\n"
"v_mul_f32 v70, v246, v70\n"
"v_mfma_f32_16x16x32_bf16 v[32:35], v[44:47], a[24:27], v[32:35]\n"
"buffer_load_dwordx4 a[248:251], v248, s[52:55], s13 offen offset:0 sc1\n"
"v_mfma_f32_16x16x32_bf16 v[32:35], v[28:31], a[20:23], v[32:35]\n"
"s_add_u32 s13, s94, 208896\n"
"v_mul_f32 v71, v247, v71\n"
"v_cvt_pk_bf16_f32 v64, v64, v65\n"
"v_mfma_f32_16x16x32_bf16 v[32:35], v[12:15], a[16:19], v[32:35]\n"
"s_waitcnt lgkmcnt(4)\n"
"v_cvt_pk_bf16_f32 v65, v66, v67\n"
"v_cvt_pk_bf16_f32 v66, v68, v69\n"
"v_mfma_f32_16x16x32_bf16 v[4:7], v[8:11], a[32:35], 0\n"
"ds_read_b128 a[16:19], v239 offset:50176\n"
"v_cvt_pk_bf16_f32 v67, v70, v71\n"
"v_mfma_f32_16x16x32_bf16 v[4:7], v[24:27], a[36:39], v[4:7]\n"
"buffer_load_dwordx4 a[252:255], v248, s[52:55], s13 offen offset:0 sc1\n"
"v_mfma_f32_16x16x32_bf16 v[4:7], v[40:43], a[40:43], v[4:7]\n"
"ds_read_b128 a[20:23], v239 offset:54272\n"
"ds_read_b128 a[24:27], v239 offset:58368\n"
"v_mfma_f32_16x16x32_bf16 v[4:7], v[56:59], a[44:47], v[4:7]\n"
"buffer_store_dwordx4 v[64:67], v224, s[52:55], s68 offen offset:0 sc1\n"
"v_mfma_f32_16x16x32_bf16 v[36:39], v[60:63], a[44:47], 0\n"
"s_add_u32 s68, s93, 360448\n"
"ds_read_b128 a[28:31], v239 offset:62464\n"
"v_mul_f32 v96, v225, v96\n"
"v_mfma_f32_16x16x32_bf16 v[36:39], v[44:47], a[40:43], v[36:39]\n"
"v_mul_f32 v97, v238, v97\n"
"v_mul_f32 v98, v252, v98\n"
"v_mfma_f32_16x16x32_bf16 v[36:39], v[28:31], a[36:39], v[36:39]\n"
"v_mul_f32 v99, v253, v99\n"
"v_mul_f32 v100, v225, v100\n"
"v_mfma_f32_16x16x32_bf16 v[36:39], v[12:15], a[32:35], v[36:39]\n"
"s_waitcnt lgkmcnt(4)\n"
"v_mul_f32 v101, v238, v101\n"
"v_mul_f32 v102, v252, v102\n"
"v_mfma_f32_16x16x32_bf16 v[0:3], v[72:75], a[0:3], v[0:3]\n"
"s_barrier\n"
"ds_read_b128 a[32:35], v240 offset:0\n"
"v_mfma_f32_16x16x32_bf16 v[0:3], v[88:91], a[4:7], v[0:3]\n"
"ds_read_b128 a[36:39], v240 offset:4096\n"
"v_mul_f32 v103, v253, v103\n"
"v_mfma_f32_16x16x32_bf16 v[0:3], v[104:107], a[8:11], v[0:3]\n"
"ds_read_b128 a[40:43], v240 offset:8192\n"
"v_cvt_pk_bf16_f32 v96, v96, v97\n"
"v_mfma_f32_16x16x32_bf16 v[0:3], v[120:123], a[12:15], v[0:3]\n"
"ds_read_b128 a[44:47], v240 offset:12288\n"
"v_cvt_pk_bf16_f32 v97, v98, v99\n"
"v_mfma_f32_16x16x32_bf16 v[32:35], v[124:127], a[12:15], v[32:35]\n"
"buffer_store_dwordx2 v[230:231], v236, s[60:63], 0 offen offset:0 sc1\n"
"v_mfma_f32_16x16x32_bf16 v[32:35], v[108:111], a[8:11], v[32:35]\n"
"v_cvt_pk_bf16_f32 v98, v100, v101\n"
"v_cvt_pk_bf16_f32 v99, v102, v103\n"
"v_mfma_f32_16x16x32_bf16 v[32:35], v[92:95], a[4:7], v[32:35]\n"
"buffer_store_dwordx4 v[96:99], v224, s[52:55], s68 offen offset:0 sc1\n"
"v_mfma_f32_16x16x32_bf16 v[32:35], v[76:79], a[0:3], v[32:35]\n"
"s_waitcnt lgkmcnt(4)\n"
"s_add_u32 s68, s93, 344064\n"
"ds_read_b128 a[0:3], v240 offset:1024\n"
"v_mfma_f32_16x16x32_bf16 v[4:7], v[72:75], a[16:19], v[4:7]\n"
"ds_read_b128 a[4:7], v240 offset:5120\n"
"v_mul_f32 v80, v242, v80\n"
"v_mfma_f32_16x16x32_bf16 v[4:7], v[88:91], a[20:23], v[4:7]\n"
"ds_read_b128 a[8:11], v240 offset:9216\n"
"v_mul_f32 v81, v243, v81\n"
"v_mfma_f32_16x16x32_bf16 v[4:7], v[104:107], a[24:27], v[4:7]\n"
"ds_read_b128 a[12:15], v240 offset:13312\n"
"v_mul_f32 v82, v246, v82\n"
"v_mfma_f32_16x16x32_bf16 v[4:7], v[120:123], a[28:31], v[4:7]\n"
"v_mul_f32 v83, v247, v83\n"
"v_mul_f32 v84, v242, v84\n"
"v_mfma_f32_16x16x32_bf16 v[36:39], v[124:127], a[28:31], v[36:39]\n"
"v_mul_f32 v85, v243, v85\n"
"v_mul_f32 v86, v246, v86\n"
"v_mfma_f32_16x16x32_bf16 v[36:39], v[108:111], a[24:27], v[36:39]\n"
"v_mul_f32 v87, v247, v87\n"
"v_cvt_pk_bf16_f32 v80, v80, v81\n"
"v_mfma_f32_16x16x32_bf16 v[36:39], v[92:95], a[20:23], v[36:39]\n"
"v_cvt_pk_bf16_f32 v81, v82, v83\n"
"v_cvt_pk_bf16_f32 v82, v84, v85\n"
"v_mfma_f32_16x16x32_bf16 v[36:39], v[76:79], a[16:19], v[36:39]\n"
"s_waitcnt lgkmcnt(4)\n"
"ds_read_b128 a[16:19], v240 offset:16384\n"
"ds_read_b128 a[20:23], v240 offset:20480\n"
"v_mfma_f32_16x16x32_bf16 v[0:3], v[128:131], a[32:35], v[0:3]\n"
"ds_read_b128 a[24:27], v240 offset:24576\n"
"v_cvt_pk_bf16_f32 v83, v86, v87\n"
"v_mfma_f32_16x16x32_bf16 v[0:3], v[136:139], a[36:39], v[0:3]\n"
"buffer_store_dwordx4 v[80:83], v224, s[52:55], s68 offen offset:0 sc1\n"
"v_mfma_f32_16x16x32_bf16 v[0:3], v[144:147], a[40:43], v[0:3]\n"
"s_add_u32 s68, s93, 376832\n"
"ds_read_b128 a[28:31], v240 offset:28672\n"
"v_mul_f32 v112, v225, v112\n"
"v_mfma_f32_16x16x32_bf16 v[0:3], v[152:155], a[44:47], v[0:3]\n"
"v_mul_f32 v113, v238, v113\n"
"v_mul_f32 v114, v252, v114\n"
"v_mfma_f32_16x16x32_bf16 v[32:35], v[156:159], a[44:47], v[32:35]\n"
"v_mul_f32 v115, v253, v115\n"
"v_mul_f32 v116, v225, v116\n"
"v_mfma_f32_16x16x32_bf16 v[32:35], v[148:151], a[40:43], v[32:35]\n"
"v_mul_f32 v117, v238, v117\n"
"v_mul_f32 v118, v252, v118\n"
"v_mfma_f32_16x16x32_bf16 v[32:35], v[140:143], a[36:39], v[32:35]\n"
"v_mul_f32 v119, v253, v119\n"
"v_cvt_pk_bf16_f32 v112, v112, v113\n"
"v_mfma_f32_16x16x32_bf16 v[32:35], v[132:135], a[32:35], v[32:35]\n"
"s_waitcnt lgkmcnt(4)\n"
"ds_read_b128 a[32:35], v240 offset:17408\n"
"ds_read_b128 a[36:39], v240 offset:21504\n"
"v_mfma_f32_16x16x32_bf16 v[4:7], v[128:131], a[0:3], v[4:7]\n"
"ds_read_b128 a[40:43], v240 offset:25600\n"
"ds_read_b128 a[44:47], v240 offset:29696\n"
"v_mfma_f32_16x16x32_bf16 v[4:7], v[136:139], a[4:7], v[4:7]\n"
"v_cvt_pk_bf16_f32 v113, v114, v115\n"
"v_cvt_pk_bf16_f32 v114, v116, v117\n"
"v_mfma_f32_16x16x32_bf16 v[4:7], v[144:147], a[8:11], v[4:7]\n"
"v_cvt_pk_bf16_f32 v115, v118, v119\n"
"v_mfma_f32_16x16x32_bf16 v[4:7], v[152:155], a[12:15], v[4:7]\n"
"buffer_store_dwordx4 v[112:115], v224, s[52:55], s68 offen offset:0 sc1\n"
"v_mfma_f32_16x16x32_bf16 v[36:39], v[156:159], a[12:15], v[36:39]\n"
"v_mfma_f32_16x16x32_bf16 v[36:39], v[148:151], a[8:11], v[36:39]\n"
"v_mfma_f32_16x16x32_bf16 v[36:39], v[140:143], a[4:7], v[36:39]\n"
"v_mfma_f32_16x16x32_bf16 v[36:39], v[132:135], a[0:3], v[36:39]\n"
"s_waitcnt lgkmcnt(4)\n"
"ds_read_b128 a[0:3], v239 offset:34816\n"
"ds_read_b128 a[4:7], v239 offset:38912\n"
"v_mfma_f32_16x16x32_bf16 v[0:3], v[160:163], a[16:19], v[0:3]\n"
"ds_read_b128 a[8:11], v239 offset:43008\n"
"ds_read_b128 a[12:15], v239 offset:47104\n"
"v_mfma_f32_16x16x32_bf16 v[0:3], v[168:171], a[20:23], v[0:3]\n"
"v_mfma_f32_16x16x32_bf16 v[0:3], v[176:179], a[24:27], v[0:3]\n"
"v_mfma_f32_16x16x32_bf16 v[0:3], v[184:187], a[28:31], v[0:3]\n"
"v_mfma_f32_16x16x32_bf16 v[32:35], v[188:191], a[28:31], v[32:35]\n"
"v_mfma_f32_16x16x32_bf16 v[32:35], v[180:183], a[24:27], v[32:35]\n"
"v_mfma_f32_16x16x32_bf16 v[32:35], v[172:175], a[20:23], v[32:35]\n"
"v_mfma_f32_16x16x32_bf16 v[32:35], v[164:167], a[16:19], v[32:35]\n"
"s_waitcnt lgkmcnt(4)\n"
"ds_read_b128 a[16:19], v239 offset:35840\n"
"ds_read_b128 a[20:23], v239 offset:39936\n"
"v_mfma_f32_16x16x32_bf16 v[4:7], v[160:163], a[32:35], v[4:7]\n"
"ds_read_b128 a[24:27], v239 offset:44032\n"
"ds_read_b128 a[28:31], v239 offset:48128\n"
"v_mfma_f32_16x16x32_bf16 v[4:7], v[168:171], a[36:39], v[4:7]\n"
"v_mfma_f32_16x16x32_bf16 v[4:7], v[176:179], a[40:43], v[4:7]\n"
"v_mfma_f32_16x16x32_bf16 v[4:7], v[184:187], a[44:47], v[4:7]\n"
"v_mfma_f32_16x16x32_bf16 v[36:39], v[188:191], a[44:47], v[36:39]\n"
"v_mfma_f32_16x16x32_bf16 v[36:39], v[180:183], a[40:43], v[36:39]\n"
"v_mfma_f32_16x16x32_bf16 v[36:39], v[172:175], a[36:39], v[36:39]\n"
"v_mfma_f32_16x16x32_bf16 v[36:39], v[164:167], a[32:35], v[36:39]\n"
"s_waitcnt lgkmcnt(4)\n"
"s_add_u32 s68, s93, 393216\n"
"v_mul_f32 v0, v242, v0\n"
"v_mfma_f32_16x16x32_bf16 v[16:19], v[8:11], a[0:3], 0\n"
"v_mul_f32 v1, v243, v1\n"
"v_mul_f32 v2, v246, v2\n"
"v_mfma_f32_16x16x32_bf16 v[16:19], v[24:27], a[4:7], v[16:19]\n"
"v_mul_f32 v3, v247, v3\n"
"v_mul_f32 v4, v242, v4\n"
"v_mfma_f32_16x16x32_bf16 v[16:19], v[40:43], a[8:11], v[16:19]\n"
"v_mul_f32 v5, v243, v5\n"
"v_mul_f32 v6, v246, v6\n"
"v_mfma_f32_16x16x32_bf16 v[16:19], v[56:59], a[12:15], v[16:19]\n"
"ds_read_b128 a[32:35], v239 offset:51200\n"
"v_mul_f32 v7, v247, v7\n"
"v_mfma_f32_16x16x32_bf16 v[48:51], v[60:63], a[12:15], 0\n"
"ds_read_b128 a[36:39], v239 offset:55296\n"
"v_cvt_pk_bf16_f32 v0, v0, v1\n"
"v_mfma_f32_16x16x32_bf16 v[48:51], v[44:47], a[8:11], v[48:51]\n"
"ds_read_b128 a[40:43], v239 offset:59392\n"
"v_cvt_pk_bf16_f32 v1, v2, v3\n"
"v_mfma_f32_16x16x32_bf16 v[48:51], v[28:31], a[4:7], v[48:51]\n"
"ds_read_b128 a[44:47], v239 offset:63488\n"
"v_cvt_pk_bf16_f32 v2, v4, v5\n"
"v_mfma_f32_16x16x32_bf16 v[48:51], v[12:15], a[0:3], v[48:51]\n"
"s_waitcnt lgkmcnt(4)\n"
"ds_read_b128 a[0:3], v239 offset:52224\n"
"v_cvt_pk_bf16_f32 v3, v6, v7\n"
"IWB10_%=:\n"
"s_cbranch_execz IRB_%=\n"
"v_mfma_f32_16x16x32_bf16 v[20:23], v[8:11], a[16:19], 0\n"
"buffer_store_dwordx4 v[0:3], v224, s[52:55], s68 offen offset:0 sc1\n"
"v_mfma_f32_16x16x32_bf16 v[20:23], v[24:27], a[20:23], v[20:23]\n"
"s_add_u32 s68, s93, 425984\n"
"v_mul_f32 v32, v225, v32\n"
"v_mul_f32 v33, v238, v33\n"
"v_mfma_f32_16x16x32_bf16 v[20:23], v[40:43], a[24:27], v[20:23]\n"
"v_mul_f32 v34, v252, v34\n"
"v_mul_f32 v35, v253, v35\n"
"v_mfma_f32_16x16x32_bf16 v[20:23], v[56:59], a[28:31], v[20:23]\n"
"v_mul_f32 v36, v225, v36\n"
"v_mul_f32 v37, v238, v37\n"
"v_mfma_f32_16x16x32_bf16 v[52:55], v[60:63], a[28:31], 0\n"
"ds_read_b128 a[4:7], v239 offset:56320\n"
"v_mul_f32 v38, v252, v38\n"
"v_mfma_f32_16x16x32_bf16 v[52:55], v[44:47], a[24:27], v[52:55]\n"
"ds_read_b128 a[8:11], v239 offset:60416\n"
"v_mul_f32 v39, v253, v39\n"
"v_mfma_f32_16x16x32_bf16 v[52:55], v[28:31], a[20:23], v[52:55]\n"
"ds_read_b128 a[12:15], v239 offset:64512\n"
"v_cvt_pk_bf16_f32 v32, v32, v33\n"
"v_mfma_f32_16x16x32_bf16 v[52:55], v[12:15], a[16:19], v[52:55]\n"
"s_waitcnt lgkmcnt(4)\n"
"v_cvt_pk_bf16_f32 v33, v34, v35\n"
"v_cvt_pk_bf16_f32 v34, v36, v37\n"
"v_mfma_f32_16x16x32_bf16 v[16:19], v[72:75], a[32:35], v[16:19]\n"
"ds_read_b128 a[16:19], v240 offset:2048\n"
"v_cvt_pk_bf16_f32 v35, v38, v39\n"
"v_mfma_f32_16x16x32_bf16 v[16:19], v[88:91], a[36:39], v[16:19]\n"
"buffer_store_dwordx4 v[32:35], v224, s[52:55], s68 offen offset:0 sc1\n"
"v_mfma_f32_16x16x32_bf16 v[16:19], v[104:107], a[40:43], v[16:19]\n"
"ds_read_b128 a[20:23], v240 offset:6144\n"
"v_mfma_f32_16x16x32_bf16 v[16:19], v[120:123], a[44:47], v[16:19]\n"
"ds_read_b128 a[24:27], v240 offset:10240\n"
"v_mfma_f32_16x16x32_bf16 v[48:51], v[124:127], a[44:47], v[48:51]\n"
"ds_read_b128 a[28:31], v240 offset:14336\n"
"v_mfma_f32_16x16x32_bf16 v[48:51], v[108:111], a[40:43], v[48:51]\n"
"v_mfma_f32_16x16x32_bf16 v[48:51], v[92:95], a[36:39], v[48:51]\n"
"v_mfma_f32_16x16x32_bf16 v[48:51], v[76:79], a[32:35], v[48:51]\n"
"s_waitcnt vmcnt(7)\n"
"ds_read_b128 a[32:35], v240 offset:3072\n"
"v_mfma_f32_16x16x32_bf16 v[220:223], a[240:243], a[52:55], v[220:223]\n"
"ds_read_b128 a[36:39], v240 offset:7168\n"
"v_mfma_f32_16x16x32_bf16 v[220:223], a[244:247], a[52:55], v[220:223]\n"
"ds_read_b128 a[40:43], v240 offset:11264\n"
"v_mfma_f32_16x16x32_bf16 v[220:223], a[248:251], a[52:55], v[220:223]\n"
"ds_read_b128 a[44:47], v240 offset:15360\n"
"v_mfma_f32_16x16x32_bf16 v[220:223], a[252:255], a[52:55], v[220:223]\n"
"s_waitcnt lgkmcnt(8)\n"
"v_mfma_f32_16x16x32_bf16 v[20:23], v[72:75], a[0:3], v[20:23]\n"
"v_lshlrev_b32 v232, 16, v210\n"
"v_and_b32 v233, 0xffff0000, v210\n"
"v_mfma_f32_16x16x32_bf16 v[20:23], v[88:91], a[4:7], v[20:23]\n"
"v_lshlrev_b32 v234, 16, v211\n"
"v_and_b32 v235, 0xffff0000, v211\n"
"v_mfma_f32_16x16x32_bf16 v[20:23], v[104:107], a[8:11], v[20:23]\n"
"v_mfma_f32_16x16x32_bf16 v[20:23], v[120:123], a[12:15], v[20:23]\n"
"s_add_u32 s71, s0, 81920\n"
"s_waitcnt vmcnt(2)\n"
"v_fmac_f32 v232, v220, v196\n"
"v_mfma_f32_16x16x32_bf16 v[52:55], v[124:127], a[12:15], v[52:55]\n"
"buffer_store_dwordx2 v[250:251], v241, s[48:51], s71 offen offset:0 sc1\n"
"v_mfma_f32_16x16x32_bf16 v[52:55], v[108:111], a[8:11], v[52:55]\n"
"v_fmac_f32 v233, v221, v197\n"
"v_fmac_f32 v234, v222, v198\n"
"v_mfma_f32_16x16x32_bf16 v[52:55], v[92:95], a[4:7], v[52:55]\n"
"v_fmac_f32 v235, v223, v199\n"
"v_cvt_pk_bf16_f32 v230, v232, v233\n"
"v_mfma_f32_16x16x32_bf16 v[52:55], v[76:79], a[0:3], v[52:55]\n"
"s_waitcnt lgkmcnt(4)\n"
"ds_read_b128 a[0:3], v240 offset:18432\n"
"ds_read_b128 a[4:7], v240 offset:22528\n"
"v_mfma_f32_16x16x32_bf16 v[16:19], v[128:131], a[16:19], v[16:19]\n"
"ds_read_b128 a[8:11], v240 offset:26624\n"
"ds_read_b128 a[12:15], v240 offset:30720\n"
"v_mfma_f32_16x16x32_bf16 v[16:19], v[136:139], a[20:23], v[16:19]\n"
"v_cvt_pk_bf16_f32 v231, v234, v235\n"
"v_mfma_f32_16x16x32_bf16 v[16:19], v[144:147], a[24:27], v[16:19]\n"
"buffer_store_dwordx2 v[230:231], v236, s[60:63], 0 offen offset:256 sc1\n"
"v_mfma_f32_16x16x32_bf16 v[16:19], v[152:155], a[28:31], v[16:19]\n"
"v_mfma_f32_16x16x32_bf16 v[48:51], v[156:159], a[28:31], v[48:51]\n"
"v_mfma_f32_16x16x32_bf16 v[48:51], v[148:151], a[24:27], v[48:51]\n"
"v_mfma_f32_16x16x32_bf16 v[48:51], v[140:143], a[20:23], v[48:51]\n"
"v_mfma_f32_16x16x32_bf16 v[48:51], v[132:135], a[16:19], v[48:51]\n"
"s_waitcnt lgkmcnt(4)\n"
"ds_read_b128 a[16:19], v240 offset:19456\n"
"ds_read_b128 a[20:23], v240 offset:23552\n"
"v_mfma_f32_16x16x32_bf16 v[20:23], v[128:131], a[32:35], v[20:23]\n"
"ds_read_b128 a[24:27], v240 offset:27648\n"
"ds_read_b128 a[28:31], v240 offset:31744\n"
"v_mfma_f32_16x16x32_bf16 v[20:23], v[136:139], a[36:39], v[20:23]\n"
"v_mfma_f32_16x16x32_bf16 v[20:23], v[144:147], a[40:43], v[20:23]\n"
"v_mfma_f32_16x16x32_bf16 v[20:23], v[152:155], a[44:47], v[20:23]\n"
"v_mfma_f32_16x16x32_bf16 v[52:55], v[156:159], a[44:47], v[52:55]\n"
"v_mfma_f32_16x16x32_bf16 v[52:55], v[148:151], a[40:43], v[52:55]\n"
"v_mfma_f32_16x16x32_bf16 v[52:55], v[140:143], a[36:39], v[52:55]\n"
"v_mfma_f32_16x16x32_bf16 v[52:55], v[132:135], a[32:35], v[52:55]\n"
"s_waitcnt lgkmcnt(4)\n"
"v_mfma_f32_16x16x32_bf16 v[16:19], v[160:163], a[0:3], v[16:19]\n"
"v_mfma_f32_16x16x32_bf16 v[16:19], v[168:171], a[4:7], v[16:19]\n"
"v_mfma_f32_16x16x32_bf16 v[16:19], v[176:179], a[8:11], v[16:19]\n"
"v_mfma_f32_16x16x32_bf16 v[16:19], v[184:187], a[12:15], v[16:19]\n"
"v_mfma_f32_16x16x32_bf16 v[48:51], v[188:191], a[12:15], v[48:51]\n"
"v_mfma_f32_16x16x32_bf16 v[48:51], v[180:183], a[8:11], v[48:51]\n"
"v_mfma_f32_16x16x32_bf16 v[48:51], v[172:175], a[4:7], v[48:51]\n"
"v_mfma_f32_16x16x32_bf16 v[48:51], v[164:167], a[0:3], v[48:51]\n"
"s_waitcnt lgkmcnt(0)\n"
"v_mfma_f32_16x16x32_bf16 v[20:23], v[160:163], a[16:19], v[20:23]\n"
"v_mfma_f32_16x16x32_bf16 v[20:23], v[168:171], a[20:23], v[20:23]\n"
"s_add_u32 s13, s1, 65536\n"
"buffer_load_dwordx2 v[244:245], v249, s[48:51], s13 offen offset:0 sc1\n"
"v_mfma_f32_16x16x32_bf16 v[20:23], v[176:179], a[24:27], v[20:23]\n"
"v_mfma_f32_16x16x32_bf16 v[20:23], v[184:187], a[28:31], v[20:23]\n"
"v_mfma_f32_16x16x32_bf16 v[52:55], v[188:191], a[28:31], v[52:55]\n"
"v_mfma_f32_16x16x32_bf16 v[52:55], v[180:183], a[24:27], v[52:55]\n"
"v_mfma_f32_16x16x32_bf16 v[52:55], v[172:175], a[20:23], v[52:55]\n"
"v_mfma_f32_16x16x32_bf16 v[52:55], v[164:167], a[16:19], v[52:55]\n"
"s_add_u32 s68, s93, 409600\n"
"v_mul_f32 v16, v242, v16\n"
"v_mul_f32 v17, v243, v17\n"
"v_mul_f32 v18, v246, v18\n"
"v_mul_f32 v19, v247, v19\n"
"v_mul_f32 v20, v242, v20\n"
"v_mul_f32 v21, v243, v21\n"
"v_mul_f32 v22, v246, v22\n"
"v_mul_f32 v23, v247, v23\n"
"v_cvt_pk_bf16_f32 v16, v16, v17\n"
"v_cvt_pk_bf16_f32 v17, v18, v19\n"
"v_cvt_pk_bf16_f32 v18, v20, v21\n"
"v_cvt_pk_bf16_f32 v19, v22, v23\n"
"buffer_store_dwordx4 v[16:19], v224, s[52:55], s68 offen offset:0 sc1\n"
"s_add_u32 s68, s93, 442368\n"
"v_mul_f32 v48, v225, v48\n"
"v_mul_f32 v49, v238, v49\n"
"v_mul_f32 v50, v252, v50\n"
"v_mul_f32 v51, v253, v51\n"
"v_mul_f32 v52, v225, v52\n"
"v_mul_f32 v53, v238, v53\n"
"v_mul_f32 v54, v252, v54\n"
"v_mul_f32 v55, v253, v55\n"
"v_cvt_pk_bf16_f32 v48, v48, v49\n"
"v_cvt_pk_bf16_f32 v49, v50, v51\n"
"v_cvt_pk_bf16_f32 v50, v52, v53\n"
"v_cvt_pk_bf16_f32 v51, v54, v55\n"
"buffer_store_dwordx4 v[48:51], v224, s[52:55], s68 offen offset:0 sc1\n"
"s_waitcnt vmcnt(2)\n"
"v_cmp_eq_u32_e64 s[14:15], s86, v244\n"
"v_cmp_eq_u32_e64 s[16:17], s87, v245\n"
"s_nop 0\n"
"s_and_b64 s[14:15], s[14:15], s[16:17]\n"
"s_cmp_eq_u64 s[14:15], -1\n"
"s_cbranch_scc0 SPD_%=\n"
"SBD_%=:\n"
"s_add_u32 s13, s94, 262144\n"
"buffer_load_dwordx4 a[192:195], v248, s[52:55], s13 offen offset:0 sc1\n"
"s_add_u32 s13, s94, 266240\n"
"buffer_load_dwordx4 a[196:199], v248, s[52:55], s13 offen offset:0 sc1\n"
"s_add_u32 s13, s94, 270336\n"
"buffer_load_dwordx4 a[200:203], v248, s[52:55], s13 offen offset:0 sc1\n"
"s_add_u32 s13, s94, 274432\n"
"buffer_load_dwordx4 a[204:207], v248, s[52:55], s13 offen offset:0 sc1\n"
"s_add_u32 s13, s1, 81920\n"
"buffer_load_dwordx2 v[244:245], v249, s[48:51], s13 offen offset:0 sc1\n"
"s_add_u32 s71, s0, 98304\n"
"s_waitcnt vmcnt(5)\n"
"buffer_store_dwordx2 v[250:251], v241, s[48:51], s71 offen offset:0 sc1\n"
"s_waitcnt vmcnt(1)\n"
"v_cmp_eq_u32_e64 s[14:15], s86, v244\n"
"v_cmp_eq_u32_e64 s[16:17], s87, v245\n"
"s_nop 0\n"
"s_and_b64 s[14:15], s[14:15], s[16:17]\n"
"s_cmp_eq_u64 s[14:15], -1\n"
"s_cbranch_scc0 SPE5_%=\n"
"SBE5_%=:\n"
"s_add_u32 s13, s94, 327680\n"
"buffer_load_dwordx4 a[208:211], v248, s[52:55], s13 offen offset:0 sc1\n"
"s_add_u32 s13, s94, 331776\n"
"buffer_load_dwordx4 a[212:215], v248, s[52:55], s13 offen offset:0 sc1\n"
"s_add_u32 s13, s94, 335872\n"
"buffer_load_dwordx4 a[216:219], v248, s[52:55], s13 offen offset:0 sc1\n"
"s_add_u32 s13, s94, 339968\n"
"buffer_load_dwordx4 a[220:223], v248, s[52:55], s13 offen offset:0 sc1\n"
"v_mfma_f32_16x16x32_bf16 v[216:219], a[192:195], a[48:51], 0\n"
"v_mfma_f32_16x16x32_bf16 v[216:219], a[196:199], a[48:51], v[216:219]\n"
"v_mfma_f32_16x16x32_bf16 v[216:219], a[200:203], a[48:51], v[216:219]\n"
"v_mfma_f32_16x16x32_bf16 v[216:219], a[204:207], a[48:51], v[216:219]\n"
"s_sleep 8\n"
"s_add_u32 s13, s1, 98304\n"
"buffer_load_dwordx2 v[244:245], v249, s[48:51], s13 offen offset:0 sc1\n"
"s_waitcnt vmcnt(0)\n"
"v_cmp_eq_u32_e64 s[14:15], s86, v244\n"
"v_cmp_eq_u32_e64 s[16:17], s87, v245\n"
"s_nop 0\n"
"s_and_b64 s[14:15], s[14:15], s[16:17]\n"
"s_cmp_eq_u64 s[14:15], -1\n"
"s_cbranch_scc0 SPE_%=\n"
"SBE_%=:\n"
"s_add_u32 s13, s94, 393216\n"
"buffer_load_dwordx4 a[224:227], v248, s[52:55], s13 offen offset:0 sc1\n"
"s_add_u32 s13, s94, 397312\n"
"buffer_load_dwordx4 a[228:231], v248, s[52:55], s13 offen offset:0 sc1\n"
"s_add_u32 s13, s94, 401408\n"
"buffer_load_dwordx4 a[232:235], v248, s[52:55], s13 offen offset:0 sc1\n"
"s_add_u32 s13, s94, 405504\n"
"buffer_load_dwordx4 a[236:239], v248, s[52:55], s13 offen offset:0 sc1\n"
"v_mfma_f32_16x16x32_bf16 v[216:219], a[208:211], a[52:55], v[216:219]\n"
"v_mfma_f32_16x16x32_bf16 v[216:219], a[212:215], a[52:55], v[216:219]\n"
"v_mfma_f32_16x16x32_bf16 v[216:219], a[216:219], a[52:55], v[216:219]\n"
"v_mfma_f32_16x16x32_bf16 v[216:219], a[220:223], a[52:55], v[216:219]\n"
"v_lshlrev_b32 v232, 16, v212\n"
"v_and_b32 v233, 0xffff0000, v212\n"
"v_lshlrev_b32 v234, 16, v213\n"
"v_and_b32 v235, 0xffff0000, v213\n"
"s_nop 3\n"
"v_fmac_f32 v232, v216, v200\n"
"v_fmac_f32 v233, v217, v201\n"
"v_fmac_f32 v234, v218, v202\n"
"v_fmac_f32 v235, v219, v203\n"
"v_cvt_pk_bf16_f32 v230, v232, v233\n"
"v_cvt_pk_bf16_f32 v231, v234, v235\n"
"buffer_store_dwordx2 v[230:231], v236, s[60:63], 0 offen offset:512 sc1\n"
"s_waitcnt vmcnt(4)\n"
"v_mfma_f32_16x16x32_bf16 v[220:223], a[224:227], a[48:51], 0\n"
"s_waitcnt vmcnt(3)\n"
"v_mfma_f32_16x16x32_bf16 v[220:223], a[228:231], a[48:51], v[220:223]\n"
"s_waitcnt vmcnt(2)\n"
"v_mfma_f32_16x16x32_bf16 v[220:223], a[232:235], a[48:51], v[220:223]\n"
"s_waitcnt vmcnt(1)\n"
"v_mfma_f32_16x16x32_bf16 v[220:223], a[236:239], a[48:51], v[220:223]\n"
"v_lshlrev_b32 v232, 16, v214\n"
"v_and_b32 v233, 0xffff0000, v214\n"
"v_lshlrev_b32 v234, 16, v215\n"
"v_and_b32 v235, 0xffff0000, v215\n"
"s_nop 3\n"
"v_fmac_f32 v232, v220, v204\n"
"v_fmac_f32 v233, v221, v205\n"
"v_fmac_f32 v234, v222, v206\n"
"v_fmac_f32 v235, v223, v207\n"
"v_cvt_pk_bf16_f32 v230, v232, v233\n"
"v_cvt_pk_bf16_f32 v231, v234, v235\n"
"s_mov_b32 exec_hi, 0x00ff00ff\n"
"s_mov_b32 exec_lo, 0x00ff00ff\n"
"buffer_store_dwordx2 v[230:231], v236, s[60:63], 0 offen offset:768 sc1\n"
"s_mov_b64 exec, -1\n"
"s_branch K1END_%=\n"
"SPX_%=:\n"
"s_nop 7\n"
"s_mov_b32 s99, 0\n"
"SLX_%=:\n"
"s_sleep 1\n"
"buffer_load_dwordx2 v[244:245], v249, s[48:51], s70 offen offset:0 sc1\n"
"s_waitcnt vmcnt(0)\n"
"v_cmp_eq_u32_e64 s[14:15], s86, v244\n"
"v_cmp_eq_u32_e64 s[16:17], s87, v245\n"
"s_nop 0\n"
"s_and_b64 s[14:15], s[14:15], s[16:17]\n"
"s_cmp_eq_u64 s[14:15], -1\n"
"s_cbranch_scc1 SBX_%=\n"
"s_add_u32 s99, s99, 1\n"
"s_cmp_lt_u32 s99, 0x40000\n"
"s_cbranch_scc1 SLX_%=\n"
"s_branch SBX_%=\n"
"SPV_%=:\n"
"s_nop 7\n"
"s_mov_b32 s99, 0\n"
"SLV_%=:\n"
"s_sleep 1\n"
"buffer_load_dwordx2 v[154:155], v156, s[48:51], s97 offen offset:0 sc1\n"
"s_waitcnt vmcnt(0)\n"
"v_cmp_eq_u32_e64 s[14:15], s86, v154\n"
"v_cmp_eq_u32_e64 s[16:17], s87, v155\n"
"s_nop 0\n"
"s_and_b64 s[14:15], s[14:15], s[16:17]\n"
"s_cmp_eq_u64 s[14:15], -1\n"
"s_cbranch_scc1 SBV_%=\n"
"s_add_u32 s99, s99, 1\n"
"s_cmp_lt_u32 s99, 0x40000\n"
"s_cbranch_scc1 SLV_%=\n"
"s_branch SBV_%=\n"
"SPA_%=:\n"
"s_nop 7\n"
"s_mov_b32 s99, 0\n"
"SLA_%=:\n"
"s_sleep 1\n"
"s_movk_i32 s68, 16384\n"
"buffer_load_dwordx4 v[160:163], v224, s[48:51], s68 offen offset:0 sc1\n"
"buffer_load_dwordx4 v[164:167], v224, s[48:51], s68 offen offset:1024 sc1\n"
"buffer_load_dwordx4 v[168:171], v224, s[48:51], s68 offen offset:2048 sc1\n"
"buffer_load_dwordx4 v[172:175], v224, s[48:51], s68 offen offset:3072 sc1\n"
"s_waitcnt vmcnt(0)\n"
"v_cmp_eq_u32_e64 s[84:85], s86, v161\n"
"v_cmp_eq_u32_e64 s[82:83], s87, v163\n"
"s_nop 0\n"
"s_and_b64 s[84:85], s[84:85], s[82:83]\n"
"v_cmp_eq_u32_e64 s[82:83], s86, v165\n"
"s_nop 0\n"
"s_and_b64 s[84:85], s[84:85], s[82:83]\n"
"v_cmp_eq_u32_e64 s[82:83], s87, v167\n"
"s_nop 0\n"
"s_and_b64 s[84:85], s[84:85], s[82:83]\n"
"v_cmp_eq_u32_e64 s[82:83], s86, v169\n"
"s_nop 0\n"
"s_and_b64 s[84:85], s[84:85], s[82:83]\n"
"v_cmp_eq_u32_e64 s[82:83], s87, v171\n"
"s_nop 0\n"
"s_and_b64 s[84:85], s[84:85], s[82:83]\n"
"v_cmp_eq_u32_e64 s[82:83], s86, v173\n"
"s_nop 0\n"
"s_and_b64 s[84:85], s[84:85], s[82:83]\n"
"v_cmp_eq_u32_e64 s[82:83], s87, v175\n"
"s_nop 0\n"
"s_and_b64 s[84:85], s[84:85], s[82:83]\n"
"s_cmp_eq_u64 s[84:85], -1\n"
"s_cbranch_scc1 SBA_%=\n"
"s_add_u32 s99, s99, 1\n"
"s_cmp_lt_u32 s99, 0x40000\n"
"s_cbranch_scc1 SLA_%=\n"
"s_branch SBA_%=\n"
"SPH_%=:\n"
"s_nop 7\n"
"s_mov_b32 s99, 0\n"
"SLH_%=:\n"
"s_sleep 1\n"
"s_lshl_b32 s68, s5, 11\n"
"s_add_u32 s68, s68, 524288\n"
"buffer_load_dwordx4 v[176:179], v224, s[48:51], s68 offen offset:0 sc1\n"
"buffer_load_dwordx4 v[180:183], v224, s[48:51], s68 offen offset:1024 sc1\n"
"s_waitcnt vmcnt(0)\n"
"v_cmp_eq_u32_e64 s[84:85], s86, v177\n"
"v_cmp_eq_u32_e64 s[82:83], s87, v179\n"
"s_nop 0\n"
"s_and_b64 s[84:85], s[84:85], s[82:83]\n"
"v_cmp_eq_u32_e64 s[82:83], s86, v181\n"
"s_nop 0\n"
"s_and_b64 s[84:85], s[84:85], s[82:83]\n"
"v_cmp_eq_u32_e64 s[82:83], s87, v183\n"
"s_nop 0\n"
"s_and_b64 s[84:85], s[84:85], s[82:83]\n"
"s_cmp_eq_u64 s[84:85], -1\n"
"s_cbranch_scc1 SBH_%=\n"
"s_add_u32 s99, s99, 1\n"
"s_cmp_lt_u32 s99, 0x40000\n"
"s_cbranch_scc1 SLH_%=\n"
"s_branch SBH_%=\n"
"SPB_%=:\n"
"s_nop 7\n"
"s_mov_b32 s99, 0\n"
"SLB_%=:\n"
"s_sleep 1\n"
"s_add_u32 s98, s97, 0\n"
"buffer_load_dwordx4 a[88:91], v255, s[48:51], s98 offen offset:0 sc1\n"
"s_add_u32 s98, s97, 262144\n"
"buffer_load_dwordx4 a[92:95], v255, s[48:51], s98 offen offset:0 sc1\n"
"s_add_u32 s98, s97, 524288\n"
"buffer_load_dwordx4 a[96:99], v255, s[48:51], s98 offen offset:0 sc1\n"
"s_add_u32 s98, s97, 786432\n"
"buffer_load_dwordx4 a[100:103], v255, s[48:51], s98 offen offset:0 sc1\n"
"s_waitcnt vmcnt(0)\n"
"v_accvgpr_read_b32 v192, a89\n"
"v_accvgpr_read_b32 v193, a91\n"
"v_accvgpr_read_b32 v194, a93\n"
"v_accvgpr_read_b32 v195, a95\n"
"v_accvgpr_read_b32 v196, a97\n"
"v_accvgpr_read_b32 v197, a99\n"
"v_accvgpr_read_b32 v198, a101\n"
"v_accvgpr_read_b32 v199, a103\n"
"v_cmp_eq_u32_e64 s[84:85], s86, v192\n"
"v_cmp_eq_u32_e64 s[82:83], s87, v193\n"
"s_nop 0\n"
"s_and_b64 s[84:85], s[84:85], s[82:83]\n"
"v_cmp_eq_u32_e64 s[82:83], s86, v194\n"
"s_nop 0\n"
"s_and_b64 s[84:85], s[84:85], s[82:83]\n"
"v_cmp_eq_u32_e64 s[82:83], s87, v195\n"
"s_nop 0\n"
"s_and_b64 s[84:85], s[84:85], s[82:83]\n"
"v_cmp_eq_u32_e64 s[82:83], s86, v196\n"
"s_nop 0\n"
"s_and_b64 s[84:85], s[84:85], s[82:83]\n"
"v_cmp_eq_u32_e64 s[82:83], s87, v197\n"
"s_nop 0\n"
"s_and_b64 s[84:85], s[84:85], s[82:83]\n"
"v_cmp_eq_u32_e64 s[82:83], s86, v198\n"
"s_nop 0\n"
"s_and_b64 s[84:85], s[84:85], s[82:83]\n"
"v_cmp_eq_u32_e64 s[82:83], s87, v199\n"
"s_nop 0\n"
"s_and_b64 s[84:85], s[84:85], s[82:83]\n"
"s_or_b64 s[84:85], s[84:85], s[6:7]\n"
"s_cmp_eq_u64 s[84:85], -1\n"
"s_cbranch_scc1 SBB_%=\n"
"s_add_u32 s99, s99, 1\n"
"s_cmp_lt_u32 s99, 0x40000\n"
"s_cbranch_scc1 SLB_%=\n"
"s_branch SBB_%=\n"
"SPZ_%=:\n"
"s_nop 7\n"
"s_mov_b32 s99, 0\n"
"SLZ_%=:\n"
"s_sleep 1\n"
"buffer_load_dwordx4 a[160:163], v237, s[48:51], s80 offen offset:0 sc1\n"
"buffer_load_dwordx4 a[164:167], v237, s[48:51], s80 offen offset:512 sc1\n"
"buffer_load_dwordx4 a[168:171], v237, s[48:51], s80 offen offset:1024 sc1\n"
"buffer_load_dwordx4 a[172:175], v237, s[48:51], s80 offen offset:1536 sc1\n"
"s_waitcnt vmcnt(0)\n"
"v_accvgpr_read_b32 v192, a160\n"
"v_accvgpr_read_b32 v193, a161\n"
"v_accvgpr_read_b32 v194, a162\n"
"v_accvgpr_read_b32 v195, a163\n"
"v_accvgpr_read_b32 v196, a164\n"
"v_accvgpr_read_b32 v197, a165\n"
"v_accvgpr_read_b32 v198, a166\n"
"v_accvgpr_read_b32 v199, a167\n"
"v_accvgpr_read_b32 v200, a168\n"
"v_accvgpr_read_b32 v201, a169\n"
"v_accvgpr_read_b32 v202, a170\n"
"v_accvgpr_read_b32 v203, a171\n"
"v_accvgpr_read_b32 v204, a172\n"
"v_accvgpr_read_b32 v205, a173\n"
"v_accvgpr_read_b32 v206, a174\n"
"v_accvgpr_read_b32 v207, a175\n"
"v_cmp_eq_u32_e64 s[84:85], s86, v193\n"
"v_cmp_eq_u32_e64 s[82:83], s86, v195\n"
"s_nop 0\n"
"s_and_b64 s[84:85], s[84:85], s[82:83]\n"
"v_cmp_eq_u32_e64 s[82:83], s86, v197\n"
"s_nop 0\n"
"s_and_b64 s[84:85], s[84:85], s[82:83]\n"
"v_cmp_eq_u32_e64 s[82:83], s86, v199\n"
"s_nop 0\n"
"s_and_b64 s[84:85], s[84:85], s[82:83]\n"
"v_cmp_eq_u32_e64 s[82:83], s86, v201\n"
"s_nop 0\n"
"s_and_b64 s[84:85], s[84:85], s[82:83]\n"
"v_cmp_eq_u32_e64 s[82:83], s86, v203\n"
"s_nop 0\n"
"s_and_b64 s[84:85], s[84:85], s[82:83]\n"
"v_cmp_eq_u32_e64 s[82:83], s86, v205\n"
"s_nop 0\n"
"s_or_b32 s82, s82, 0xff00ff00\n"
"s_or_b32 s83, s83, 0xff00ff00\n"
"s_and_b64 s[84:85], s[84:85], s[82:83]\n"
"v_cmp_eq_u32_e64 s[82:83], s86, v207\n"
"s_nop 0\n"
"s_or_b32 s82, s82, 0xff00ff00\n"
"s_or_b32 s83, s83, 0xff00ff00\n"
"s_and_b64 s[84:85], s[84:85], s[82:83]\n"
"s_orn2_b64 s[84:85], s[84:85], s[72:73]\n"
"s_cmp_eq_u64 s[84:85], -1\n"
"s_cbranch_scc1 SBZ_%=\n"
"s_add_u32 s99, s99, 1\n"
"s_cmp_lt_u32 s99, 0x40000\n"
"s_cbranch_scc1 SLZ_%=\n"
"s_branch SBZ_%=\n"
"SPC0_%=:\n"
"s_nop 7\n"
"s_mov_b32 s99, 0\n"
"SLC0_%=:\n"
"s_sleep 1\n"
"s_add_u32 s13, s1, 0\n"
"buffer_load_dwordx2 v[244:245], v249, s[48:51], s13 offen offset:0 sc1\n"
"s_waitcnt vmcnt(0)\n"
"v_cmp_eq_u32_e64 s[14:15], s86, v244\n"
"v_cmp_eq_u32_e64 s[16:17], s87, v245\n"
"s_nop 0\n"
"s_and_b64 s[14:15], s[14:15], s[16:17]\n"
"s_cmp_eq_u64 s[14:15], -1\n"
"s_cbranch_scc1 SBC0_%=\n"
"s_add_u32 s99, s99, 1\n"
"s_cmp_lt_u32 s99, 0x40000\n"
"s_cbranch_scc1 SLC0_%=\n"
"s_branch SBC0_%=\n"
"SPC1_%=:\n"
"s_nop 7\n"
"s_mov_b32 s99, 0\n"
"SLC1_%=:\n"
"s_sleep 1\n"
"s_add_u32 s13, s1, 16384\n"
"buffer_load_dwordx2 v[244:245], v249, s[48:51], s13 offen offset:0 sc1\n"
"s_waitcnt vmcnt(0)\n"
"v_cmp_eq_u32_e64 s[14:15], s86, v244\n"
"v_cmp_eq_u32_e64 s[16:17], s87, v245\n"
"s_nop 0\n"
"s_and_b64 s[14:15], s[14:15], s[16:17]\n"
"s_cmp_eq_u64 s[14:15], -1\n"
"s_cbranch_scc1 SBC1_%=\n"
"s_add_u32 s99, s99, 1\n"
"s_cmp_lt_u32 s99, 0x40000\n"
"s_cbranch_scc1 SLC1_%=\n"
"s_branch SBC1_%=\n"
"SPC2_%=:\n"
"s_nop 7\n"
"s_mov_b32 s99, 0\n"
"SLC2_%=:\n"
"s_sleep 1\n"
"s_add_u32 s13, s1, 32768\n"
"buffer_load_dwordx2 v[244:245], v249, s[48:51], s13 offen offset:0 sc1\n"
"s_waitcnt vmcnt(0)\n"
"v_cmp_eq_u32_e64 s[14:15], s86, v244\n"
"v_cmp_eq_u32_e64 s[16:17], s87, v245\n"
"s_nop 0\n"
"s_and_b64 s[14:15], s[14:15], s[16:17]\n"
"s_cmp_eq_u64 s[14:15], -1\n"
"s_cbranch_scc1 SBC2_%=\n"
"s_add_u32 s99, s99, 1\n"
"s_cmp_lt_u32 s99, 0x40000\n"
"s_cbranch_scc1 SLC2_%=\n"
"s_branch SBC2_%=\n"
"SPC3_%=:\n"
"s_nop 7\n"
"s_mov_b32 s99, 0\n"
"SLC3_%=:\n"
"s_sleep 1\n"
"s_add_u32 s13, s1, 49152\n"
"buffer_load_dwordx2 v[244:245], v249, s[48:51], s13 offen offset:0 sc1\n"
"s_waitcnt vmcnt(0)\n"
"v_cmp_eq_u32_e64 s[14:15], s86, v244\n"
"v_cmp_eq_u32_e64 s[16:17], s87, v245\n"
"s_nop 0\n"
"s_and_b64 s[14:15], s[14:15], s[16:17]\n"
"s_cmp_eq_u64 s[14:15], -1\n"
"s_cbranch_scc1 SBC3_%=\n"
"s_add_u32 s99, s99, 1\n"
"s_cmp_lt_u32 s99, 0x40000\n"
"s_cbranch_scc1 SLC3_%=\n"
"s_branch SBC3_%=\n"
"SPD_%=:\n"
"s_nop 7\n"
"s_mov_b32 s99, 0\n"
"SLD_%=:\n"
"s_sleep 1\n"
"s_add_u32 s13, s1, 65536\n"
"buffer_load_dwordx2 v[244:245], v249, s[48:51], s13 offen offset:0 sc1\n"
"s_waitcnt vmcnt(0)\n"
"v_cmp_eq_u32_e64 s[14:15], s86, v244\n"
"v_cmp_eq_u32_e64 s[16:17], s87, v245\n"
"s_nop 0\n"
"s_and_b64 s[14:15], s[14:15], s[16:17]\n"
"s_cmp_eq_u64 s[14:15], -1\n"
"s_cbranch_scc1 SBD_%=\n"
"s_add_u32 s99, s99, 1\n"
"s_cmp_lt_u32 s99, 0x40000\n"
"s_cbranch_scc1 SLD_%=\n"
"s_branch SBD_%=\n"
"SPE5_%=:\n"
"s_nop 7\n"
"s_mov_b32 s99, 0\n"
"SLE5_%=:\n"
"s_sleep 1\n"
"s_add_u32 s13, s1, 81920\n"
"buffer_load_dwordx2 v[244:245], v249, s[48:51], s13 offen offset:0 sc1\n"
"s_waitcnt vmcnt(0)\n"
"v_cmp_eq_u32_e64 s[14:15], s86, v244\n"
"v_cmp_eq_u32_e64 s[16:17], s87, v245\n"
"s_nop 0\n"
"s_and_b64 s[14:15], s[14:15], s[16:17]\n"
"s_cmp_eq_u64 s[14:15], -1\n"
"s_cbranch_scc1 SBE5_%=\n"
"s_add_u32 s99, s99, 1\n"
"s_cmp_lt_u32 s99, 0x40000\n"
"s_cbranch_scc1 SLE5_%=\n"
"s_branch SBE5_%=\n"
"SPE_%=:\n"
"s_nop 7\n"
"s_mov_b32 s99, 0\n"
"SLE_%=:\n"
"s_sleep 1\n"
"s_add_u32 s13, s1, 98304\n"
"buffer_load_dwordx2 v[244:245], v249, s[48:51], s13 offen offset:0 sc1\n"
"s_waitcnt vmcnt(0)\n"
"v_cmp_eq_u32_e64 s[14:15], s86, v244\n"
"v_cmp_eq_u32_e64 s[16:17], s87, v245\n"
"s_nop 0\n"
"s_and_b64 s[14:15], s[14:15], s[16:17]\n"
"s_cmp_eq_u64 s[14:15], -1\n"
"s_cbranch_scc1 SBE_%=\n"
"s_add_u32 s99, s99, 1\n"
"s_cmp_lt_u32 s99, 0x40000\n"
"s_cbranch_scc1 SLE_%=\n"
"s_branch SBE_%=\n"
"K1SLOW_%=:\n"
"s_nop 15\n"
"s_mov_b32 s70, m0\n"
"s_mov_b32 s71, s67\n"
"s_waitcnt vmcnt(0) lgkmcnt(0)\n"
"s_barrier\n"
"v_mov_b32 v244, 0xff800000\n"
"v_mov_b32 v245, 0xff800000\n"
"v_mov_b32 v246, 0xff800000\n"
"v_mov_b32 v247, 0xff800000\n"
"s_mov_b32 s69, 0\n"
"K1L1_%=:\n"
"s_call_b64 s[18:19], K1QK_%=\n"
"v_max3_f32 v244, v244, v192, v193\n"
"v_max3_f32 v245, v245, v194, v195\n"
"v_max3_f32 v246, v246, v196, v197\n"
"v_max3_f32 v247, v247, v198, v199\n"
"v_max3_f32 v244, v244, v200, v201\n"
"v_max3_f32 v245, v245, v202, v203\n"
"v_max3_f32 v246, v246, v204, v205\n"
"v_max3_f32 v247, v247, v206, v207\n"
"s_cmp_lt_u32 s69, 262144\n"
"s_cbranch_scc1 K1L1_%=\n"
"v_max_f32 v244, v244, v245\n"
"v_max_f32 v246, v246, v247\n"
"v_mov_b32 v245, v244\n"
"v_mov_b32 v247, v246\n"
"s_nop 0\n"
"v_permlane16_swap_b32 v244, v245\n"
"v_permlane16_swap_b32 v246, v247\n"
"v_max_f32 v244, v244, v245\n"
"v_max_f32 v246, v246, v247\n"
"v_mov_b32 v245, v244\n"
"v_mov_b32 v247, v246\n"
"s_nop 0\n"
"v_permlane32_swap_b32 v244, v245\n"
"v_permlane32_swap_b32 v246, v247\n"
"v_max_f32 v244, v244, v245\n"
"v_max_f32 v246, v246, v247\n"
"s_mov_b32 s69, 0\n"
"s_call_b64 s[18:19], K1QK_%=\n"
"s_call_b64 s[18:19], K1EX_%=\n"
"v_mov_b32 v8, v208\n"
"v_mov_b32 v9, v209\n"
"v_mov_b32 v10, v210\n"
"v_mov_b32 v11, v211\n"
"v_mov_b32 v12, v212\n"
"v_mov_b32 v13, v213\n"
"v_mov_b32 v14, v214\n"
"v_mov_b32 v15, v215\n"
"v_mfma_f32_16x16x32_bf16 a[112:115], v[8:11], a[120:123], 0\n"
"s_nop 0\n"
"v_mfma_f32_16x16x32_bf16 a[116:119], v[12:15], a[120:123], 0\n"
"s_call_b64 s[18:19], K1QK_%=\n"
"s_call_b64 s[18:19], K1EX_%=\n"
"v_mov_b32 v24, v208\n"
"v_mov_b32 v25, v209\n"
"v_mov_b32 v26, v210\n"
"v_mov_b32 v27, v211\n"
"v_mov_b32 v28, v212\n"
"v_mov_b32 v29, v213\n"
"v_mov_b32 v30, v214\n"
"v_mov_b32 v31, v215\n"
"v_mfma_f32_16x16x32_bf16 a[112:115], v[24:27], a[120:123], a[112:115]\n"
"s_nop 0\n"
"v_mfma_f32_16x16x32_bf16 a[116:119], v[28:31], a[120:123], a[116:119]\n"
"s_call_b64 s[18:19], K1QK_%=\n"
"s_call_b64 s[18:19], K1EX_%=\n"
"v_mov_b32 v40, v208\n"
"v_mov_b32 v41, v209\n"
"v_mov_b32 v42, v210\n"
"v_mov_b32 v43, v211\n"
"v_mov_b32 v44, v212\n"
"v_mov_b32 v45, v213\n"
"v_mov_b32 v46, v214\n"
"v_mov_b32 v47, v215\n"
"v_mfma_f32_16x16x32_bf16 a[112:115], v[40:43], a[120:123], a[112:115]\n"
"s_nop 0\n"
"v_mfma_f32_16x16x32_bf16 a[116:119], v[44:47], a[120:123], a[116:119]\n"
"s_call_b64 s[18:19], K1QK_%=\n"
"s_call_b64 s[18:19], K1EX_%=\n"
"v_mov_b32 v56, v208\n"
"v_mov_b32 v57, v209\n"
"v_mov_b32 v58, v210\n"
"v_mov_b32 v59, v211\n"
"v_mov_b32 v60, v212\n"
"v_mov_b32 v61, v213\n"
"v_mov_b32 v62, v214\n"
"v_mov_b32 v63, v215\n"
"v_mfma_f32_16x16x32_bf16 a[112:115], v[56:59], a[120:123], a[112:115]\n"
"s_nop 0\n"
"v_mfma_f32_16x16x32_bf16 a[116:119], v[60:63], a[120:123], a[116:119]\n"
"s_call_b64 s[18:19], K1QK_%=\n"
"s_call_b64 s[18:19], K1EX_%=\n"
"v_mov_b32 v72, v208\n"
"v_mov_b32 v73, v209\n"
"v_mov_b32 v74, v210\n"
"v_mov_b32 v75, v211\n"
"v_mov_b32 v76, v212\n"
"v_mov_b32 v77, v213\n"
"v_mov_b32 v78, v214\n"
"v_mov_b32 v79, v215\n"
"v_mfma_f32_16x16x32_bf16 a[112:115], v[72:75], a[120:123], a[112:115]\n"
"s_nop 0\n"
"v_mfma_f32_16x16x32_bf16 a[116:119], v[76:79], a[120:123], a[116:119]\n"
"s_call_b64 s[18:19], K1QK_%=\n"
"s_call_b64 s[18:19], K1EX_%=\n"
"v_mov_b32 v88, v208\n"
"v_mov_b32 v89, v209\n"
"v_mov_b32 v90, v210\n"
"v_mov_b32 v91, v211\n"
"v_mov_b32 v92, v212\n"
"v_mov_b32 v93, v213\n"
"v_mov_b32 v94, v214\n"
"v_mov_b32 v95, v215\n"
"v_mfma_f32_16x16x32_bf16 a[112:115], v[88:91], a[120:123], a[112:115]\n"
"s_nop 0\n"
"v_mfma_f32_16x16x32_bf16 a[116:119], v[92:95], a[120:123], a[116:119]\n"
"s_call_b64 s[18:19], K1QK_%=\n"
"s_call_b64 s[18:19], K1EX_%=\n"
"v_mov_b32 v104, v208\n"
"v_mov_b32 v105, v209\n"
"v_mov_b32 v106, v210\n"
"v_mov_b32 v107, v211\n"
"v_mov_b32 v108, v212\n"
"v_mov_b32 v109, v213\n"
"v_mov_b32 v110, v214\n"
"v_mov_b32 v111, v215\n"
"v_mfma_f32_16x16x32_bf16 a[112:115], v[104:107], a[120:123], a[112:115]\n"
"s_nop 0\n"
"v_mfma_f32_16x16x32_bf16 a[116:119], v[108:111], a[120:123], a[116:119]\n"
"s_call_b64 s[18:19], K1QK_%=\n"
"s_call_b64 s[18:19], K1EX_%=\n"
"v_mov_b32 v120, v208\n"
"v_mov_b32 v121, v209\n"
"v_mov_b32 v122, v210\n"
"v_mov_b32 v123, v211\n"
"v_mov_b32 v124, v212\n"
"v_mov_b32 v125, v213\n"
"v_mov_b32 v126, v214\n"
"v_mov_b32 v127, v215\n"
"v_mfma_f32_16x16x32_bf16 a[112:115], v[120:123], a[120:123], a[112:115]\n"
"s_nop 0\n"
"v_mfma_f32_16x16x32_bf16 a[116:119], v[124:127], a[120:123], a[116:119]\n"
"s_call_b64 s[18:19], K1QK_%=\n"
"s_call_b64 s[18:19], K1EX_%=\n"
"v_mov_b32 v128, v208\n"
"v_mov_b32 v129, v209\n"
"v_mov_b32 v130, v210\n"
"v_mov_b32 v131, v211\n"
"v_mov_b32 v132, v212\n"
"v_mov_b32 v133, v213\n"
"v_mov_b32 v134, v214\n"
"v_mov_b32 v135, v215\n"
"v_mfma_f32_16x16x32_bf16 a[112:115], v[128:131], a[120:123], a[112:115]\n"
"s_nop 0\n"
"v_mfma_f32_16x16x32_bf16 a[116:119], v[132:135], a[120:123], a[116:119]\n"
"s_call_b64 s[18:19], K1QK_%=\n"
"s_call_b64 s[18:19], K1EX_%=\n"
"v_mov_b32 v136, v208\n"
"v_mov_b32 v137, v209\n"
"v_mov_b32 v138, v210\n"
"v_mov_b32 v139, v211\n"
"v_mov_b32 v140, v212\n"
"v_mov_b32 v141, v213\n"
"v_mov_b32 v142, v214\n"
"v_mov_b32 v143, v215\n"
"v_mfma_f32_16x16x32_bf16 a[112:115], v[136:139], a[120:123], a[112:115]\n"
"s_nop 0\n"
"v_mfma_f32_16x16x32_bf16 a[116:119], v[140:143], a[120:123], a[116:119]\n"
"s_call_b64 s[18:19], K1QK_%=\n"
"s_call_b64 s[18:19], K1EX_%=\n"
"v_mov_b32 v144, v208\n"
"v_mov_b32 v145, v209\n"
"v_mov_b32 v146, v210\n"
"v_mov_b32 v147, v211\n"
"v_mov_b32 v148, v212\n"
"v_mov_b32 v149, v213\n"
"v_mov_b32 v150, v214\n"
"v_mov_b32 v151, v215\n"
"v_mfma_f32_16x16x32_bf16 a[112:115], v[144:147], a[120:123], a[112:115]\n"
"s_nop 0\n"
"v_mfma_f32_16x16x32_bf16 a[116:119], v[148:151], a[120:123], a[116:119]\n"
"s_call_b64 s[18:19], K1QK_%=\n"
"s_call_b64 s[18:19], K1EX_%=\n"
"v_mov_b32 v152, v208\n"
"v_mov_b32 v153, v209\n"
"v_mov_b32 v154, v210\n"
"v_mov_b32 v155, v211\n"
"v_mov_b32 v156, v212\n"
"v_mov_b32 v157, v213\n"
"v_mov_b32 v158, v214\n"
"v_mov_b32 v159, v215\n"
"v_mfma_f32_16x16x32_bf16 a[112:115], v[152:155], a[120:123], a[112:115]\n"
"s_nop 0\n"
"v_mfma_f32_16x16x32_bf16 a[116:119], v[156:159], a[120:123], a[116:119]\n"
"s_call_b64 s[18:19], K1QK_%=\n"
"s_call_b64 s[18:19], K1EX_%=\n"
"v_mov_b32 v160, v208\n"
"v_mov_b32 v161, v209\n"
"v_mov_b32 v162, v210\n"
"v_mov_b32 v163, v211\n"
"v_mov_b32 v164, v212\n"
"v_mov_b32 v165, v213\n"
"v_mov_b32 v166, v214\n"
"v_mov_b32 v167, v215\n"
"v_mfma_f32_16x16x32_bf16 a[112:115], v[160:163], a[120:123], a[112:115]\n"
"s_nop 0\n"
"v_mfma_f32_16x16x32_bf16 a[116:119], v[164:167], a[120:123], a[116:119]\n"
"s_call_b64 s[18:19], K1QK_%=\n"
"s_call_b64 s[18:19], K1EX_%=\n"
"v_mov_b32 v168, v208\n"
"v_mov_b32 v169, v209\n"
"v_mov_b32 v170, v210\n"
"v_mov_b32 v171, v211\n"
"v_mov_b32 v172, v212\n"
"v_mov_b32 v173, v213\n"
"v_mov_b32 v174, v214\n"
"v_mov_b32 v175, v215\n"
"v_mfma_f32_16x16x32_bf16 a[112:115], v[168:171], a[120:123], a[112:115]\n"
"s_nop 0\n"
"v_mfma_f32_16x16x32_bf16 a[116:119], v[172:175], a[120:123], a[116:119]\n"
"s_call_b64 s[18:19], K1QK_%=\n"
"s_call_b64 s[18:19], K1EX_%=\n"
"v_mov_b32 v176, v208\n"
"v_mov_b32 v177, v209\n"
"v_mov_b32 v178, v210\n"
"v_mov_b32 v179, v211\n"
"v_mov_b32 v180, v212\n"
"v_mov_b32 v181, v213\n"
"v_mov_b32 v182, v214\n"
"v_mov_b32 v183, v215\n"
"v_mfma_f32_16x16x32_bf16 a[112:115], v[176:179], a[120:123], a[112:115]\n"
"s_nop 0\n"
"v_mfma_f32_16x16x32_bf16 a[116:119], v[180:183], a[120:123], a[116:119]\n"
"s_call_b64 s[18:19], K1QK_%=\n"
"s_call_b64 s[18:19], K1EX_%=\n"
"v_mov_b32 v184, v208\n"
"v_mov_b32 v185, v209\n"
"v_mov_b32 v186, v210\n"
"v_mov_b32 v187, v211\n"
"v_mov_b32 v188, v212\n"
"v_mov_b32 v189, v213\n"
"v_mov_b32 v190, v214\n"
"v_mov_b32 v191, v215\n"
"v_mfma_f32_16x16x32_bf16 a[112:115], v[184:187], a[120:123], a[112:115]\n"
"s_nop 0\n"
"v_mfma_f32_16x16x32_bf16 a[116:119], v[188:191], a[120:123], a[116:119]\n"
"s_add_u32 m0, s65, 81920\n"
"s_add_u32 s67, s65, 0\n"
"buffer_load_dwordx4 v224, s[44:47], s67 offen offset:0 sc1 lds\n"
"buffer_load_dwordx4 v224, s[44:47], s67 offen offset:1024 sc1 lds\n"
"buffer_load_dwordx4 v224, s[44:47], s67 offen offset:2048 sc1 lds\n"
"buffer_load_dwordx4 v224, s[44:47], s67 offen offset:3072 sc1 lds\n"
"s_waitcnt vmcnt(0) lgkmcnt(0)\n"
"s_barrier\n"
"ds_read_b128 a[192:195], v239 offset:16384\n"
"ds_read_b128 a[196:199], v239 offset:17408\n"
"ds_read_b128 a[200:203], v239 offset:18432\n"
"ds_read_b128 a[204:207], v239 offset:19456\n"
"ds_read_b128 a[208:211], v239 offset:20480\n"
"ds_read_b128 a[212:215], v239 offset:21504\n"
"ds_read_b128 a[216:219], v239 offset:22528\n"
"ds_read_b128 a[220:223], v239 offset:23552\n"
"s_waitcnt lgkmcnt(0)\n"
"ds_read_b128 a[224:227], v239 offset:24576\n"
"ds_read_b128 a[228:231], v239 offset:25600\n"
"ds_read_b128 a[232:235], v239 offset:26624\n"
"ds_read_b128 a[236:239], v239 offset:27648\n"
"ds_read_b128 a[240:243], v239 offset:28672\n"
"ds_read_b128 a[244:247], v239 offset:29696\n"
"ds_read_b128 a[248:251], v239 offset:30720\n"
"ds_read_b128 a[252:255], v239 offset:31744\n"
"s_waitcnt lgkmcnt(0)\n"
"v_mfma_f32_16x16x32_bf16 a[128:131], v[8:11], a[192:195], 0\n"
"v_mfma_f32_16x16x32_bf16 a[144:147], v[12:15], a[192:195], 0\n"
"v_mfma_f32_16x16x32_bf16 a[132:135], v[8:11], a[196:199], 0\n"
"v_mfma_f32_16x16x32_bf16 a[148:151], v[12:15], a[196:199], 0\n"
"v_mfma_f32_16x16x32_bf16 a[136:139], v[8:11], a[200:203], 0\n"
"v_mfma_f32_16x16x32_bf16 a[152:155], v[12:15], a[200:203], 0\n"
"v_mfma_f32_16x16x32_bf16 a[140:143], v[8:11], a[204:207], 0\n"
"v_mfma_f32_16x16x32_bf16 a[156:159], v[12:15], a[204:207], 0\n"
"v_mfma_f32_16x16x32_bf16 a[128:131], v[24:27], a[208:211], a[128:131]\n"
"v_mfma_f32_16x16x32_bf16 a[144:147], v[28:31], a[208:211], a[144:147]\n"
"v_mfma_f32_16x16x32_bf16 a[132:135], v[24:27], a[212:215], a[132:135]\n"
"v_mfma_f32_16x16x32_bf16 a[148:151], v[28:31], a[212:215], a[148:151]\n"
"v_mfma_f32_16x16x32_bf16 a[136:139], v[24:27], a[216:219], a[136:139]\n"
"v_mfma_f32_16x16x32_bf16 a[152:155], v[28:31], a[216:219], a[152:155]\n"
"v_mfma_f32_16x16x32_bf16 a[140:143], v[24:27], a[220:223], a[140:143]\n"
"v_mfma_f32_16x16x32_bf16 a[156:159], v[28:31], a[220:223], a[156:159]\n"
"v_mfma_f32_16x16x32_bf16 a[128:131], v[40:43], a[224:227], a[128:131]\n"
"v_mfma_f32_16x16x32_bf16 a[144:147], v[44:47], a[224:227], a[144:147]\n"
"v_mfma_f32_16x16x32_bf16 a[132:135], v[40:43], a[228:231], a[132:135]\n"
"v_mfma_f32_16x16x32_bf16 a[148:151], v[44:47], a[228:231], a[148:151]\n"
"v_mfma_f32_16x16x32_bf16 a[136:139], v[40:43], a[232:235], a[136:139]\n"
"v_mfma_f32_16x16x32_bf16 a[152:155], v[44:47], a[232:235], a[152:155]\n"
"v_mfma_f32_16x16x32_bf16 a[140:143], v[40:43], a[236:239], a[140:143]\n"
"v_mfma_f32_16x16x32_bf16 a[156:159], v[44:47], a[236:239], a[156:159]\n"
"v_mfma_f32_16x16x32_bf16 a[128:131], v[56:59], a[240:243], a[128:131]\n"
"v_mfma_f32_16x16x32_bf16 a[144:147], v[60:63], a[240:243], a[144:147]\n"
"v_mfma_f32_16x16x32_bf16 a[132:135], v[56:59], a[244:247], a[132:135]\n"
"v_mfma_f32_16x16x32_bf16 a[148:151], v[60:63], a[244:247], a[148:151]\n"
"v_mfma_f32_16x16x32_bf16 a[136:139], v[56:59], a[248:251], a[136:139]\n"
"v_mfma_f32_16x16x32_bf16 a[152:155], v[60:63], a[248:251], a[152:155]\n"
"v_mfma_f32_16x16x32_bf16 a[140:143], v[56:59], a[252:255], a[140:143]\n"
"v_mfma_f32_16x16x32_bf16 a[156:159], v[60:63], a[252:255], a[156:159]\n"
"s_waitcnt vmcnt(0) lgkmcnt(0)\n"
"s_barrier\n"
"s_add_u32 m0, s65, 81920\n"
"s_add_u32 s67, s65, 16384\n"
"buffer_load_dwordx4 v224, s[44:47], s67 offen offset:0 sc1 lds\n"
"buffer_load_dwordx4 v224, s[44:47], s67 offen offset:1024 sc1 lds\n"
"buffer_load_dwordx4 v224, s[44:47], s67 offen offset:2048 sc1 lds\n"
"buffer_load_dwordx4 v224, s[44:47], s67 offen offset:3072 sc1 lds\n"
"s_waitcnt vmcnt(0) lgkmcnt(0)\n"
"s_barrier\n"
"ds_read_b128 a[192:195], v239 offset:16384\n"
"ds_read_b128 a[196:199], v239 offset:17408\n"
"ds_read_b128 a[200:203], v239 offset:18432\n"
"ds_read_b128 a[204:207], v239 offset:19456\n"
"ds_read_b128 a[208:211], v239 offset:20480\n"
"ds_read_b128 a[212:215], v239 offset:21504\n"
"ds_read_b128 a[216:219], v239 offset:22528\n"
"ds_read_b128 a[220:223], v239 offset:23552\n"
"s_waitcnt lgkmcnt(0)\n"
"ds_read_b128 a[224:227], v239 offset:24576\n"
"ds_read_b128 a[228:231], v239 offset:25600\n"
"ds_read_b128 a[232:235], v239 offset:26624\n"
"ds_read_b128 a[236:239], v239 offset:27648\n"
"ds_read_b128 a[240:243], v239 offset:28672\n"
"ds_read_b128 a[244:247], v239 offset:29696\n"
"ds_read_b128 a[248:251], v239 offset:30720\n"
"ds_read_b128 a[252:255], v239 offset:31744\n"
"s_waitcnt lgkmcnt(0)\n"
"v_mfma_f32_16x16x32_bf16 a[128:131], v[72:75], a[192:195], a[128:131]\n"
"v_mfma_f32_16x16x32_bf16 a[144:147], v[76:79], a[192:195], a[144:147]\n"
"v_mfma_f32_16x16x32_bf16 a[132:135], v[72:75], a[196:199], a[132:135]\n"
"v_mfma_f32_16x16x32_bf16 a[148:151], v[76:79], a[196:199], a[148:151]\n"
"v_mfma_f32_16x16x32_bf16 a[136:139], v[72:75], a[200:203], a[136:139]\n"
"v_mfma_f32_16x16x32_bf16 a[152:155], v[76:79], a[200:203], a[152:155]\n"
"v_mfma_f32_16x16x32_bf16 a[140:143], v[72:75], a[204:207], a[140:143]\n"
"v_mfma_f32_16x16x32_bf16 a[156:159], v[76:79], a[204:207], a[156:159]\n"
"v_mfma_f32_16x16x32_bf16 a[128:131], v[88:91], a[208:211], a[128:131]\n"
"v_mfma_f32_16x16x32_bf16 a[144:147], v[92:95], a[208:211], a[144:147]\n"
"v_mfma_f32_16x16x32_bf16 a[132:135], v[88:91], a[212:215], a[132:135]\n"
"v_mfma_f32_16x16x32_bf16 a[148:151], v[92:95], a[212:215], a[148:151]\n"
"v_mfma_f32_16x16x32_bf16 a[136:139], v[88:91], a[216:219], a[136:139]\n"
"v_mfma_f32_16x16x32_bf16 a[152:155], v[92:95], a[216:219], a[152:155]\n"
"v_mfma_f32_16x16x32_bf16 a[140:143], v[88:91], a[220:223], a[140:143]\n"
"v_mfma_f32_16x16x32_bf16 a[156:159], v[92:95], a[220:223], a[156:159]\n"
"v_mfma_f32_16x16x32_bf16 a[128:131], v[104:107], a[224:227], a[128:131]\n"
"v_mfma_f32_16x16x32_bf16 a[144:147], v[108:111], a[224:227], a[144:147]\n"
"v_mfma_f32_16x16x32_bf16 a[132:135], v[104:107], a[228:231], a[132:135]\n"
"v_mfma_f32_16x16x32_bf16 a[148:151], v[108:111], a[228:231], a[148:151]\n"
"v_mfma_f32_16x16x32_bf16 a[136:139], v[104:107], a[232:235], a[136:139]\n"
"v_mfma_f32_16x16x32_bf16 a[152:155], v[108:111], a[232:235], a[152:155]\n"
"v_mfma_f32_16x16x32_bf16 a[140:143], v[104:107], a[236:239], a[140:143]\n"
"v_mfma_f32_16x16x32_bf16 a[156:159], v[108:111], a[236:239], a[156:159]\n"
"v_mfma_f32_16x16x32_bf16 a[128:131], v[120:123], a[240:243], a[128:131]\n"
"v_mfma_f32_16x16x32_bf16 a[144:147], v[124:127], a[240:243], a[144:147]\n"
"v_mfma_f32_16x16x32_bf16 a[132:135], v[120:123], a[244:247], a[132:135]\n"
"v_mfma_f32_16x16x32_bf16 a[148:151], v[124:127], a[244:247], a[148:151]\n"
"v_mfma_f32_16x16x32_bf16 a[136:139], v[120:123], a[248:251], a[136:139]\n"
"v_mfma_f32_16x16x32_bf16 a[152:155], v[124:127], a[248:251], a[152:155]\n"
"v_mfma_f32_16x16x32_bf16 a[140:143], v[120:123], a[252:255], a[140:143]\n"
"v_mfma_f32_16x16x32_bf16 a[156:159], v[124:127], a[252:255], a[156:159]\n"
"s_waitcnt vmcnt(0) lgkmcnt(0)\n"
"s_barrier\n"
"s_add_u32 m0, s65, 81920\n"
"s_add_u32 s67, s65, 32768\n"
"buffer_load_dwordx4 v224, s[44:47], s67 offen offset:0 sc1 lds\n"
"buffer_load_dwordx4 v224, s[44:47], s67 offen offset:1024 sc1 lds\n"
"buffer_load_dwordx4 v224, s[44:47], s67 offen offset:2048 sc1 lds\n"
"buffer_load_dwordx4 v224, s[44:47], s67 offen offset:3072 sc1 lds\n"
"s_waitcnt vmcnt(0) lgkmcnt(0)\n"
"s_barrier\n"
"ds_read_b128 a[192:195], v239 offset:16384\n"
"ds_read_b128 a[196:199], v239 offset:17408\n"
"ds_read_b128 a[200:203], v239 offset:18432\n"
"ds_read_b128 a[204:207], v239 offset:19456\n"
"ds_read_b128 a[208:211], v239 offset:20480\n"
"ds_read_b128 a[212:215], v239 offset:21504\n"
"ds_read_b128 a[216:219], v239 offset:22528\n"
"ds_read_b128 a[220:223], v239 offset:23552\n"
"s_waitcnt lgkmcnt(0)\n"
"ds_read_b128 a[224:227], v239 offset:24576\n"
"ds_read_b128 a[228:231], v239 offset:25600\n"
"ds_read_b128 a[232:235], v239 offset:26624\n"
"ds_read_b128 a[236:239], v239 offset:27648\n"
"ds_read_b128 a[240:243], v239 offset:28672\n"
"ds_read_b128 a[244:247], v239 offset:29696\n"
"ds_read_b128 a[248:251], v239 offset:30720\n"
"ds_read_b128 a[252:255], v239 offset:31744\n"
"s_waitcnt lgkmcnt(0)\n"
"v_mfma_f32_16x16x32_bf16 a[128:131], v[128:131], a[192:195], a[128:131]\n"
"v_mfma_f32_16x16x32_bf16 a[144:147], v[132:135], a[192:195], a[144:147]\n"
"v_mfma_f32_16x16x32_bf16 a[132:135], v[128:131], a[196:199], a[132:135]\n"
"v_mfma_f32_16x16x32_bf16 a[148:151], v[132:135], a[196:199], a[148:151]\n"
"v_mfma_f32_16x16x32_bf16 a[136:139], v[128:131], a[200:203], a[136:139]\n"
"v_mfma_f32_16x16x32_bf16 a[152:155], v[132:135], a[200:203], a[152:155]\n"
"v_mfma_f32_16x16x32_bf16 a[140:143], v[128:131], a[204:207], a[140:143]\n"
"v_mfma_f32_16x16x32_bf16 a[156:159], v[132:135], a[204:207], a[156:159]\n"
"v_mfma_f32_16x16x32_bf16 a[128:131], v[136:139], a[208:211], a[128:131]\n"
"v_mfma_f32_16x16x32_bf16 a[144:147], v[140:143], a[208:211], a[144:147]\n"
"v_mfma_f32_16x16x32_bf16 a[132:135], v[136:139], a[212:215], a[132:135]\n"
"v_mfma_f32_16x16x32_bf16 a[148:151], v[140:143], a[212:215], a[148:151]\n"
"v_mfma_f32_16x16x32_bf16 a[136:139], v[136:139], a[216:219], a[136:139]\n"
"v_mfma_f32_16x16x32_bf16 a[152:155], v[140:143], a[216:219], a[152:155]\n"
"v_mfma_f32_16x16x32_bf16 a[140:143], v[136:139], a[220:223], a[140:143]\n"
"v_mfma_f32_16x16x32_bf16 a[156:159], v[140:143], a[220:223], a[156:159]\n"
"v_mfma_f32_16x16x32_bf16 a[128:131], v[144:147], a[224:227], a[128:131]\n"
"v_mfma_f32_16x16x32_bf16 a[144:147], v[148:151], a[224:227], a[144:147]\n"
"v_mfma_f32_16x16x32_bf16 a[132:135], v[144:147], a[228:231], a[132:135]\n"
"v_mfma_f32_16x16x32_bf16 a[148:151], v[148:151], a[228:231], a[148:151]\n"
"v_mfma_f32_16x16x32_bf16 a[136:139], v[144:147], a[232:235], a[136:139]\n"
"v_mfma_f32_16x16x32_bf16 a[152:155], v[148:151], a[232:235], a[152:155]\n"
"v_mfma_f32_16x16x32_bf16 a[140:143], v[144:147], a[236:239], a[140:143]\n"
"v_mfma_f32_16x16x32_bf16 a[156:159], v[148:151], a[236:239], a[156:159]\n"
"v_mfma_f32_16x16x32_bf16 a[128:131], v[152:155], a[240:243], a[128:131]\n"
"v_mfma_f32_16x16x32_bf16 a[144:147], v[156:159], a[240:243], a[144:147]\n"
"v_mfma_f32_16x16x32_bf16 a[132:135], v[152:155], a[244:247], a[132:135]\n"
"v_mfma_f32_16x16x32_bf16 a[148:151], v[156:159], a[244:247], a[148:151]\n"
"v_mfma_f32_16x16x32_bf16 a[136:139], v[152:155], a[248:251], a[136:139]\n"
"v_mfma_f32_16x16x32_bf16 a[152:155], v[156:159], a[248:251], a[152:155]\n"
"v_mfma_f32_16x16x32_bf16 a[140:143], v[152:155], a[252:255], a[140:143]\n"
"v_mfma_f32_16x16x32_bf16 a[156:159], v[156:159], a[252:255], a[156:159]\n"
"s_waitcnt vmcnt(0) lgkmcnt(0)\n"
"s_barrier\n"
"s_add_u32 m0, s65, 81920\n"
"s_add_u32 s67, s65, 49152\n"
"buffer_load_dwordx4 v224, s[44:47], s67 offen offset:0 sc1 lds\n"
"buffer_load_dwordx4 v224, s[44:47], s67 offen offset:1024 sc1 lds\n"
"buffer_load_dwordx4 v224, s[44:47], s67 offen offset:2048 sc1 lds\n"
"buffer_load_dwordx4 v224, s[44:47], s67 offen offset:3072 sc1 lds\n"
"s_waitcnt vmcnt(0) lgkmcnt(0)\n"
"s_barrier\n"
"ds_read_b128 a[192:195], v239 offset:16384\n"
"ds_read_b128 a[196:199], v239 offset:17408\n"
"ds_read_b128 a[200:203], v239 offset:18432\n"
"ds_read_b128 a[204:207], v239 offset:19456\n"
"ds_read_b128 a[208:211], v239 offset:20480\n"
"ds_read_b128 a[212:215], v239 offset:21504\n"
"ds_read_b128 a[216:219], v239 offset:22528\n"
"ds_read_b128 a[220:223], v239 offset:23552\n"
"s_waitcnt lgkmcnt(0)\n"
"ds_read_b128 a[224:227], v239 offset:24576\n"
"ds_read_b128 a[228:231], v239 offset:25600\n"
"ds_read_b128 a[232:235], v239 offset:26624\n"
"ds_read_b128 a[236:239], v239 offset:27648\n"
"ds_read_b128 a[240:243], v239 offset:28672\n"
"ds_read_b128 a[244:247], v239 offset:29696\n"
"ds_read_b128 a[248:251], v239 offset:30720\n"
"ds_read_b128 a[252:255], v239 offset:31744\n"
"s_waitcnt lgkmcnt(0)\n"
"v_mfma_f32_16x16x32_bf16 a[128:131], v[160:163], a[192:195], a[128:131]\n"
"v_mfma_f32_16x16x32_bf16 a[144:147], v[164:167], a[192:195], a[144:147]\n"
"v_mfma_f32_16x16x32_bf16 a[132:135], v[160:163], a[196:199], a[132:135]\n"
"v_mfma_f32_16x16x32_bf16 a[148:151], v[164:167], a[196:199], a[148:151]\n"
"v_mfma_f32_16x16x32_bf16 a[136:139], v[160:163], a[200:203], a[136:139]\n"
"v_mfma_f32_16x16x32_bf16 a[152:155], v[164:167], a[200:203], a[152:155]\n"
"v_mfma_f32_16x16x32_bf16 a[140:143], v[160:163], a[204:207], a[140:143]\n"
"v_mfma_f32_16x16x32_bf16 a[156:159], v[164:167], a[204:207], a[156:159]\n"
"v_mfma_f32_16x16x32_bf16 a[128:131], v[168:171], a[208:211], a[128:131]\n"
"v_mfma_f32_16x16x32_bf16 a[144:147], v[172:175], a[208:211], a[144:147]\n"
"v_mfma_f32_16x16x32_bf16 a[132:135], v[168:171], a[212:215], a[132:135]\n"
"v_mfma_f32_16x16x32_bf16 a[148:151], v[172:175], a[212:215], a[148:151]\n"
"v_mfma_f32_16x16x32_bf16 a[136:139], v[168:171], a[216:219], a[136:139]\n"
"v_mfma_f32_16x16x32_bf16 a[152:155], v[172:175], a[216:219], a[152:155]\n"
"v_mfma_f32_16x16x32_bf16 a[140:143], v[168:171], a[220:223], a[140:143]\n"
"v_mfma_f32_16x16x32_bf16 a[156:159], v[172:175], a[220:223], a[156:159]\n"
"v_mfma_f32_16x16x32_bf16 a[128:131], v[176:179], a[224:227], a[128:131]\n"
"v_mfma_f32_16x16x32_bf16 a[144:147], v[180:183], a[224:227], a[144:147]\n"
"v_mfma_f32_16x16x32_bf16 a[132:135], v[176:179], a[228:231], a[132:135]\n"
"v_mfma_f32_16x16x32_bf16 a[148:151], v[180:183], a[228:231], a[148:151]\n"
"v_mfma_f32_16x16x32_bf16 a[136:139], v[176:179], a[232:235], a[136:139]\n"
"v_mfma_f32_16x16x32_bf16 a[152:155], v[180:183], a[232:235], a[152:155]\n"
"v_mfma_f32_16x16x32_bf16 a[140:143], v[176:179], a[236:239], a[140:143]\n"
"v_mfma_f32_16x16x32_bf16 a[156:159], v[180:183], a[236:239], a[156:159]\n"
"v_mfma_f32_16x16x32_bf16 a[128:131], v[184:187], a[240:243], a[128:131]\n"
"v_mfma_f32_16x16x32_bf16 a[144:147], v[188:191], a[240:243], a[144:147]\n"
"v_mfma_f32_16x16x32_bf16 a[132:135], v[184:187], a[244:247], a[132:135]\n"
"v_mfma_f32_16x16x32_bf16 a[148:151], v[188:191], a[244:247], a[148:151]\n"
"v_mfma_f32_16x16x32_bf16 a[136:139], v[184:187], a[248:251], a[136:139]\n"
"v_mfma_f32_16x16x32_bf16 a[152:155], v[188:191], a[248:251], a[152:155]\n"
"v_mfma_f32_16x16x32_bf16 a[140:143], v[184:187], a[252:255], a[140:143]\n"
"v_mfma_f32_16x16x32_bf16 a[156:159], v[188:191], a[252:255], a[156:159]\n"
"s_waitcnt vmcnt(0) lgkmcnt(0)\n"
"s_barrier\n"
"s_mov_b32 m0, s70\n"
"s_mov_b32 s67, s71\n"
"s_nop 7\n"
"s_branch K1BACK_%=\n"
"K1QK_%=:\n"
"s_nop 7\n"
"s_add_u32 m0, s65, 81920\n"
"s_add_u32 s67, s64, s69\n"
"buffer_load_dwordx4 v238, s[40:43], s67 offen offset:0 lds\n"
"buffer_load_dwordx4 v238, s[40:43], s67 offen offset:1024 lds\n"
"buffer_load_dwordx4 v238, s[40:43], s67 offen offset:2048 lds\n"
"buffer_load_dwordx4 v238, s[40:43], s67 offen offset:3072 lds\n"
"s_waitcnt vmcnt(0) lgkmcnt(0)\n"
"s_barrier\n"
"ds_read_b128 a[192:195], v226 offset:16384\n"
"ds_read_b128 a[196:199], v226 offset:18432\n"
"ds_read_b128 a[200:203], v226 offset:16896\n"
"ds_read_b128 a[204:207], v226 offset:18944\n"
"ds_read_b128 a[208:211], v226 offset:20480\n"
"ds_read_b128 a[212:215], v226 offset:22528\n"
"ds_read_b128 a[216:219], v226 offset:20992\n"
"ds_read_b128 a[220:223], v226 offset:23040\n"
"s_waitcnt lgkmcnt(0)\n"
"ds_read_b128 a[224:227], v226 offset:24576\n"
"ds_read_b128 a[228:231], v226 offset:26624\n"
"ds_read_b128 a[232:235], v226 offset:25088\n"
"ds_read_b128 a[236:239], v226 offset:27136\n"
"ds_read_b128 a[240:243], v226 offset:28672\n"
"ds_read_b128 a[244:247], v226 offset:30720\n"
"ds_read_b128 a[248:251], v226 offset:29184\n"
"ds_read_b128 a[252:255], v226 offset:31232\n"
"s_waitcnt lgkmcnt(0)\n"
"v_mfma_f32_16x16x32_bf16 v[192:195], a[192:195], v[0:3], 0\n"
"v_mfma_f32_16x16x32_bf16 v[196:199], a[192:195], v[4:7], 0\n"
"v_mfma_f32_16x16x32_bf16 v[200:203], a[196:199], v[0:3], 0\n"
"v_mfma_f32_16x16x32_bf16 v[204:207], a[196:199], v[4:7], 0\n"
"v_mfma_f32_16x16x32_bf16 v[192:195], a[200:203], v[16:19], v[192:195]\n"
"v_mfma_f32_16x16x32_bf16 v[196:199], a[200:203], v[20:23], v[196:199]\n"
"v_mfma_f32_16x16x32_bf16 v[200:203], a[204:207], v[16:19], v[200:203]\n"
"v_mfma_f32_16x16x32_bf16 v[204:207], a[204:207], v[20:23], v[204:207]\n"
"v_mfma_f32_16x16x32_bf16 v[192:195], a[208:211], v[32:35], v[192:195]\n"
"v_mfma_f32_16x16x32_bf16 v[196:199], a[208:211], v[36:39], v[196:199]\n"
"v_mfma_f32_16x16x32_bf16 v[200:203], a[212:215], v[32:35], v[200:203]\n"
"v_mfma_f32_16x16x32_bf16 v[204:207], a[212:215], v[36:39], v[204:207]\n"
"v_mfma_f32_16x16x32_bf16 v[192:195], a[216:219], v[48:51], v[192:195]\n"
"v_mfma_f32_16x16x32_bf16 v[196:199], a[216:219], v[52:55], v[196:199]\n"
"v_mfma_f32_16x16x32_bf16 v[200:203], a[220:223], v[48:51], v[200:203]\n"
"v_mfma_f32_16x16x32_bf16 v[204:207], a[220:223], v[52:55], v[204:207]\n"
"v_mfma_f32_16x16x32_bf16 v[192:195], a[224:227], v[64:67], v[192:195]\n"
"v_mfma_f32_16x16x32_bf16 v[196:199], a[224:227], v[68:71], v[196:199]\n"
"v_mfma_f32_16x16x32_bf16 v[200:203], a[228:231], v[64:67], v[200:203]\n"
"v_mfma_f32_16x16x32_bf16 v[204:207], a[228:231], v[68:71], v[204:207]\n"
"v_mfma_f32_16x16x32_bf16 v[192:195], a[232:235], v[80:83], v[192:195]\n"
"v_mfma_f32_16x16x32_bf16 v[196:199], a[232:235], v[84:87], v[196:199]\n"
"v_mfma_f32_16x16x32_bf16 v[200:203], a[236:239], v[80:83], v[200:203]\n"
"v_mfma_f32_16x16x32_bf16 v[204:207], a[236:239], v[84:87], v[204:207]\n"
"v_mfma_f32_16x16x32_bf16 v[192:195], a[240:243], v[96:99], v[192:195]\n"
"v_mfma_f32_16x16x32_bf16 v[196:199], a[240:243], v[100:103], v[196:199]\n"
"v_mfma_f32_16x16x32_bf16 v[200:203], a[244:247], v[96:99], v[200:203]\n"
"v_mfma_f32_16x16x32_bf16 v[204:207], a[244:247], v[100:103], v[204:207]\n"
"v_mfma_f32_16x16x32_bf16 v[192:195], a[248:251], v[112:115], v[192:195]\n"
"v_mfma_f32_16x16x32_bf16 v[196:199], a[248:251], v[116:119], v[196:199]\n"
"v_mfma_f32_16x16x32_bf16 v[200:203], a[252:255], v[112:115], v[200:203]\n"
"v_mfma_f32_16x16x32_bf16 v[204:207], a[252:255], v[116:119], v[204:207]\n"
"s_waitcnt vmcnt(0) lgkmcnt(0)\n"
"s_barrier\n"
"s_add_u32 s69, s69, 16384\n"
"s_nop 7\n"
"s_setpc_b64 s[18:19]\n"
"K1EX_%=:\n"
"v_sub_f32 v192, v192, v244\n"
"v_sub_f32 v193, v193, v244\n"
"v_exp_f32 v192, v192\n"
"v_sub_f32 v194, v194, v244\n"
"v_exp_f32 v193, v193\n"
"s_nop 0\n"
"v_cvt_pk_bf16_f32 v208, v192, v193\n"
"v_sub_f32 v195, v195, v244\n"
"v_exp_f32 v194, v194\n"
"v_sub_f32 v200, v200, v244\n"
"v_exp_f32 v195, v195\n"
"s_nop 0\n"
"v_cvt_pk_bf16_f32 v209, v194, v195\n"
"v_sub_f32 v201, v201, v244\n"
"v_exp_f32 v200, v200\n"
"v_sub_f32 v202, v202, v244\n"
"v_exp_f32 v201, v201\n"
"s_nop 0\n"
"v_cvt_pk_bf16_f32 v210, v200, v201\n"
"v_sub_f32 v203, v203, v244\n"
"v_exp_f32 v202, v202\n"
"v_exp_f32 v203, v203\n"
"s_nop 0\n"
"v_cvt_pk_bf16_f32 v211, v202, v203\n"
"v_sub_f32 v196, v196, v246\n"
"v_sub_f32 v197, v197, v246\n"
"v_exp_f32 v196, v196\n"
"v_sub_f32 v198, v198, v246\n"
"v_exp_f32 v197, v197\n"
"s_nop 0\n"
"v_cvt_pk_bf16_f32 v212, v196, v197\n"
"v_sub_f32 v199, v199, v246\n"
"v_exp_f32 v198, v198\n"
"v_sub_f32 v204, v204, v246\n"
"v_exp_f32 v199, v199\n"
"s_nop 0\n"
"v_cvt_pk_bf16_f32 v213, v198, v199\n"
"v_sub_f32 v205, v205, v246\n"
"v_exp_f32 v204, v204\n"
"v_sub_f32 v206, v206, v246\n"
"v_exp_f32 v205, v205\n"
"s_nop 0\n"
"v_cvt_pk_bf16_f32 v214, v204, v205\n"
"v_sub_f32 v207, v207, v246\n"
"v_exp_f32 v206, v206\n"
"v_exp_f32 v207, v207\n"
"s_nop 0\n"
"v_cvt_pk_bf16_f32 v215, v206, v207\n"
"s_nop 1\n"
"s_setpc_b64 s[18:19]\n"
"K1END_%=:\n"
  : "+{s[0:1]}"(ka) : "{s30}"(b), "{s31}"(w)
  : "v0","v1","v2","v3","v4","v5","v6","v7","v8","v9","v10","v11","v12","v13","v14","v15","v16","v17","v18","v19","v20","v21","v22","v23","v24","v25","v26","v27","v28","v29","v30","v31","v32","v33","v34","v35","v36","v37","v38","v39","v40","v41","v42","v43","v44","v45","v46","v47","v48","v49","v50","v51","v52","v53","v54","v55","v56","v57","v58","v59","v60","v61","v62","v63","v64","v65","v66","v67","v68","v69","v70","v71","v72","v73","v74","v75","v76","v77","v78","v79","v80","v81","v82","v83","v84","v85","v86","v87","v88","v89","v90","v91","v92","v93","v94","v95","v96","v97","v98","v99","v100","v101","v102","v103","v104","v105","v106","v107","v108","v109","v110","v111","v112","v113","v114","v115","v116","v117","v118","v119","v120","v121","v122","v123","v124","v125","v126","v127","v128","v129","v130","v131","v132","v133","v134","v135","v136","v137","v138","v139","v140","v141","v142","v143","v144","v145","v146","v147","v148","v149","v150","v151","v152","v153","v154","v155","v156","v157","v158","v159","v160","v161","v162","v163","v164","v165","v166","v167","v168","v169","v170","v171","v172","v173","v174","v175","v176","v177","v178","v179","v180","v181","v182","v183","v184","v185","v186","v187","v188","v189","v190","v191","v192","v193","v194","v195","v196","v197","v198","v199","v200","v201","v202","v203","v204","v205","v206","v207","v208","v209","v210","v211","v212","v213","v214","v215","v216","v217","v218","v219","v220","v221","v222","v223","v224","v225","v226","v227","v228","v229","v230","v231","v232","v233","v234","v235","v236","v237","v238","v239","v240","v241","v242","v243","v244","v245","v246","v247","v248","v249","v250","v251","v252","v253","v254","v255",
    "a0","a1","a2","a3","a4","a5","a6","a7","a8","a9","a10","a11","a12","a13","a14","a15","a16","a17","a18","a19","a20","a21","a22","a23","a24","a25","a26","a27","a28","a29","a30","a31","a32","a33","a34","a35","a36","a37","a38","a39","a40","a41","a42","a43","a44","a45","a46","a47","a48","a49","a50","a51","a52","a53","a54","a55","a56","a57","a58","a59","a60","a61","a62","a63","a64","a65","a66","a67","a68","a69","a70","a71","a72","a73","a74","a75","a76","a77","a78","a79","a80","a81","a82","a83","a84","a85","a86","a87","a88","a89","a90","a91","a92","a93","a94","a95","a96","a97","a98","a99","a100","a101","a102","a103","a104","a105","a106","a107","a108","a109","a110","a111","a112","a113","a114","a115","a116","a117","a118","a119","a120","a121","a122","a123","a124","a125","a126","a127","a128","a129","a130","a131","a132","a133","a134","a135","a136","a137","a138","a139","a140","a141","a142","a143","a144","a145","a146","a147","a148","a149","a150","a151","a152","a153","a154","a155","a156","a157","a158","a159","a160","a161","a162","a163","a164","a165","a166","a167","a168","a169","a170","a171","a172","a173","a174","a175","a176","a177","a178","a179","a180","a181","a182","a183","a184","a185","a186","a187","a188","a189","a190","a191","a192","a193","a194","a195","a196","a197","a198","a199","a200","a201","a202","a203","a204","a205","a206","a207","a208","a209","a210","a211","a212","a213","a214","a215","a216","a217","a218","a219","a220","a221","a222","a223","a224","a225","a226","a227","a228","a229","a230","a231","a232","a233","a234","a235","a236","a237","a238","a239","a240","a241","a242","a243","a244","a245","a246","a247","a248","a249","a250","a251","a252","a253","a254","a255",
    "s2","s3","s4","s5","s6","s7","s8","s9","s10","s11","s12","s13","s14","s15","s16","s17","s18","s19","s20","s21","s22","s23","s24","s25","s26","s27","s28","s29","s32","s33","s34","s35","s36","s37","s38","s39","s40","s41","s42","s43","s44","s45","s46","s47","s48","s49","s50","s51","s52","s53","s54","s55","s56","s57","s58","s59","s60","s61","s62","s63","s64","s65","s66","s67","s68","s69","s70","s71","s72","s73","s74","s75","s76","s77","s78","s79","s80","s81","s82","s83","s84","s85","s86","s87","s88","s89","s90","s91","s92","s93","s94","s95","s96","s97","s98","s99","memory","scc","vcc");
}

extern "C" void kernel_launch(void* const* d_in, const int* in_sizes, int n_in, void* d_out, int out_size, void* d_ws, size_t ws_size, hipStream_t stream) {
  (void)in_sizes; (void)n_in; (void)out_size; (void)ws_size;
  (void)hipFuncSetAttribute((const void*)fused_block, hipFuncAttributeMaxDynamicSharedMemorySize, 163840);
  fused_block<<<256, 256, 163840, stream>>>((const char*)d_in[0], (const char*)d_in[1], (const char*)d_in[2], (const char*)d_in[3], (const char*)d_in[4], (const char*)d_in[6],
                                            (const char*)d_in[7], (const char*)d_in[8], (const char*)d_in[9], (char*)d_ws, (char*)d_out);
}
```
